```python
import torch

def _softmax(x, axis=-1):
    return torch.softmax(x, dim=axis)
def _gelu(x):
    return torch.nn.functional.gelu(x, approximate="tanh")
def _silu(x):
    return torch.nn.functional.silu(x)
def _sigmoid(x):
    return torch.sigmoid(x)
def _layernorm(x, axis=-1, eps=1e-5):
    m = x.mean(dim=axis, keepdim=True)
    v = x.var(dim=axis, keepdim=True, unbiased=False)
    return (x - m) / torch.sqrt(v + eps)
def _scatter_add(tgt, idx, src, axis=0):
    return tgt.clone().index_add_(axis, idx, src)
def _segment_sum(src, seg, n):
    out = torch.zeros((n,) + src.shape[1:], dtype=src.dtype)
    return out.index_add_(0, seg, src)
def _pad(x, pw):
    flat = tuple(v for pair in reversed(pw) for v in pair)
    return torch.nn.functional.pad(x, flat)

kernel_name = 'concat_heads__gqa_block_cached__mha_block_cached__near_ridge'

def setup_inputs():

    _w = (lambda a: (lambda t: t.to(torch.bfloat16) if t.is_floating_point() else t)(torch.as_tensor(a)))
    def _A_setup():
        B, Sq, D = 16, 32, 640
        Hq, Hkv, Sk, Dh = 32, 32, 32, 192
        assert Hq % Hkv == 0, (Hq, Hkv)
        return {
            "x": _w((lambda *s: torch.randn(*s, dtype=torch.bfloat16) * 0.8)(B, Sq, D)),
            "k_cache": _w((lambda *s: torch.randn(*s, dtype=torch.bfloat16) * 0.8)(B, Hkv, Sk, Dh)),
            "v_cache": _w((lambda *s: torch.randn(*s, dtype=torch.bfloat16) * 0.05)(B, Hkv, Sk, Dh)),
            "wq": _w((lambda *s: torch.randn(*s, dtype=torch.bfloat16) * 0.8)(D, Hq * Dh) * (D ** -0.5)),
            "wo": _w((lambda *s: torch.randn(*s, dtype=torch.bfloat16) * 0.05)(Hq * Dh, D) * ((Hq * Dh) ** -0.5)),
        }
    _A_in = _A_setup()

    def _B_setup():
        B, Sq, D = 16, 32, 640
        H, Sk, Dh = 32, 32, 192
        return {
            "x": _w((lambda *s: torch.randn(*s, dtype=torch.bfloat16) * 0.8)(B, Sq, D)),
            "k_cache": _w((lambda *s: torch.randn(*s, dtype=torch.bfloat16) * 0.8)(B, H, Sk, Dh)),
            "v_cache": _w((lambda *s: torch.randn(*s, dtype=torch.bfloat16) * 0.05)(B, H, Sk, Dh)),
            "wq": _w((lambda *s: torch.randn(*s, dtype=torch.bfloat16) * 0.8)(D, H * Dh) * (D ** -0.5)),
            "wo": _w((lambda *s: torch.randn(*s, dtype=torch.bfloat16) * 0.05)(H * Dh, D) * ((H * Dh) ** -0.5)),
        }
    _B_in = _B_setup()

    return {
        "A_x": _A_in["x"],
        "A_k_cache": _A_in["k_cache"],
        "A_v_cache": _A_in["v_cache"],
        "A_wq": _A_in["wq"],
        "A_wo": _A_in["wo"],
        "B_x": _B_in["x"],
        "B_k_cache": _B_in["k_cache"],
        "B_v_cache": _B_in["v_cache"],
        "B_wq": _B_in["wq"],
        "B_wo": _B_in["wo"],
    }

def reference(A_x, A_k_cache, A_v_cache, A_wq, A_wo, B_x, B_k_cache, B_v_cache, B_wq, B_wo):
    def _A_ref(x, k_cache, v_cache, wq, wo):
        B, Sq, D = x.shape
        Hq, Dh = 32, 192
        Hkv = k_cache.shape[1]
        rep = Hq // Hkv
        q = (lambda x, *d: x.permute(*d))((x @ wq).reshape(B, Sq, Hq, Dh), 0, 2, 1, 3)
        k = (lambda x, n, axis: torch.repeat_interleave(x, n, dim=axis))(k_cache, rep, axis=1)
        v = (lambda x, n, axis: torch.repeat_interleave(x, n, dim=axis))(v_cache, rep, axis=1)
        scores = torch.einsum("bhqd,bhkd->bhqk", q, k) * (Dh ** -0.5)
        p = _softmax(scores, axis=-1)
        o = torch.einsum("bhqk,bhkd->bhqd", p, v)
        return (lambda x, *d: x.permute(*d))(o, 0, 2, 1, 3).reshape(B, Sq, Hq * Dh) @ wo

    def _B_ref(x, k_cache, v_cache, wq, wo):
        B, Sq, D = x.shape
        H, Dh = 32, 192
        q = (lambda x, *d: x.permute(*d))((x @ wq).reshape(B, Sq, H, Dh), 0, 2, 1, 3)
        scores = torch.einsum("bhqd,bhkd->bhqk", q, k_cache) * (Dh ** -0.5)
        p = _softmax(scores, axis=-1)
        o = torch.einsum("bhqk,bhkd->bhqd", p, v_cache)
        return (lambda x, *d: x.permute(*d))(o, 0, 2, 1, 3).reshape(B, Sq, H * Dh) @ wo

    a = _A_ref(A_x, A_k_cache, A_v_cache, A_wq, A_wo)
    b = _B_ref(B_x, B_k_cache, B_v_cache, B_wq, B_wo)
    return torch.concatenate([a, b], axis=-1)
```

```cpp
#include <hip/hip_runtime.h>
#include <hip/hip_bf16.h>

typedef unsigned short u16;
__device__ __forceinline__ float bf2f(u16 v){ return __uint_as_float(((unsigned)v)<<16); }
__device__ __forceinline__ u16 f2bf(float f){ __hip_bfloat16 b = __float2bfloat16(f); return *(u16*)&b; }

__global__ void __launch_bounds__(256) k1(const u16* xA, const u16* kA, const u16* vA, const u16* wqA,
                   const u16* xB, const u16* kB, const u16* vB, const u16* wqB, u16* o){
  int bid = blockIdx.x;
  int br = bid >> 9; int b = (bid >> 5) & 15; int h = bid & 31;
  const u16* x = br ? xB : xA; const u16* k = br ? kB : kA; const u16* v = br ? vB : vA; const u16* wq = br ? wqB : wqA;
  __shared__ float q[32][193];
  __shared__ float s[32][33];
  int t = threadIdx.x;
  for (int i = t; i < 32*192; i += 256) {
    int r = i / 192, c = i % 192;
    float acc = 0;
    for (int d = 0; d < 640; ++d) acc += bf2f(x[(b*32+r)*640+d]) * bf2f(wq[d*6144 + h*192 + c]);
    q[r][c] = bf2f(f2bf(acc));
  }
  __syncthreads();
  const u16* kb = k + (size_t)(b*32+h)*32*192;
  const u16* vb = v + (size_t)(b*32+h)*32*192;
  for (int i = t; i < 32*32; i += 256) {
    int r = i / 32, c = i % 32;
    float acc = 0;
    for (int d = 0; d < 192; ++d) acc += q[r][d] * bf2f(kb[c*192+d]);
    acc = bf2f(f2bf(acc));
    s[r][c] = bf2f(f2bf(acc * 0.07216878364870322f));
  }
  __syncthreads();
  if (t < 32) {
    float m = -1e30f;
    for (int c = 0; c < 32; ++c) m = fmaxf(m, s[t][c]);
    float sum = 0;
    for (int c = 0; c < 32; ++c) { float e = __expf(s[t][c]-m); s[t][c] = e; sum += e; }
    float inv = 1.f/sum;
    for (int c = 0; c < 32; ++c) s[t][c] = bf2f(f2bf(s[t][c]*inv));
  }
  __syncthreads();
  for (int i = t; i < 32*192; i += 256) {
    int r = i / 192, c = i % 192;
    float acc = 0;
    for (int j = 0; j < 32; ++j) acc += s[r][j] * bf2f(vb[j*192+c]);
    o[(size_t)br*512*6144 + (size_t)(b*32+r)*6144 + h*192 + c] = f2bf(acc);
  }
}

__global__ void __launch_bounds__(256) k2(const u16* o, const u16* woA, const u16* woB, u16* out){
  int idx = blockIdx.x*256 + threadIdx.x;
  int row = idx / 1280, col = idx % 1280;
  int br = col >= 640; int c = col - br*640;
  const u16* wo = br ? woB : woA;
  const u16* orow = o + (size_t)br*512*6144 + (size_t)row*6144;
  float acc = 0;
  for (int k = 0; k < 6144; ++k) acc += bf2f(orow[k]) * bf2f(wo[k*640+c]);
  out[idx] = f2bf(acc);
}

extern "C" void kernel_launch(void* const* d_in, const int* in_sizes, int n_in,
                              void* d_out, int out_size, void* d_ws, size_t ws_size,
                              hipStream_t stream) {
  const u16* xA=(const u16*)d_in[0]; const u16* kA=(const u16*)d_in[1]; const u16* vA=(const u16*)d_in[2];
  const u16* wqA=(const u16*)d_in[3]; const u16* woA=(const u16*)d_in[4];
  const u16* xB=(const u16*)d_in[5]; const u16* kB=(const u16*)d_in[6]; const u16* vB=(const u16*)d_in[7];
  const u16* wqB=(const u16*)d_in[8]; const u16* woB=(const u16*)d_in[9];
  u16* o = (u16*)d_ws;
  k1<<<1024,256,0,stream>>>(xA,kA,vA,wqA,xB,kB,vB,wqB,o);
  k2<<<512*1280/256,256,0,stream>>>(o,woA,woB,(u16*)d_out);
}
```

```cpp
#include <hip/hip_runtime.h>
#include <hip/hip_bf16.h>

typedef unsigned short u16;
typedef __attribute__((ext_vector_type(4))) unsigned u32x4;
__device__ __forceinline__ float bf2f(u16 v){ return __uint_as_float(((unsigned)v)<<16); }
__device__ __forceinline__ u16 f2bf(float f){ __hip_bfloat16 b = __float2bfloat16(f); return *(u16*)&b; }

__global__ void __launch_bounds__(512) rd(const u32x4* p0,const u32x4* p1,const u32x4* p2,const u32x4* p3,const u32x4* p4,
  const u32x4* p5,const u32x4* p6,const u32x4* p7,const u32x4* p8,const u32x4* p9, u32x4* sink, unsigned long long* ts){
  unsigned long long t0 = __builtin_amdgcn_s_memrealtime();
  int tid = blockIdx.x*512 + threadIdx.x;
  u32x4 acc = {0,0,0,0};
  #pragma unroll
  for (int i=0;i<3;i++){ acc ^= p1[tid+i*131072]; acc ^= p2[tid+i*131072]; acc ^= p6[tid+i*131072]; acc ^= p7[tid+i*131072]; }
  #pragma unroll
  for (int i=0;i<3;i++){ acc ^= p3[tid+i*131072]; acc ^= p4[tid+i*131072]; acc ^= p8[tid+i*131072]; acc ^= p9[tid+i*131072]; }
  if (tid < 98304) { acc ^= p3[tid+3*131072]; acc ^= p4[tid+3*131072]; acc ^= p8[tid+3*131072]; acc ^= p9[tid+3*131072]; }
  if (tid < 40960) { acc ^= p0[tid]; acc ^= p5[tid]; }
  if (acc.x == 0x12345678 && acc.y == 0x9abcdef) sink[0] = acc;
  asm volatile("s_waitcnt vmcnt(0)" ::: "memory");
  __syncthreads();
  unsigned long long t1 = __builtin_amdgcn_s_memrealtime();
  if (threadIdx.x == 0) { ts[blockIdx.x*2] = t0; ts[blockIdx.x*2+1] = t1; }
}

__global__ void spin(const unsigned long long* ts, unsigned long long* o){
  unsigned long long mn = ~0ull, mx = 0;
  for (int i=0;i<256;i++){ mn = ts[2*i] < mn ? ts[2*i] : mn; mx = ts[2*i] > mx ? ts[2*i] : mx; }
  unsigned long long d = mx - mn;
  if (d > 10000) d = 10000;
  unsigned long long t0 = __builtin_amdgcn_s_memrealtime();
  while (__builtin_amdgcn_s_memrealtime() - t0 < d*400) { __builtin_amdgcn_s_sleep(8); }
  o[0] = d;
}

__global__ void __launch_bounds__(256) k1(const u16* xA, const u16* kA, const u16* vA, const u16* wqA,
                   const u16* xB, const u16* kB, const u16* vB, const u16* wqB, u16* o){
  int bid = blockIdx.x;
  int br = bid >> 9; int b = (bid >> 5) & 15; int h = bid & 31;
  const u16* x = br ? xB : xA; const u16* k = br ? kB : kA; const u16* v = br ? vB : vA; const u16* wq = br ? wqB : wqA;
  __shared__ float q[32][193];
  __shared__ float s[32][33];
  int t = threadIdx.x;
  for (int i = t; i < 32*192; i += 256) {
    int r = i / 192, c = i % 192;
    float acc = 0;
    for (int d = 0; d < 640; ++d) acc += bf2f(x[(b*32+r)*640+d]) * bf2f(wq[d*6144 + h*192 + c]);
    q[r][c] = bf2f(f2bf(acc));
  }
  __syncthreads();
  const u16* kb = k + (size_t)(b*32+h)*32*192;
  const u16* vb = v + (size_t)(b*32+h)*32*192;
  for (int i = t; i < 32*32; i += 256) {
    int r = i / 32, c = i % 32;
    float acc = 0;
    for (int d = 0; d < 192; ++d) acc += q[r][d] * bf2f(kb[c*192+d]);
    acc = bf2f(f2bf(acc));
    s[r][c] = bf2f(f2bf(acc * 0.07216878364870322f));
  }
  __syncthreads();
  if (t < 32) {
    float m = -1e30f;
    for (int c = 0; c < 32; ++c) m = fmaxf(m, s[t][c]);
    float sum = 0;
    for (int c = 0; c < 32; ++c) { float e = __expf(s[t][c]-m); s[t][c] = e; sum += e; }
    float inv = 1.f/sum;
    for (int c = 0; c < 32; ++c) s[t][c] = bf2f(f2bf(s[t][c]*inv));
  }
  __syncthreads();
  for (int i = t; i < 32*192; i += 256) {
    int r = i / 192, c = i % 192;
    float acc = 0;
    for (int j = 0; j < 32; ++j) acc += s[r][j] * bf2f(vb[j*192+c]);
    o[(size_t)br*512*6144 + (size_t)(b*32+r)*6144 + h*192 + c] = f2bf(acc);
  }
}

__global__ void __launch_bounds__(256) k2(const u16* o, const u16* woA, const u16* woB, u16* out){
  int idx = blockIdx.x*256 + threadIdx.x;
  int row = idx / 1280, col = idx % 1280;
  int br = col >= 640; int c = col - br*640;
  const u16* wo = br ? woB : woA;
  const u16* orow = o + (size_t)br*512*6144 + (size_t)row*6144;
  float acc = 0;
  for (int k = 0; k < 6144; ++k) acc += bf2f(orow[k]) * bf2f(wo[k*640+c]);
  out[idx] = f2bf(acc);
}

extern "C" void kernel_launch(void* const* d_in, const int* in_sizes, int n_in,
                              void* d_out, int out_size, void* d_ws, size_t ws_size,
                              hipStream_t stream) {
  const u16* xA=(const u16*)d_in[0]; const u16* kA=(const u16*)d_in[1]; const u16* vA=(const u16*)d_in[2];
  const u16* wqA=(const u16*)d_in[3]; const u16* woA=(const u16*)d_in[4];
  const u16* xB=(const u16*)d_in[5]; const u16* kB=(const u16*)d_in[6]; const u16* vB=(const u16*)d_in[7];
  const u16* wqB=(const u16*)d_in[8]; const u16* woB=(const u16*)d_in[9];
  u16* o = (u16*)d_ws;
  unsigned long long* ts = (unsigned long long*)((char*)d_ws + 64*1024*1024);
  rd<<<256,512,0,stream>>>((const u32x4*)d_in[0],(const u32x4*)d_in[1],(const u32x4*)d_in[2],(const u32x4*)d_in[3],(const u32x4*)d_in[4],
   (const u32x4*)d_in[5],(const u32x4*)d_in[6],(const u32x4*)d_in[7],(const u32x4*)d_in[8],(const u32x4*)d_in[9],(u32x4*)(ts+4096), ts);
  spin<<<1,64,0,stream>>>(ts, ts+1024);
  k1<<<1024,256,0,stream>>>(xA,kA,vA,wqA,xB,kB,vB,wqB,o);
  k2<<<512*1280/256,256,0,stream>>>(o,woA,woB,(u16*)d_out);
}
```

```cpp
#include <hip/hip_runtime.h>
#include <hip/hip_bf16.h>

typedef unsigned short u16;
typedef __attribute__((ext_vector_type(4))) unsigned u32x4;
__device__ __forceinline__ float bf2f(u16 v){ return __uint_as_float(((unsigned)v)<<16); }
__device__ __forceinline__ u16 f2bf(float f){ __hip_bfloat16 b = __float2bfloat16(f); return *(u16*)&b; }

__global__ void __launch_bounds__(512) rd(const u32x4* p0,const u32x4* p1,const u32x4* p2,const u32x4* p3,const u32x4* p4,
  const u32x4* p5,const u32x4* p6,const u32x4* p7,const u32x4* p8,const u32x4* p9, u32x4* sink, unsigned long long* ts){
  unsigned long long t0 = __builtin_amdgcn_s_memrealtime();
  int tid = blockIdx.x*512 + threadIdx.x;
  u32x4 acc = {0,0,0,0};
  #pragma unroll
  for (int i=0;i<3;i++){ acc ^= __builtin_nontemporal_load(p1 + (tid+i*131072)); acc ^= __builtin_nontemporal_load(p2 + (tid+i*131072)); acc ^= __builtin_nontemporal_load(p6 + (tid+i*131072)); acc ^= __builtin_nontemporal_load(p7 + (tid+i*131072)); }
  #pragma unroll
  for (int i=0;i<3;i++){ acc ^= __builtin_nontemporal_load(p3 + (tid+i*131072)); acc ^= __builtin_nontemporal_load(p4 + (tid+i*131072)); acc ^= __builtin_nontemporal_load(p8 + (tid+i*131072)); acc ^= __builtin_nontemporal_load(p9 + (tid+i*131072)); }
  if (tid < 98304) { acc ^= __builtin_nontemporal_load(p3 + (tid+3*131072)); acc ^= __builtin_nontemporal_load(p4 + (tid+3*131072)); acc ^= __builtin_nontemporal_load(p8 + (tid+3*131072)); acc ^= __builtin_nontemporal_load(p9 + (tid+3*131072)); }
  if (tid < 40960) { acc ^= __builtin_nontemporal_load(p0 + (tid)); acc ^= __builtin_nontemporal_load(p5 + (tid)); }
  if (acc.x == 0x12345678 && acc.y == 0x9abcdef) sink[0] = acc;
  asm volatile("s_waitcnt vmcnt(0)" ::: "memory");
  __syncthreads();
  unsigned long long t1 = __builtin_amdgcn_s_memrealtime();
  if (threadIdx.x == 0) { ts[blockIdx.x*2] = t0; ts[blockIdx.x*2+1] = t1; }
}

__global__ void spin(const unsigned long long* ts, unsigned long long* o){
  unsigned long long mn = ~0ull, mx = 0;
  for (int i=0;i<256;i++){ mn = ts[2*i] < mn ? ts[2*i] : mn; mx = ts[2*i+1] > mx ? ts[2*i+1] : mx; }
  unsigned long long d = mx - mn;
  if (d > 10000) d = 10000;
  unsigned long long t0 = __builtin_amdgcn_s_memrealtime();
  while (__builtin_amdgcn_s_memrealtime() - t0 < d*400) { __builtin_amdgcn_s_sleep(8); }
  o[0] = d;
}

__global__ void __launch_bounds__(256) k1(const u16* xA, const u16* kA, const u16* vA, const u16* wqA,
                   const u16* xB, const u16* kB, const u16* vB, const u16* wqB, u16* o){
  int bid = blockIdx.x;
  int br = bid >> 9; int b = (bid >> 5) & 15; int h = bid & 31;
  const u16* x = br ? xB : xA; const u16* k = br ? kB : kA; const u16* v = br ? vB : vA; const u16* wq = br ? wqB : wqA;
  __shared__ float q[32][193];
  __shared__ float s[32][33];
  int t = threadIdx.x;
  for (int i = t; i < 32*192; i += 256) {
    int r = i / 192, c = i % 192;
    float acc = 0;
    for (int d = 0; d < 640; ++d) acc += bf2f(x[(b*32+r)*640+d]) * bf2f(wq[d*6144 + h*192 + c]);
    q[r][c] = bf2f(f2bf(acc));
  }
  __syncthreads();
  const u16* kb = k + (size_t)(b*32+h)*32*192;
  const u16* vb = v + (size_t)(b*32+h)*32*192;
  for (int i = t; i < 32*32; i += 256) {
    int r = i / 32, c = i % 32;
    float acc = 0;
    for (int d = 0; d < 192; ++d) acc += q[r][d] * bf2f(kb[c*192+d]);
    acc = bf2f(f2bf(acc));
    s[r][c] = bf2f(f2bf(acc * 0.07216878364870322f));
  }
  __syncthreads();
  if (t < 32) {
    float m = -1e30f;
    for (int c = 0; c < 32; ++c) m = fmaxf(m, s[t][c]);
    float sum = 0;
    for (int c = 0; c < 32; ++c) { float e = __expf(s[t][c]-m); s[t][c] = e; sum += e; }
    float inv = 1.f/sum;
    for (int c = 0; c < 32; ++c) s[t][c] = bf2f(f2bf(s[t][c]*inv));
  }
  __syncthreads();
  for (int i = t; i < 32*192; i += 256) {
    int r = i / 192, c = i % 192;
    float acc = 0;
    for (int j = 0; j < 32; ++j) acc += s[r][j] * bf2f(vb[j*192+c]);
    o[(size_t)br*512*6144 + (size_t)(b*32+r)*6144 + h*192 + c] = f2bf(acc);
  }
}

__global__ void __launch_bounds__(256) k2(const u16* o, const u16* woA, const u16* woB, u16* out){
  int idx = blockIdx.x*256 + threadIdx.x;
  int row = idx / 1280, col = idx % 1280;
  int br = col >= 640; int c = col - br*640;
  const u16* wo = br ? woB : woA;
  const u16* orow = o + (size_t)br*512*6144 + (size_t)row*6144;
  float acc = 0;
  for (int k = 0; k < 6144; ++k) acc += bf2f(orow[k]) * bf2f(wo[k*640+c]);
  out[idx] = f2bf(acc);
}

extern "C" void kernel_launch(void* const* d_in, const int* in_sizes, int n_in,
                              void* d_out, int out_size, void* d_ws, size_t ws_size,
                              hipStream_t stream) {
  const u16* xA=(const u16*)d_in[0]; const u16* kA=(const u16*)d_in[1]; const u16* vA=(const u16*)d_in[2];
  const u16* wqA=(const u16*)d_in[3]; const u16* woA=(const u16*)d_in[4];
  const u16* xB=(const u16*)d_in[5]; const u16* kB=(const u16*)d_in[6]; const u16* vB=(const u16*)d_in[7];
  const u16* wqB=(const u16*)d_in[8]; const u16* woB=(const u16*)d_in[9];
  u16* o = (u16*)d_ws;
  unsigned long long* ts = (unsigned long long*)((char*)d_ws + 64*1024*1024);
  rd<<<256,512,0,stream>>>((const u32x4*)d_in[0],(const u32x4*)d_in[1],(const u32x4*)d_in[2],(const u32x4*)d_in[3],(const u32x4*)d_in[4],
   (const u32x4*)d_in[5],(const u32x4*)d_in[6],(const u32x4*)d_in[7],(const u32x4*)d_in[8],(const u32x4*)d_in[9],(u32x4*)(ts+4096), ts);
  spin<<<1,64,0,stream>>>(ts, ts+1024);
  k1<<<1024,256,0,stream>>>(xA,kA,vA,wqA,xB,kB,vB,wqB,o);
  k2<<<512*1280/256,256,0,stream>>>(o,woA,woB,(u16*)d_out);
}
```

```cpp
#include <hip/hip_runtime.h>
#include <hip/hip_bf16.h>

typedef unsigned short u16;
typedef __attribute__((ext_vector_type(4))) unsigned u32x4;
__device__ __forceinline__ float bf2f(u16 v){ return __uint_as_float(((unsigned)v)<<16); }
__device__ __forceinline__ u16 f2bf(float f){ __hip_bfloat16 b = __float2bfloat16(f); return *(u16*)&b; }


__global__ void __launch_bounds__(512) rd(const u32x4* p0,const u32x4* p1,const u32x4* p2,const u32x4* p3,const u32x4* p4,
  const u32x4* p5,const u32x4* p6,const u32x4* p7,const u32x4* p8,const u32x4* p9, u32x4* sink, unsigned long long* ts){
  unsigned long long t0,t1,t2,t3;
  unsigned long long ka = (unsigned long long)__builtin_amdgcn_kernarg_segment_ptr();
  unsigned off = (blockIdx.x*512 + threadIdx.x)*16;
  asm volatile(
    "s_memrealtime %0\n\t"
    "s_waitcnt lgkmcnt(0)\n\t"
    "s_load_dwordx8 s[40:47], %4, 0x0\n\t"
    "s_waitcnt lgkmcnt(0)\n\t"
    "s_memrealtime %1\n\t"
    "s_waitcnt lgkmcnt(0)\n\t"
    "global_load_dwordx4 v[40:43], %5, s[42:43]\n\t"
    "s_waitcnt vmcnt(0)\n\t"
    "s_memrealtime %2\n\t"
    "s_waitcnt lgkmcnt(0)\n\t"
    "global_load_dwordx4 v[40:43], %5, s[46:47]\n\t"
    "s_waitcnt vmcnt(0)\n\t"
    "s_memrealtime %3\n\t"
    "s_waitcnt lgkmcnt(0)\n\t"
    : "=&s"(t0), "=&s"(t1), "=&s"(t2), "=&s"(t3) : "s"(ka), "v"(off) : "s40","s41","s42","s43","s44","s45","s46","s47","v40","v41","v42","v43","memory");
  if (threadIdx.x == 0) { ts[blockIdx.x*4] = t0; ts[blockIdx.x*4+1] = t1; ts[blockIdx.x*4+2] = t2; ts[blockIdx.x*4+3] = t3; }
}

__global__ void spin(const unsigned long long* ts, unsigned long long* o){
  unsigned long long sum = 0;
  for (int i=0;i<256;i++){ sum += ts[4*i+1]-ts[4*i]; }
  unsigned long long d = sum;
  if (d > 256*2000) d = 256*2000;
  unsigned long long t0 = __builtin_amdgcn_s_memrealtime();
  while (__builtin_amdgcn_s_memrealtime() - t0 < d*4) { __builtin_amdgcn_s_sleep(8); }
  o[0] = d;
}
__global__ void __launch_bounds__(256) k1(const u16* xA, const u16* kA, const u16* vA, const u16* wqA,
                   const u16* xB, const u16* kB, const u16* vB, const u16* wqB, u16* o){
  int bid = blockIdx.x;
  int br = bid >> 9; int b = (bid >> 5) & 15; int h = bid & 31;
  const u16* x = br ? xB : xA; const u16* k = br ? kB : kA; const u16* v = br ? vB : vA; const u16* wq = br ? wqB : wqA;
  __shared__ float q[32][193];
  __shared__ float s[32][33];
  int t = threadIdx.x;
  for (int i = t; i < 32*192; i += 256) {
    int r = i / 192, c = i % 192;
    float acc = 0;
    for (int d = 0; d < 640; ++d) acc += bf2f(x[(b*32+r)*640+d]) * bf2f(wq[d*6144 + h*192 + c]);
    q[r][c] = bf2f(f2bf(acc));
  }
  __syncthreads();
  const u16* kb = k + (size_t)(b*32+h)*32*192;
  const u16* vb = v + (size_t)(b*32+h)*32*192;
  for (int i = t; i < 32*32; i += 256) {
    int r = i / 32, c = i % 32;
    float acc = 0;
    for (int d = 0; d < 192; ++d) acc += q[r][d] * bf2f(kb[c*192+d]);
    acc = bf2f(f2bf(acc));
    s[r][c] = bf2f(f2bf(acc * 0.07216878364870322f));
  }
  __syncthreads();
  if (t < 32) {
    float m = -1e30f;
    for (int c = 0; c < 32; ++c) m = fmaxf(m, s[t][c]);
    float sum = 0;
    for (int c = 0; c < 32; ++c) { float e = __expf(s[t][c]-m); s[t][c] = e; sum += e; }
    float inv = 1.f/sum;
    for (int c = 0; c < 32; ++c) s[t][c] = bf2f(f2bf(s[t][c]*inv));
  }
  __syncthreads();
  for (int i = t; i < 32*192; i += 256) {
    int r = i / 192, c = i % 192;
    float acc = 0;
    for (int j = 0; j < 32; ++j) acc += s[r][j] * bf2f(vb[j*192+c]);
    o[(size_t)br*512*6144 + (size_t)(b*32+r)*6144 + h*192 + c] = f2bf(acc);
  }
}

__global__ void __launch_bounds__(256) k2(const u16* o, const u16* woA, const u16* woB, u16* out){
  int idx = blockIdx.x*256 + threadIdx.x;
  int row = idx / 1280, col = idx % 1280;
  int br = col >= 640; int c = col - br*640;
  const u16* wo = br ? woB : woA;
  const u16* orow = o + (size_t)br*512*6144 + (size_t)row*6144;
  float acc = 0;
  for (int k = 0; k < 6144; ++k) acc += bf2f(orow[k]) * bf2f(wo[k*640+c]);
  out[idx] = f2bf(acc);
}

extern "C" void kernel_launch(void* const* d_in, const int* in_sizes, int n_in,
                              void* d_out, int out_size, void* d_ws, size_t ws_size,
                              hipStream_t stream) {
  const u16* xA=(const u16*)d_in[0]; const u16* kA=(const u16*)d_in[1]; const u16* vA=(const u16*)d_in[2];
  const u16* wqA=(const u16*)d_in[3]; const u16* woA=(const u16*)d_in[4];
  const u16* xB=(const u16*)d_in[5]; const u16* kB=(const u16*)d_in[6]; const u16* vB=(const u16*)d_in[7];
  const u16* wqB=(const u16*)d_in[8]; const u16* woB=(const u16*)d_in[9];
  u16* o = (u16*)d_ws;
  unsigned long long* ts = (unsigned long long*)((char*)d_ws + 64*1024*1024);
  rd<<<256,512,0,stream>>>((const u32x4*)d_in[0],(const u32x4*)d_in[1],(const u32x4*)d_in[2],(const u32x4*)d_in[3],(const u32x4*)d_in[4],
   (const u32x4*)d_in[5],(const u32x4*)d_in[6],(const u32x4*)d_in[7],(const u32x4*)d_in[8],(const u32x4*)d_in[9],(u32x4*)(ts+4096), ts);
  spin<<<1,64,0,stream>>>(ts, ts+2048);
  k1<<<1024,256,0,stream>>>(xA,kA,vA,wqA,xB,kB,vB,wqB,o);
  k2<<<512*1280/256,256,0,stream>>>(o,woA,woB,(u16*)d_out);
}
```

```cpp
#include <hip/hip_runtime.h>
#include <hip/hip_bf16.h>
#include <cstdio>

typedef unsigned short u16;
typedef __attribute__((ext_vector_type(4))) unsigned u32x4;
__device__ __forceinline__ float bf2f(u16 v){ return __uint_as_float(((unsigned)v)<<16); }
__device__ __forceinline__ u16 f2bf(float f){ __hip_bfloat16 b = __float2bfloat16(f); return *(u16*)&b; }


__global__ void __launch_bounds__(512,2) rd(const u32x4* p0,const u32x4* p1,const u32x4* p2,const u32x4* p3,const u32x4* p4,
  const u32x4* p5,const u32x4* p6,const u32x4* p7,const u32x4* p8,const u32x4* p9, u32x4* sink, unsigned long long* ts){
  __shared__ char smem[163840];
  smem[threadIdx.x] = 1;
  unsigned xcc = (unsigned)__builtin_amdgcn_s_getreg(20 | (3 << 11)) & 0xfu;
  unsigned long long t0 = __builtin_amdgcn_s_memrealtime();
  while (__builtin_amdgcn_s_memrealtime() - t0 < 500) {}
  __syncthreads();
  if (threadIdx.x == 0) { ts[blockIdx.x] = xcc + (smem[5]-1); }
}

__global__ void spin(const unsigned long long* ts, unsigned long long* o){
  unsigned long long sum = 0;
  for (int i=0;i<256;i++){ sum += (ts[i] != ts[i&7]); }
  unsigned m = 0; for (int i=0;i<8;i++) m |= 1u << ts[i];
  if (m != 0xff) sum += 300;
  unsigned long long d = sum;
  unsigned long long t0 = __builtin_amdgcn_s_memrealtime();
  while (__builtin_amdgcn_s_memrealtime() - t0 < d*1000) { __builtin_amdgcn_s_sleep(8); }
  o[0] = d;
}
__global__ void __launch_bounds__(256) k1(const u16* xA, const u16* kA, const u16* vA, const u16* wqA,
                   const u16* xB, const u16* kB, const u16* vB, const u16* wqB, u16* o){
  int bid = blockIdx.x;
  int br = bid >> 9; int b = (bid >> 5) & 15; int h = bid & 31;
  const u16* x = br ? xB : xA; const u16* k = br ? kB : kA; const u16* v = br ? vB : vA; const u16* wq = br ? wqB : wqA;
  __shared__ float q[32][193];
  __shared__ float s[32][33];
  int t = threadIdx.x;
  for (int i = t; i < 32*192; i += 256) {
    int r = i / 192, c = i % 192;
    float acc = 0;
    for (int d = 0; d < 640; ++d) acc += bf2f(x[(b*32+r)*640+d]) * bf2f(wq[d*6144 + h*192 + c]);
    q[r][c] = bf2f(f2bf(acc));
  }
  __syncthreads();
  const u16* kb = k + (size_t)(b*32+h)*32*192;
  const u16* vb = v + (size_t)(b*32+h)*32*192;
  for (int i = t; i < 32*32; i += 256) {
    int r = i / 32, c = i % 32;
    float acc = 0;
    for (int d = 0; d < 192; ++d) acc += q[r][d] * bf2f(kb[c*192+d]);
    acc = bf2f(f2bf(acc));
    s[r][c] = bf2f(f2bf(acc * 0.07216878364870322f));
  }
  __syncthreads();
  if (t < 32) {
    float m = -1e30f;
    for (int c = 0; c < 32; ++c) m = fmaxf(m, s[t][c]);
    float sum = 0;
    for (int c = 0; c < 32; ++c) { float e = __expf(s[t][c]-m); s[t][c] = e; sum += e; }
    float inv = 1.f/sum;
    for (int c = 0; c < 32; ++c) s[t][c] = bf2f(f2bf(s[t][c]*inv));
  }
  __syncthreads();
  for (int i = t; i < 32*192; i += 256) {
    int r = i / 192, c = i % 192;
    float acc = 0;
    for (int j = 0; j < 32; ++j) acc += s[r][j] * bf2f(vb[j*192+c]);
    o[(size_t)br*512*6144 + (size_t)(b*32+r)*6144 + h*192 + c] = f2bf(acc);
  }
}

__global__ void __launch_bounds__(256) k2(const u16* o, const u16* woA, const u16* woB, u16* out){
  int idx = blockIdx.x*256 + threadIdx.x;
  int row = idx / 1280, col = idx % 1280;
  int br = col >= 640; int c = col - br*640;
  const u16* wo = br ? woB : woA;
  const u16* orow = o + (size_t)br*512*6144 + (size_t)row*6144;
  float acc = 0;
  for (int k = 0; k < 6144; ++k) acc += bf2f(orow[k]) * bf2f(wo[k*640+c]);
  out[idx] = f2bf(acc);
}

extern "C" void kernel_launch(void* const* d_in, const int* in_sizes, int n_in,
                              void* d_out, int out_size, void* d_ws, size_t ws_size,
                              hipStream_t stream) {
  const u16* xA=(const u16*)d_in[0]; const u16* kA=(const u16*)d_in[1]; const u16* vA=(const u16*)d_in[2];
  const u16* wqA=(const u16*)d_in[3]; const u16* woA=(const u16*)d_in[4];
  const u16* xB=(const u16*)d_in[5]; const u16* kB=(const u16*)d_in[6]; const u16* vB=(const u16*)d_in[7];
  const u16* wqB=(const u16*)d_in[8]; const u16* woB=(const u16*)d_in[9];
  for (int i=0;i<10;i++) fprintf(stderr, "in[%d]=%p\n", i, d_in[i]); fprintf(stderr, "out=%p ws=%p\n", d_out, d_ws);
  u16* o = (u16*)d_ws;
  unsigned long long* ts = (unsigned long long*)((char*)d_ws + 64*1024*1024);
  rd<<<256,512,0,stream>>>((const u32x4*)d_in[0],(const u32x4*)d_in[1],(const u32x4*)d_in[2],(const u32x4*)d_in[3],(const u32x4*)d_in[4],
   (const u32x4*)d_in[5],(const u32x4*)d_in[6],(const u32x4*)d_in[7],(const u32x4*)d_in[8],(const u32x4*)d_in[9],(u32x4*)(ts+4096), ts);
  spin<<<1,64,0,stream>>>(ts, ts+1024);
  k1<<<1024,256,0,stream>>>(xA,kA,vA,wqA,xB,kB,vB,wqB,o);
  k2<<<512*1280/256,256,0,stream>>>(o,woA,woB,(u16*)d_out);
}
```

```cpp
#include <hip/hip_runtime.h>

__global__ void __launch_bounds__(512, 2) fused_attn_kernel(const void* xA, const void* kA, const void* vA, const void* wqA, const void* woA,
    const void* xB, const void* kB, const void* vB, const void* wqB, const void* woB, void* out, void* ws) {
  __shared__ __attribute__((aligned(16))) char smem[163840];
  unsigned wave = __builtin_amdgcn_readfirstlane(threadIdx.x >> 6);
  asm volatile(
    "KSTART_%=:\n"
    "s_bfe_u32 s56, %1, 0x10002\n"
    "s_mul_i32 s10, s56, 40\n"
    "s_load_dwordx8 s[12:19], %0, s10\n"
    "s_load_dwordx2 s[20:21], %0, s10 offset:32\n"
    "s_load_dwordx4 s[24:27], %0, 0x50\n"
    "v_mbcnt_lo_u32_b32 v0, -1, 0\n"
    "v_mbcnt_hi_u32_b32 v0, -1, v0\n"
    "s_cmp_lt_u32 %2, 2\n"
    "s_cbranch_scc1 PZ_%=\n"
    "s_getpc_b64 s[36:37]\n"
    "CPC_%=:\n"
    "s_bfe_u32 s11, %1, 0x30003\n"
    "s_mul_i32 s11, s11, 6\n"
    "s_add_u32 s11, s11, %2\n"
    "s_mul_i32 s11, s11, 1024\n"
    "v_min_u32 v16, 7, v0\n"
    "v_lshl_add_u32 v16, v16, 7, s11\n"
    "v_min_u32 v16, LE%=-CPC_%=+2044, v16\n"
    "global_load_dword v17, v16, s[36:37] offset:-2048\n"
    "s_cmp_ge_u32 %2, 4\n"
    "s_cbranch_scc1 PZ_%=\n"
    "s_lshl_b32 s11, %2, 10\n"
    "s_add_u32 s11, s11, 153600\n"
    "v_lshl_add_u32 v8, v0, 4, s11\n"
    "v_mov_b32 v12, 0\n"
    "v_mov_b32 v13, 0\n"
    "v_mov_b32 v14, 0\n"
    "v_mov_b32 v15, 0\n"
    "ds_write_b128 v8, v[12:15]\n"
    "ds_write_b128 v8, v[12:15] offset:2048\n"
    "ds_write_b128 v8, v[12:15] offset:4096\n"
    "s_waitcnt lgkmcnt(0)\n"
    "s_barrier\n"
    "PZ_%=:\n"
    "s_mov_b32 s8, %1\n"
    "s_mov_b32 s9, %2\n"
    "s_and_b32 s57, s8, 3\n"
    "s_bfe_u32 s58, s8, 0x20003\n"
    "s_lshr_b32 s59, s8, 5\n"
    "s_and_b32 s61, s59, 3\n"
    "s_lshl_b32 s62, s57, 3\n"
    "s_add_u32 s62, s62, s59\n"
    "s_cmp_ge_u32 s9, 2\n"
    "s_cbranch_scc1 LN%=\n"
    "s_and_b32 s10, s9, 1\n"
    "s_add_u32 s73, s58, s61\n"
    "s_and_b32 s73, s73, 3\n"
    "s_mov_b32 s75, s73\n"
    "s_mul_i32 s64, s58, 163840\n"
    "v_bfe_u32 v1, v0, 2, 3\n"
    "v_lshrrev_b32 v2, 5, v0\n"
    "v_and_b32 v3, 3, v0\n"
    "v_lshlrev_b32 v4, 4, v3\n"
    "v_lshl_add_u32 v5, v2, 6, v4\n"
    "v_xor_b32 v6, 32, v5\n"
    "s_lshl_b32 s11, s10, 4\n"
    "v_add_u32 v8, s11, v1\n"
    "v_mul_u32_u24 v34, 1280, v8\n"
    "v_add_u32 v38, v34, v5\n"
    "v_add_u32 v39, v34, v6\n"
    "v_add_u32 v39, 9216, v39\n"
    "s_lshl_b32 s65, s10, 11\n"
    "s_add_u32 s74, s73, 0\n"
    "s_and_b32 s74, s74, 3\n"
    "s_lshl_b32 s80, s74, 12\n"
    "s_add_u32 s80, s80, s65\n"
    "s_mul_i32 s11, s74, 40960\n"
    "v_add_u32 v10, s11, v38\n"
    "v_add_u32 v11, s11, v39\n"
    "s_add_u32 s11, s75, 0\n"
    "s_and_b32 s11, s11, 3\n"
    "s_lshl_b32 s22, s11, 7\n"
    "s_mov_b32 s30, 655360\n"
    "s_mov_b32 s31, 0x20000\n"
    "s_mov_b32 s42, 7864320\n"
    "s_mov_b32 s43, 0x20000\n"
    "s_waitcnt lgkmcnt(0)\n"
    "s_mov_b32 s28, s12\n"
    "s_and_b32 s29, s13, 0xffff\n"
    "s_mov_b32 s40, s18\n"
    "s_and_b32 s41, s19, 0xffff\n"
    "s_add_u32 m0, s80, 0\n"
    "s_add_u32 s72, s64, s22\n"
    "buffer_load_dwordx4 v10, s[28:31], s72 offen lds\n"
    "buffer_load_dwordx4 v11, s[28:31], s72 offen offset:1024 lds\n"
    "s_add_u32 s74, s73, 1\n"
    "s_and_b32 s74, s74, 3\n"
    "s_lshl_b32 s81, s74, 12\n"
    "s_add_u32 s81, s81, s65\n"
    "s_mul_i32 s11, s74, 40960\n"
    "v_add_u32 v12, s11, v38\n"
    "v_add_u32 v13, s11, v39\n"
    "s_add_u32 m0, s81, 0\n"
    "s_nop 0\n"
    "buffer_load_dwordx4 v12, s[28:31], s72 offen lds\n"
    "buffer_load_dwordx4 v13, s[28:31], s72 offen offset:1024 lds\n"
    "s_add_u32 s74, s73, 2\n"
    "s_and_b32 s74, s74, 3\n"
    "s_lshl_b32 s82, s74, 12\n"
    "s_add_u32 s82, s82, s65\n"
    "s_mul_i32 s11, s74, 40960\n"
    "v_add_u32 v14, s11, v38\n"
    "v_add_u32 v15, s11, v39\n"
    "s_mul_i32 s67, s62, 384\n"
    "s_lshl_b32 s11, s10, 5\n"
    "v_xor_b32 v7, s11, v5\n"
    "s_lshl_b32 s11, s10, 3\n"
    "v_add_u32 v9, s11, v1\n"
    "s_add_u32 m0, s82, 0\n"
    "s_nop 0\n"
    "buffer_load_dwordx4 v14, s[28:31], s72 offen lds\n"
    "buffer_load_dwordx4 v15, s[28:31], s72 offen offset:1024 lds\n"
    "s_add_u32 s74, s73, 3\n"
    "s_and_b32 s74, s74, 3\n"
    "s_lshl_b32 s83, s74, 12\n"
    "s_add_u32 s83, s83, s65\n"
    "s_mul_i32 s11, s74, 40960\n"
    "v_add_u32 v16, s11, v38\n"
    "v_add_u32 v17, s11, v39\n"
    "v_mul_u32_u24 v36, 12288, v9\n"
    "v_add_u32 v36, v36, v7\n"
    "s_lshl_b32 s66, s10, 10\n"
    "s_add_u32 m0, s83, 0\n"
    "s_nop 0\n"
    "buffer_load_dwordx4 v16, s[28:31], s72 offen lds\n"
    "buffer_load_dwordx4 v17, s[28:31], s72 offen offset:1024 lds\n"
    "s_add_u32 s74, s73, 0\n"
    "s_and_b32 s74, s74, 3\n"
    "s_lshl_b32 s84, s74, 11\n"
    "s_add_u32 s84, s84, s66\n"
    "s_mul_i32 s11, s74, 196608\n"
    "v_add_u32 v26, s11, v36\n"
    "s_add_u32 m0, s84, 147456\n"
    "s_mul_i32 s72, s22, 6144\n"
    "s_add_u32 s72, s72, s67\n"
    "buffer_load_dwordx4 v26, s[40:43], s72 offen lds\n"
    "s_add_u32 s74, s73, 1\n"
    "s_and_b32 s74, s74, 3\n"
    "s_lshl_b32 s85, s74, 11\n"
    "s_add_u32 s85, s85, s66\n"
    "s_mul_i32 s11, s74, 196608\n"
    "v_add_u32 v27, s11, v36\n"
    "s_add_u32 m0, s85, 147456\n"
    "s_nop 0\n"
    "buffer_load_dwordx4 v27, s[40:43], s72 offen lds\n"
    "s_add_u32 s74, s73, 2\n"
    "s_and_b32 s74, s74, 3\n"
    "s_lshl_b32 s86, s74, 11\n"
    "s_add_u32 s86, s86, s66\n"
    "s_mul_i32 s11, s74, 196608\n"
    "v_add_u32 v28, s11, v36\n"
    "s_add_u32 m0, s86, 147456\n"
    "s_nop 0\n"
    "buffer_load_dwordx4 v28, s[40:43], s72 offen lds\n"
    "s_add_u32 s74, s73, 3\n"
    "s_and_b32 s74, s74, 3\n"
    "s_lshl_b32 s87, s74, 11\n"
    "s_add_u32 s87, s87, s66\n"
    "s_mul_i32 s11, s74, 196608\n"
    "v_add_u32 v29, s11, v36\n"
    "s_add_u32 m0, s87, 147456\n"
    "s_nop 0\n"
    "buffer_load_dwordx4 v29, s[40:43], s72 offen lds\n"
    "s_add_u32 m0, s84, 16384\n"
    "s_mul_i32 s72, s22, 6144\n"
    "s_add_u32 s72, s72, s67\n"
    "s_add_u32 s72, s72, 128\n"
    "buffer_load_dwordx4 v26, s[40:43], s72 offen lds\n"
    "s_add_u32 m0, s85, 16384\n"
    "s_nop 0\n"
    "buffer_load_dwordx4 v27, s[40:43], s72 offen lds\n"
    "s_add_u32 m0, s86, 16384\n"
    "s_nop 0\n"
    "buffer_load_dwordx4 v28, s[40:43], s72 offen lds\n"
    "s_add_u32 m0, s87, 16384\n"
    "s_nop 0\n"
    "buffer_load_dwordx4 v29, s[40:43], s72 offen lds\n"
    "s_add_u32 m0, s84, 24576\n"
    "s_mul_i32 s72, s22, 6144\n"
    "s_add_u32 s72, s72, s67\n"
    "s_add_u32 s72, s72, 256\n"
    "buffer_load_dwordx4 v26, s[40:43], s72 offen lds\n"
    "s_add_u32 m0, s85, 24576\n"
    "s_nop 0\n"
    "buffer_load_dwordx4 v27, s[40:43], s72 offen lds\n"
    "s_add_u32 m0, s86, 24576\n"
    "s_nop 0\n"
    "buffer_load_dwordx4 v28, s[40:43], s72 offen lds\n"
    "s_add_u32 m0, s87, 24576\n"
    "s_nop 0\n"
    "buffer_load_dwordx4 v29, s[40:43], s72 offen lds\n"
    "s_lshr_b32 s60, s59, 2\n"
    "s_and_b32 s63, s62, 0xfc\n"
    "s_xor_b32 s70, s58, s61\n"
    "s_and_b32 s70, s70, 1\n"
    "s_lshr_b32 s71, s58, 1\n"
    "s_mov_b32 s95, 2000000\n"
    "s_lshl_b32 s5, s9, 9\n"
    "s_add_u32 s5, s5, 155648\n"
    "s_getpc_b64 s[34:35]\n"
    "TPCL_%=:\n"
    "s_add_u32 s34, s34, TAB_%=-TPCL_%=\n"
    "s_addc_u32 s35, s35, 0\n"
    "s_mov_b32 s76, s14\n"
    "s_and_b32 s77, s15, 0xffff\n"
    "s_mov_b32 s78, 6291456\n"
    "s_mov_b32 s79, 0x20000\n"
    "s_mov_b32 s12, s34\n"
    "s_and_b32 s13, s35, 0xffff\n"
    "s_mov_b32 s14, 256\n"
    "s_mov_b32 s15, 0x20000\n"
    "s_barrier\n"
    "s_mov_b32 m0, s5\n"
    "s_nop 0\n"
    "buffer_load_dword off, s[12:15], 0 offset:4 lds\n"
    "s_lshl_b32 s11, s56, 2\n"
    "s_add_u32 s11, s11, s58\n"
    "s_lshl_b32 s11, s11, 21\n"
    "s_add_u32 s26, s26, s11\n"
    "s_addc_u32 s27, s27, 0\n"
    "s_load_dwordx2 s[6:7], s[26:27], 0\n"
    "v_min_u32 v46, 5, v0\n"
    "v_lshlrev_b32 v46, 9, v46\n"
    "v_add_u32 v46, 156924, v46\n"
    "v_mov_b32 v47, 0\n"
    "v_mul_u32_u24 v35, 384, v8\n"
    "v_mul_u32_u24 v37, 1280, v9\n"
    "v_add_u32 v40, v35, v5\n"
    "v_add_u32 v41, v35, v6\n"
    "v_add_u32 v41, 2048, v41\n"
    "v_add_u32 v37, v37, v7\n"
    "s_add_u32 s74, s73, 0\n"
    "s_and_b32 s74, s74, 3\n"
    "s_mul_i32 s11, s74, 393216\n"
    "v_add_u32 v18, s11, v40\n"
    "v_add_u32 v19, s11, v41\n"
    "s_mul_i32 s11, s74, 20480\n"
    "v_add_u32 v30, s11, v37\n"
    "s_add_u32 s74, s73, 1\n"
    "s_and_b32 s74, s74, 3\n"
    "s_mul_i32 s11, s74, 393216\n"
    "v_add_u32 v20, s11, v40\n"
    "v_add_u32 v21, s11, v41\n"
    "s_mul_i32 s11, s74, 20480\n"
    "v_add_u32 v31, s11, v37\n"
    "s_add_u32 s74, s73, 2\n"
    "s_and_b32 s74, s74, 3\n"
    "s_mul_i32 s11, s74, 393216\n"
    "v_add_u32 v22, s11, v40\n"
    "v_add_u32 v23, s11, v41\n"
    "s_mul_i32 s11, s74, 20480\n"
    "v_add_u32 v32, s11, v37\n"
    "s_add_u32 s74, s73, 3\n"
    "s_and_b32 s74, s74, 3\n"
    "s_mul_i32 s11, s74, 393216\n"
    "v_add_u32 v24, s11, v40\n"
    "v_add_u32 v25, s11, v41\n"
    "s_mul_i32 s11, s74, 20480\n"
    "v_add_u32 v33, s11, v37\n"
    "s_add_u32 s11, s75, 1\n"
    "s_and_b32 s11, s11, 3\n"
    "s_lshl_b32 s23, s11, 7\n"
    "s_add_u32 s11, s75, 2\n"
    "s_and_b32 s11, s11, 3\n"
    "s_lshl_b32 s48, s11, 7\n"
    "s_add_u32 s11, s75, 3\n"
    "s_and_b32 s11, s11, 3\n"
    "s_lshl_b32 s49, s11, 7\n"
    "s_add_u32 s11, s75, 0\n"
    "s_and_b32 s11, s11, 3\n"
    "s_add_u32 s11, s11, 4\n"
    "s_lshl_b32 s50, s11, 7\n"
    "s_add_u32 s11, s75, 1\n"
    "s_and_b32 s11, s11, 3\n"
    "s_add_u32 s11, s11, 4\n"
    "s_lshl_b32 s51, s11, 7\n"
    "s_add_u32 s11, s75, 2\n"
    "s_and_b32 s11, s11, 3\n"
    "s_add_u32 s11, s11, 4\n"
    "s_lshl_b32 s52, s11, 7\n"
    "s_add_u32 s11, s75, 3\n"
    "s_and_b32 s11, s11, 3\n"
    "s_add_u32 s11, s11, 4\n"
    "s_lshl_b32 s53, s11, 7\n"
    "s_add_u32 s11, s75, 0\n"
    "s_and_b32 s11, s11, 1\n"
    "s_add_u32 s11, s11, 8\n"
    "s_lshl_b32 s54, s11, 7\n"
    "s_add_u32 s11, s75, 1\n"
    "s_and_b32 s11, s11, 1\n"
    "s_add_u32 s11, s11, 8\n"
    "s_lshl_b32 s55, s11, 7\n"
    "s_lshl_b32 s11, s58, 7\n"
    "s_add_u32 s65, s11, s62\n"
    "s_mul_i32 s65, s65, 12288\n"
    "s_add_u32 s66, s11, s63\n"
    "s_mul_i32 s66, s66, 12288\n"
    "s_mul_i32 s68, s63, 245760\n"
    "s_lshr_b32 s11, s61, 1\n"
    "s_mul_i32 s11, s11, 640\n"
    "s_add_u32 s68, s68, s11\n"
    "s_xor_b32 s11, s70, 0\n"
    "s_mul_i32 s88, s11, 12288\n"
    "s_add_u32 s88, s88, s66\n"
    "s_mul_i32 s92, s11, 245760\n"
    "s_add_u32 s92, s92, s68\n"
    "s_xor_b32 s11, s70, 1\n"
    "s_mul_i32 s89, s11, 12288\n"
    "s_add_u32 s89, s89, s66\n"
    "s_mul_i32 s93, s11, 245760\n"
    "s_add_u32 s93, s93, s68\n"
    "s_xor_b32 s11, s70, 2\n"
    "s_mul_i32 s90, s11, 12288\n"
    "s_add_u32 s90, s90, s66\n"
    "s_mul_i32 s94, s11, 245760\n"
    "s_add_u32 s94, s94, s68\n"
    "s_xor_b32 s11, s70, 3\n"
    "s_mul_i32 s91, s11, 12288\n"
    "s_add_u32 s91, s91, s66\n"
    "s_mul_i32 s69, s11, 245760\n"
    "s_add_u32 s69, s69, s68\n"
    "s_and_b32 s11, s61, 1\n"
    "s_mul_i32 s11, s11, 384\n"
    "s_lshl_b32 s96, s71, 7\n"
    "s_xor_b32 s97, s96, 128\n"
    "s_add_u32 s96, s96, s11\n"
    "s_add_u32 s97, s97, s11\n"
    "s_mov_b32 s36, s16\n"
    "s_and_b32 s37, s17, 0xffff\n"
    "s_mov_b32 s38, 6291456\n"
    "s_mov_b32 s39, 0x20000\n"
    "s_mov_b32 s44, s20\n"
    "s_and_b32 s45, s21, 0xffff\n"
    "s_mov_b32 s46, 7864320\n"
    "s_mov_b32 s47, 0x20000\n"
    "s_add_u32 m0, s80, 32768\n"
    "s_add_u32 s72, s64, s23\n"
    "s_waitcnt vmcnt(18)\n"
    "buffer_load_dwordx4 v10, s[28:31], s72 offen lds\n"
    "buffer_load_dwordx4 v11, s[28:31], s72 offen offset:1024 lds\n"
    "s_add_u32 m0, s81, 32768\n"
    "s_waitcnt vmcnt(18)\n"
    "buffer_load_dwordx4 v12, s[28:31], s72 offen lds\n"
    "buffer_load_dwordx4 v13, s[28:31], s72 offen offset:1024 lds\n"
    "s_add_u32 m0, s82, 32768\n"
    "s_waitcnt vmcnt(18)\n"
    "buffer_load_dwordx4 v14, s[28:31], s72 offen lds\n"
    "buffer_load_dwordx4 v15, s[28:31], s72 offen offset:1024 lds\n"
    "s_add_u32 m0, s83, 32768\n"
    "s_waitcnt vmcnt(18)\n"
    "buffer_load_dwordx4 v16, s[28:31], s72 offen lds\n"
    "buffer_load_dwordx4 v17, s[28:31], s72 offen offset:1024 lds\n"
    "s_add_u32 m0, s84, 49152\n"
    "s_mul_i32 s72, s23, 6144\n"
    "s_add_u32 s72, s72, s67\n"
    "s_waitcnt vmcnt(19)\n"
    "buffer_load_dwordx4 v26, s[40:43], s72 offen lds\n"
    "s_add_u32 m0, s85, 49152\n"
    "s_waitcnt vmcnt(19)\n"
    "buffer_load_dwordx4 v27, s[40:43], s72 offen lds\n"
    "s_add_u32 m0, s86, 49152\n"
    "s_waitcnt vmcnt(19)\n"
    "buffer_load_dwordx4 v28, s[40:43], s72 offen lds\n"
    "s_add_u32 m0, s87, 49152\n"
    "s_waitcnt vmcnt(19)\n"
    "buffer_load_dwordx4 v29, s[40:43], s72 offen lds\n"
    "s_add_u32 m0, s84, 57344\n"
    "s_mul_i32 s72, s23, 6144\n"
    "s_add_u32 s72, s72, s67\n"
    "s_add_u32 s72, s72, 128\n"
    "s_waitcnt vmcnt(19)\n"
    "buffer_load_dwordx4 v26, s[40:43], s72 offen lds\n"
    "s_add_u32 m0, s85, 57344\n"
    "s_waitcnt vmcnt(19)\n"
    "buffer_load_dwordx4 v27, s[40:43], s72 offen lds\n"
    "s_add_u32 m0, s86, 57344\n"
    "s_waitcnt vmcnt(19)\n"
    "buffer_load_dwordx4 v28, s[40:43], s72 offen lds\n"
    "s_add_u32 m0, s87, 57344\n"
    "s_waitcnt vmcnt(19)\n"
    "buffer_load_dwordx4 v29, s[40:43], s72 offen lds\n"
    "s_add_u32 m0, s84, 65536\n"
    "s_mul_i32 s72, s23, 6144\n"
    "s_add_u32 s72, s72, s67\n"
    "s_add_u32 s72, s72, 256\n"
    "s_waitcnt vmcnt(19)\n"
    "buffer_load_dwordx4 v26, s[40:43], s72 offen lds\n"
    "s_add_u32 m0, s85, 65536\n"
    "s_waitcnt vmcnt(19)\n"
    "buffer_load_dwordx4 v27, s[40:43], s72 offen lds\n"
    "s_add_u32 m0, s86, 65536\n"
    "s_waitcnt vmcnt(19)\n"
    "buffer_load_dwordx4 v28, s[40:43], s72 offen lds\n"
    "s_add_u32 m0, s87, 65536\n"
    "s_waitcnt vmcnt(19)\n"
    "buffer_load_dwordx4 v29, s[40:43], s72 offen lds\n"
    "s_mov_b32 m0, s5\n"
    "s_nop 0\n"
    "buffer_load_dword off, s[12:15], 0 offset:8 lds\n"
    "s_add_u32 m0, s80, 81920\n"
    "s_add_u32 s72, s64, s48\n"
    "s_waitcnt vmcnt(18)\n"
    "buffer_load_dwordx4 v10, s[28:31], s72 offen lds\n"
    "buffer_load_dwordx4 v11, s[28:31], s72 offen offset:1024 lds\n"
    "s_add_u32 m0, s81, 81920\n"
    "s_waitcnt vmcnt(18)\n"
    "buffer_load_dwordx4 v12, s[28:31], s72 offen lds\n"
    "buffer_load_dwordx4 v13, s[28:31], s72 offen offset:1024 lds\n"
    "s_add_u32 m0, s82, 81920\n"
    "s_waitcnt vmcnt(18)\n"
    "buffer_load_dwordx4 v14, s[28:31], s72 offen lds\n"
    "buffer_load_dwordx4 v15, s[28:31], s72 offen offset:1024 lds\n"
    "s_add_u32 m0, s83, 81920\n"
    "s_waitcnt vmcnt(18)\n"
    "buffer_load_dwordx4 v16, s[28:31], s72 offen lds\n"
    "buffer_load_dwordx4 v17, s[28:31], s72 offen offset:1024 lds\n"
    "s_add_u32 m0, s84, 73728\n"
    "s_mul_i32 s72, s48, 6144\n"
    "s_add_u32 s72, s72, s67\n"
    "s_waitcnt vmcnt(19)\n"
    "buffer_load_dwordx4 v26, s[40:43], s72 offen lds\n"
    "s_add_u32 m0, s85, 73728\n"
    "s_waitcnt vmcnt(19)\n"
    "buffer_load_dwordx4 v27, s[40:43], s72 offen lds\n"
    "s_add_u32 m0, s86, 73728\n"
    "s_waitcnt vmcnt(19)\n"
    "buffer_load_dwordx4 v28, s[40:43], s72 offen lds\n"
    "s_add_u32 m0, s87, 73728\n"
    "s_waitcnt vmcnt(19)\n"
    "buffer_load_dwordx4 v29, s[40:43], s72 offen lds\n"
    "s_add_u32 m0, s84, 98304\n"
    "s_mul_i32 s72, s48, 6144\n"
    "s_add_u32 s72, s72, s67\n"
    "s_add_u32 s72, s72, 128\n"
    "s_waitcnt vmcnt(19)\n"
    "buffer_load_dwordx4 v26, s[40:43], s72 offen lds\n"
    "s_add_u32 m0, s85, 98304\n"
    "s_waitcnt vmcnt(19)\n"
    "buffer_load_dwordx4 v27, s[40:43], s72 offen lds\n"
    "s_add_u32 m0, s86, 98304\n"
    "s_waitcnt vmcnt(19)\n"
    "buffer_load_dwordx4 v28, s[40:43], s72 offen lds\n"
    "s_add_u32 m0, s87, 98304\n"
    "s_waitcnt vmcnt(19)\n"
    "buffer_load_dwordx4 v29, s[40:43], s72 offen lds\n"
    "s_add_u32 m0, s84, 106496\n"
    "s_mul_i32 s72, s48, 6144\n"
    "s_add_u32 s72, s72, s67\n"
    "s_add_u32 s72, s72, 256\n"
    "s_waitcnt vmcnt(19)\n"
    "buffer_load_dwordx4 v26, s[40:43], s72 offen lds\n"
    "s_add_u32 m0, s85, 106496\n"
    "s_waitcnt vmcnt(19)\n"
    "buffer_load_dwordx4 v27, s[40:43], s72 offen lds\n"
    "s_add_u32 m0, s86, 106496\n"
    "s_waitcnt vmcnt(19)\n"
    "buffer_load_dwordx4 v28, s[40:43], s72 offen lds\n"
    "s_add_u32 m0, s87, 106496\n"
    "s_waitcnt vmcnt(19)\n"
    "buffer_load_dwordx4 v29, s[40:43], s72 offen lds\n"
    "s_mov_b32 m0, s5\n"
    "s_nop 0\n"
    "buffer_load_dword off, s[12:15], 0 offset:12 lds\n"
    "s_add_u32 m0, s80, 114688\n"
    "s_add_u32 s72, s65, 0\n"
    "s_waitcnt vmcnt(18)\n"
    "buffer_load_dwordx4 v18, s[76:79], s72 offen lds\n"
    "buffer_load_dwordx4 v19, s[76:79], s72 offen offset:1024 lds\n"
    "s_add_u32 m0, s81, 114688\n"
    "s_waitcnt vmcnt(18)\n"
    "buffer_load_dwordx4 v20, s[76:79], s72 offen lds\n"
    "buffer_load_dwordx4 v21, s[76:79], s72 offen offset:1024 lds\n"
    "s_add_u32 m0, s82, 114688\n"
    "s_waitcnt vmcnt(18)\n"
    "buffer_load_dwordx4 v22, s[76:79], s72 offen lds\n"
    "buffer_load_dwordx4 v23, s[76:79], s72 offen offset:1024 lds\n"
    "s_add_u32 m0, s83, 114688\n"
    "s_waitcnt vmcnt(18)\n"
    "buffer_load_dwordx4 v24, s[76:79], s72 offen lds\n"
    "buffer_load_dwordx4 v25, s[76:79], s72 offen offset:1024 lds\n"
    "s_add_u32 m0, s80, 131072\n"
    "s_add_u32 s72, s64, s49\n"
    "s_waitcnt vmcnt(18)\n"
    "buffer_load_dwordx4 v10, s[28:31], s72 offen lds\n"
    "buffer_load_dwordx4 v11, s[28:31], s72 offen offset:1024 lds\n"
    "s_add_u32 m0, s81, 131072\n"
    "s_waitcnt vmcnt(18)\n"
    "buffer_load_dwordx4 v12, s[28:31], s72 offen lds\n"
    "buffer_load_dwordx4 v13, s[28:31], s72 offen offset:1024 lds\n"
    "s_add_u32 m0, s82, 131072\n"
    "s_waitcnt vmcnt(18)\n"
    "buffer_load_dwordx4 v14, s[28:31], s72 offen lds\n"
    "buffer_load_dwordx4 v15, s[28:31], s72 offen offset:1024 lds\n"
    "s_add_u32 m0, s83, 131072\n"
    "s_waitcnt vmcnt(18)\n"
    "buffer_load_dwordx4 v16, s[28:31], s72 offen lds\n"
    "buffer_load_dwordx4 v17, s[28:31], s72 offen offset:1024 lds\n"
    "v_cmp_gt_u32 vcc, 0xb0000001, v47\n"
    "s_cbranch_vccz LF1_%=\n"
    "LS1_%=:\n"
    "ds_read_b32 v47, v46\n"
    "s_waitcnt lgkmcnt(0)\n"
    "s_sub_u32 s95, s95, 1\n"
    "s_cbranch_scc1 LE%=\n"
    "v_cmp_gt_u32 vcc, 0xb0000001, v47\n"
    "s_cbranch_vccnz LS1_%=\n"
    "LF1_%=:\n"
    "s_add_u32 m0, s84, 0\n"
    "s_mul_i32 s72, s49, 6144\n"
    "s_add_u32 s72, s72, s67\n"
    "s_waitcnt vmcnt(19)\n"
    "buffer_load_dwordx4 v26, s[40:43], s72 offen lds\n"
    "s_add_u32 m0, s85, 0\n"
    "s_waitcnt vmcnt(19)\n"
    "buffer_load_dwordx4 v27, s[40:43], s72 offen lds\n"
    "s_add_u32 m0, s86, 0\n"
    "s_waitcnt vmcnt(19)\n"
    "buffer_load_dwordx4 v28, s[40:43], s72 offen lds\n"
    "s_add_u32 m0, s87, 0\n"
    "s_waitcnt vmcnt(19)\n"
    "buffer_load_dwordx4 v29, s[40:43], s72 offen lds\n"
    "s_add_u32 m0, s84, 8192\n"
    "s_mul_i32 s72, s49, 6144\n"
    "s_add_u32 s72, s72, s67\n"
    "s_add_u32 s72, s72, 128\n"
    "s_waitcnt vmcnt(19)\n"
    "buffer_load_dwordx4 v26, s[40:43], s72 offen lds\n"
    "s_add_u32 m0, s85, 8192\n"
    "s_waitcnt vmcnt(19)\n"
    "buffer_load_dwordx4 v27, s[40:43], s72 offen lds\n"
    "s_add_u32 m0, s86, 8192\n"
    "s_waitcnt vmcnt(19)\n"
    "buffer_load_dwordx4 v28, s[40:43], s72 offen lds\n"
    "s_add_u32 m0, s87, 8192\n"
    "s_waitcnt vmcnt(19)\n"
    "buffer_load_dwordx4 v29, s[40:43], s72 offen lds\n"
    "v_cmp_gt_u32 vcc, 0xb0000002, v47\n"
    "s_cbranch_vccz LF2_%=\n"
    "LS2_%=:\n"
    "ds_read_b32 v47, v46\n"
    "s_waitcnt lgkmcnt(0)\n"
    "s_sub_u32 s95, s95, 1\n"
    "s_cbranch_scc1 LE%=\n"
    "v_cmp_gt_u32 vcc, 0xb0000002, v47\n"
    "s_cbranch_vccnz LS2_%=\n"
    "LF2_%=:\n"
    "s_add_u32 m0, s84, 147456\n"
    "s_mul_i32 s72, s49, 6144\n"
    "s_add_u32 s72, s72, s67\n"
    "s_add_u32 s72, s72, 256\n"
    "s_waitcnt vmcnt(19)\n"
    "buffer_load_dwordx4 v26, s[40:43], s72 offen lds\n"
    "s_add_u32 m0, s85, 147456\n"
    "s_waitcnt vmcnt(19)\n"
    "buffer_load_dwordx4 v27, s[40:43], s72 offen lds\n"
    "s_add_u32 m0, s86, 147456\n"
    "s_waitcnt vmcnt(19)\n"
    "buffer_load_dwordx4 v28, s[40:43], s72 offen lds\n"
    "s_add_u32 m0, s87, 147456\n"
    "s_waitcnt vmcnt(19)\n"
    "buffer_load_dwordx4 v29, s[40:43], s72 offen lds\n"
    "s_mov_b32 m0, s5\n"
    "s_nop 0\n"
    "buffer_load_dword off, s[12:15], 0 offset:16 lds\n"
    "s_add_u32 m0, s80, 16384\n"
    "s_add_u32 s72, s64, s50\n"
    "s_waitcnt vmcnt(18)\n"
    "buffer_load_dwordx4 v10, s[28:31], s72 offen lds\n"
    "buffer_load_dwordx4 v11, s[28:31], s72 offen offset:1024 lds\n"
    "s_add_u32 m0, s81, 16384\n"
    "s_waitcnt vmcnt(18)\n"
    "buffer_load_dwordx4 v12, s[28:31], s72 offen lds\n"
    "buffer_load_dwordx4 v13, s[28:31], s72 offen offset:1024 lds\n"
    "s_add_u32 m0, s82, 16384\n"
    "s_waitcnt vmcnt(18)\n"
    "buffer_load_dwordx4 v14, s[28:31], s72 offen lds\n"
    "buffer_load_dwordx4 v15, s[28:31], s72 offen offset:1024 lds\n"
    "s_add_u32 m0, s83, 16384\n"
    "s_waitcnt vmcnt(18)\n"
    "buffer_load_dwordx4 v16, s[28:31], s72 offen lds\n"
    "buffer_load_dwordx4 v17, s[28:31], s72 offen offset:1024 lds\n"
    "v_cmp_gt_u32 vcc, 0xb0000003, v47\n"
    "s_cbranch_vccz LF3_%=\n"
    "LS3_%=:\n"
    "ds_read_b32 v47, v46\n"
    "s_waitcnt lgkmcnt(0)\n"
    "s_sub_u32 s95, s95, 1\n"
    "s_cbranch_scc1 LE%=\n"
    "v_cmp_gt_u32 vcc, 0xb0000003, v47\n"
    "s_cbranch_vccnz LS3_%=\n"
    "LF3_%=:\n"
    "s_add_u32 m0, s84, 32768\n"
    "s_mul_i32 s72, s50, 6144\n"
    "s_add_u32 s72, s72, s67\n"
    "s_waitcnt vmcnt(19)\n"
    "buffer_load_dwordx4 v26, s[40:43], s72 offen lds\n"
    "s_add_u32 m0, s85, 32768\n"
    "s_waitcnt vmcnt(19)\n"
    "buffer_load_dwordx4 v27, s[40:43], s72 offen lds\n"
    "s_add_u32 m0, s86, 32768\n"
    "s_waitcnt vmcnt(19)\n"
    "buffer_load_dwordx4 v28, s[40:43], s72 offen lds\n"
    "s_add_u32 m0, s87, 32768\n"
    "s_waitcnt vmcnt(19)\n"
    "buffer_load_dwordx4 v29, s[40:43], s72 offen lds\n"
    "s_add_u32 m0, s84, 40960\n"
    "s_mul_i32 s72, s50, 6144\n"
    "s_add_u32 s72, s72, s67\n"
    "s_add_u32 s72, s72, 128\n"
    "s_waitcnt vmcnt(19)\n"
    "buffer_load_dwordx4 v26, s[40:43], s72 offen lds\n"
    "s_add_u32 m0, s85, 40960\n"
    "s_waitcnt vmcnt(19)\n"
    "buffer_load_dwordx4 v27, s[40:43], s72 offen lds\n"
    "s_add_u32 m0, s86, 40960\n"
    "s_waitcnt vmcnt(19)\n"
    "buffer_load_dwordx4 v28, s[40:43], s72 offen lds\n"
    "s_add_u32 m0, s87, 40960\n"
    "s_waitcnt vmcnt(19)\n"
    "buffer_load_dwordx4 v29, s[40:43], s72 offen lds\n"
    "v_cmp_gt_u32 vcc, 0xb0000004, v47\n"
    "s_cbranch_vccz LF4_%=\n"
    "LS4_%=:\n"
    "ds_read_b32 v47, v46\n"
    "s_waitcnt lgkmcnt(0)\n"
    "s_sub_u32 s95, s95, 1\n"
    "s_cbranch_scc1 LE%=\n"
    "v_cmp_gt_u32 vcc, 0xb0000004, v47\n"
    "s_cbranch_vccnz LS4_%=\n"
    "LF4_%=:\n"
    "s_add_u32 m0, s84, 65536\n"
    "s_mul_i32 s72, s50, 6144\n"
    "s_add_u32 s72, s72, s67\n"
    "s_add_u32 s72, s72, 256\n"
    "s_waitcnt vmcnt(19)\n"
    "buffer_load_dwordx4 v26, s[40:43], s72 offen lds\n"
    "s_add_u32 m0, s85, 65536\n"
    "s_waitcnt vmcnt(19)\n"
    "buffer_load_dwordx4 v27, s[40:43], s72 offen lds\n"
    "s_add_u32 m0, s86, 65536\n"
    "s_waitcnt vmcnt(19)\n"
    "buffer_load_dwordx4 v28, s[40:43], s72 offen lds\n"
    "s_add_u32 m0, s87, 65536\n"
    "s_waitcnt vmcnt(19)\n"
    "buffer_load_dwordx4 v29, s[40:43], s72 offen lds\n"
    "s_mov_b32 m0, s5\n"
    "s_nop 0\n"
    "buffer_load_dword off, s[12:15], 0 offset:20 lds\n"
    "s_add_u32 m0, s80, 49152\n"
    "s_add_u32 s72, s64, s51\n"
    "s_waitcnt vmcnt(18)\n"
    "buffer_load_dwordx4 v10, s[28:31], s72 offen lds\n"
    "buffer_load_dwordx4 v11, s[28:31], s72 offen offset:1024 lds\n"
    "s_add_u32 m0, s81, 49152\n"
    "s_waitcnt vmcnt(18)\n"
    "buffer_load_dwordx4 v12, s[28:31], s72 offen lds\n"
    "buffer_load_dwordx4 v13, s[28:31], s72 offen offset:1024 lds\n"
    "s_add_u32 m0, s82, 49152\n"
    "s_waitcnt vmcnt(18)\n"
    "buffer_load_dwordx4 v14, s[28:31], s72 offen lds\n"
    "buffer_load_dwordx4 v15, s[28:31], s72 offen offset:1024 lds\n"
    "s_add_u32 m0, s83, 49152\n"
    "s_waitcnt vmcnt(18)\n"
    "buffer_load_dwordx4 v16, s[28:31], s72 offen lds\n"
    "buffer_load_dwordx4 v17, s[28:31], s72 offen offset:1024 lds\n"
    "v_cmp_gt_u32 vcc, 0xb0000005, v47\n"
    "s_cbranch_vccz LF5_%=\n"
    "LS5_%=:\n"
    "ds_read_b32 v47, v46\n"
    "s_waitcnt lgkmcnt(0)\n"
    "s_sub_u32 s95, s95, 1\n"
    "s_cbranch_scc1 LE%=\n"
    "v_cmp_gt_u32 vcc, 0xb0000005, v47\n"
    "s_cbranch_vccnz LS5_%=\n"
    "LF5_%=:\n"
    "s_add_u32 m0, s84, 81920\n"
    "s_mul_i32 s72, s51, 6144\n"
    "s_add_u32 s72, s72, s67\n"
    "s_waitcnt vmcnt(19)\n"
    "buffer_load_dwordx4 v26, s[40:43], s72 offen lds\n"
    "s_add_u32 m0, s85, 81920\n"
    "s_waitcnt vmcnt(19)\n"
    "buffer_load_dwordx4 v27, s[40:43], s72 offen lds\n"
    "s_add_u32 m0, s86, 81920\n"
    "s_waitcnt vmcnt(19)\n"
    "buffer_load_dwordx4 v28, s[40:43], s72 offen lds\n"
    "s_add_u32 m0, s87, 81920\n"
    "s_waitcnt vmcnt(19)\n"
    "buffer_load_dwordx4 v29, s[40:43], s72 offen lds\n"
    "s_add_u32 m0, s84, 90112\n"
    "s_mul_i32 s72, s51, 6144\n"
    "s_add_u32 s72, s72, s67\n"
    "s_add_u32 s72, s72, 128\n"
    "s_waitcnt vmcnt(19)\n"
    "buffer_load_dwordx4 v26, s[40:43], s72 offen lds\n"
    "s_add_u32 m0, s85, 90112\n"
    "s_waitcnt vmcnt(19)\n"
    "buffer_load_dwordx4 v27, s[40:43], s72 offen lds\n"
    "s_add_u32 m0, s86, 90112\n"
    "s_waitcnt vmcnt(19)\n"
    "buffer_load_dwordx4 v28, s[40:43], s72 offen lds\n"
    "s_add_u32 m0, s87, 90112\n"
    "s_waitcnt vmcnt(19)\n"
    "buffer_load_dwordx4 v29, s[40:43], s72 offen lds\n"
    "v_cmp_gt_u32 vcc, 0xb0000006, v47\n"
    "s_cbranch_vccz LF6_%=\n"
    "LS6_%=:\n"
    "ds_read_b32 v47, v46\n"
    "s_waitcnt lgkmcnt(0)\n"
    "s_sub_u32 s95, s95, 1\n"
    "s_cbranch_scc1 LE%=\n"
    "v_cmp_gt_u32 vcc, 0xb0000006, v47\n"
    "s_cbranch_vccnz LS6_%=\n"
    "LF6_%=:\n"
    "s_add_u32 m0, s84, 73728\n"
    "s_mul_i32 s72, s51, 6144\n"
    "s_add_u32 s72, s72, s67\n"
    "s_add_u32 s72, s72, 256\n"
    "s_waitcnt vmcnt(19)\n"
    "buffer_load_dwordx4 v26, s[40:43], s72 offen lds\n"
    "s_add_u32 m0, s85, 73728\n"
    "s_waitcnt vmcnt(19)\n"
    "buffer_load_dwordx4 v27, s[40:43], s72 offen lds\n"
    "s_add_u32 m0, s86, 73728\n"
    "s_waitcnt vmcnt(19)\n"
    "buffer_load_dwordx4 v28, s[40:43], s72 offen lds\n"
    "s_add_u32 m0, s87, 73728\n"
    "s_waitcnt vmcnt(19)\n"
    "buffer_load_dwordx4 v29, s[40:43], s72 offen lds\n"
    "s_mov_b32 m0, s5\n"
    "s_nop 0\n"
    "buffer_load_dword off, s[12:15], 0 offset:24 lds\n"
    "s_add_u32 m0, s80, 98304\n"
    "s_add_u32 s72, s64, s52\n"
    "s_waitcnt vmcnt(18)\n"
    "buffer_load_dwordx4 v10, s[28:31], s72 offen lds\n"
    "buffer_load_dwordx4 v11, s[28:31], s72 offen offset:1024 lds\n"
    "s_add_u32 m0, s81, 98304\n"
    "s_waitcnt vmcnt(18)\n"
    "buffer_load_dwordx4 v12, s[28:31], s72 offen lds\n"
    "buffer_load_dwordx4 v13, s[28:31], s72 offen offset:1024 lds\n"
    "s_add_u32 m0, s82, 98304\n"
    "s_waitcnt vmcnt(18)\n"
    "buffer_load_dwordx4 v14, s[28:31], s72 offen lds\n"
    "buffer_load_dwordx4 v15, s[28:31], s72 offen offset:1024 lds\n"
    "s_add_u32 m0, s83, 98304\n"
    "s_waitcnt vmcnt(18)\n"
    "buffer_load_dwordx4 v16, s[28:31], s72 offen lds\n"
    "buffer_load_dwordx4 v17, s[28:31], s72 offen offset:1024 lds\n"
    "v_cmp_gt_u32 vcc, 0xb0000007, v47\n"
    "s_cbranch_vccz LF7_%=\n"
    "LS7_%=:\n"
    "ds_read_b32 v47, v46\n"
    "s_waitcnt lgkmcnt(0)\n"
    "s_sub_u32 s95, s95, 1\n"
    "s_cbranch_scc1 LE%=\n"
    "v_cmp_gt_u32 vcc, 0xb0000007, v47\n"
    "s_cbranch_vccnz LS7_%=\n"
    "LF7_%=:\n"
    "s_add_u32 m0, s84, 131072\n"
    "s_mul_i32 s72, s52, 6144\n"
    "s_add_u32 s72, s72, s67\n"
    "s_waitcnt vmcnt(19)\n"
    "buffer_load_dwordx4 v26, s[40:43], s72 offen lds\n"
    "s_add_u32 m0, s85, 131072\n"
    "s_waitcnt vmcnt(19)\n"
    "buffer_load_dwordx4 v27, s[40:43], s72 offen lds\n"
    "s_add_u32 m0, s86, 131072\n"
    "s_waitcnt vmcnt(19)\n"
    "buffer_load_dwordx4 v28, s[40:43], s72 offen lds\n"
    "s_add_u32 m0, s87, 131072\n"
    "s_waitcnt vmcnt(19)\n"
    "buffer_load_dwordx4 v29, s[40:43], s72 offen lds\n"
    "s_add_u32 m0, s84, 139264\n"
    "s_mul_i32 s72, s52, 6144\n"
    "s_add_u32 s72, s72, s67\n"
    "s_add_u32 s72, s72, 128\n"
    "s_waitcnt vmcnt(19)\n"
    "buffer_load_dwordx4 v26, s[40:43], s72 offen lds\n"
    "s_add_u32 m0, s85, 139264\n"
    "s_waitcnt vmcnt(19)\n"
    "buffer_load_dwordx4 v27, s[40:43], s72 offen lds\n"
    "s_add_u32 m0, s86, 139264\n"
    "s_waitcnt vmcnt(19)\n"
    "buffer_load_dwordx4 v28, s[40:43], s72 offen lds\n"
    "s_add_u32 m0, s87, 139264\n"
    "s_waitcnt vmcnt(19)\n"
    "buffer_load_dwordx4 v29, s[40:43], s72 offen lds\n"
    "v_cmp_gt_u32 vcc, 0xb0000008, v47\n"
    "s_cbranch_vccz LF8_%=\n"
    "LS8_%=:\n"
    "ds_read_b32 v47, v46\n"
    "s_waitcnt lgkmcnt(0)\n"
    "s_sub_u32 s95, s95, 1\n"
    "s_cbranch_scc1 LE%=\n"
    "v_cmp_gt_u32 vcc, 0xb0000008, v47\n"
    "s_cbranch_vccnz LS8_%=\n"
    "LF8_%=:\n"
    "s_add_u32 m0, s84, 147456\n"
    "s_mul_i32 s72, s52, 6144\n"
    "s_add_u32 s72, s72, s67\n"
    "s_add_u32 s72, s72, 256\n"
    "s_waitcnt vmcnt(19)\n"
    "buffer_load_dwordx4 v26, s[40:43], s72 offen lds\n"
    "s_add_u32 m0, s85, 147456\n"
    "s_waitcnt vmcnt(19)\n"
    "buffer_load_dwordx4 v27, s[40:43], s72 offen lds\n"
    "s_add_u32 m0, s86, 147456\n"
    "s_waitcnt vmcnt(19)\n"
    "buffer_load_dwordx4 v28, s[40:43], s72 offen lds\n"
    "s_add_u32 m0, s87, 147456\n"
    "s_waitcnt vmcnt(19)\n"
    "buffer_load_dwordx4 v29, s[40:43], s72 offen lds\n"
    "s_mov_b32 m0, s5\n"
    "s_nop 0\n"
    "buffer_load_dword off, s[12:15], 0 offset:28 lds\n"
    "s_add_u32 m0, s80, 0\n"
    "s_add_u32 s72, s65, 128\n"
    "s_waitcnt vmcnt(18)\n"
    "buffer_load_dwordx4 v18, s[76:79], s72 offen lds\n"
    "buffer_load_dwordx4 v19, s[76:79], s72 offen offset:1024 lds\n"
    "s_add_u32 m0, s81, 0\n"
    "s_waitcnt vmcnt(18)\n"
    "buffer_load_dwordx4 v20, s[76:79], s72 offen lds\n"
    "buffer_load_dwordx4 v21, s[76:79], s72 offen offset:1024 lds\n"
    "s_add_u32 m0, s82, 0\n"
    "s_waitcnt vmcnt(18)\n"
    "buffer_load_dwordx4 v22, s[76:79], s72 offen lds\n"
    "buffer_load_dwordx4 v23, s[76:79], s72 offen offset:1024 lds\n"
    "s_add_u32 m0, s83, 0\n"
    "s_waitcnt vmcnt(18)\n"
    "buffer_load_dwordx4 v24, s[76:79], s72 offen lds\n"
    "buffer_load_dwordx4 v25, s[76:79], s72 offen offset:1024 lds\n"
    "s_add_u32 m0, s80, 114688\n"
    "s_add_u32 s72, s64, s53\n"
    "s_waitcnt vmcnt(18)\n"
    "buffer_load_dwordx4 v10, s[28:31], s72 offen lds\n"
    "buffer_load_dwordx4 v11, s[28:31], s72 offen offset:1024 lds\n"
    "s_add_u32 m0, s81, 114688\n"
    "s_waitcnt vmcnt(18)\n"
    "buffer_load_dwordx4 v12, s[28:31], s72 offen lds\n"
    "buffer_load_dwordx4 v13, s[28:31], s72 offen offset:1024 lds\n"
    "s_add_u32 m0, s82, 114688\n"
    "s_waitcnt vmcnt(18)\n"
    "buffer_load_dwordx4 v14, s[28:31], s72 offen lds\n"
    "buffer_load_dwordx4 v15, s[28:31], s72 offen offset:1024 lds\n"
    "s_add_u32 m0, s83, 114688\n"
    "s_waitcnt vmcnt(18)\n"
    "buffer_load_dwordx4 v16, s[28:31], s72 offen lds\n"
    "buffer_load_dwordx4 v17, s[28:31], s72 offen offset:1024 lds\n"
    "v_cmp_gt_u32 vcc, 0xb0000009, v47\n"
    "s_cbranch_vccz LF9_%=\n"
    "LS9_%=:\n"
    "ds_read_b32 v47, v46\n"
    "s_waitcnt lgkmcnt(0)\n"
    "s_sub_u32 s95, s95, 1\n"
    "s_cbranch_scc1 LE%=\n"
    "v_cmp_gt_u32 vcc, 0xb0000009, v47\n"
    "s_cbranch_vccnz LS9_%=\n"
    "LF9_%=:\n"
    "s_add_u32 m0, s84, 16384\n"
    "s_mul_i32 s72, s53, 6144\n"
    "s_add_u32 s72, s72, s67\n"
    "s_waitcnt vmcnt(19)\n"
    "buffer_load_dwordx4 v26, s[40:43], s72 offen lds\n"
    "s_add_u32 m0, s85, 16384\n"
    "s_waitcnt vmcnt(19)\n"
    "buffer_load_dwordx4 v27, s[40:43], s72 offen lds\n"
    "s_add_u32 m0, s86, 16384\n"
    "s_waitcnt vmcnt(19)\n"
    "buffer_load_dwordx4 v28, s[40:43], s72 offen lds\n"
    "s_add_u32 m0, s87, 16384\n"
    "s_waitcnt vmcnt(19)\n"
    "buffer_load_dwordx4 v29, s[40:43], s72 offen lds\n"
    "s_add_u32 m0, s84, 24576\n"
    "s_mul_i32 s72, s53, 6144\n"
    "s_add_u32 s72, s72, s67\n"
    "s_add_u32 s72, s72, 128\n"
    "s_waitcnt vmcnt(19)\n"
    "buffer_load_dwordx4 v26, s[40:43], s72 offen lds\n"
    "s_add_u32 m0, s85, 24576\n"
    "s_waitcnt vmcnt(19)\n"
    "buffer_load_dwordx4 v27, s[40:43], s72 offen lds\n"
    "s_add_u32 m0, s86, 24576\n"
    "s_waitcnt vmcnt(19)\n"
    "buffer_load_dwordx4 v28, s[40:43], s72 offen lds\n"
    "s_add_u32 m0, s87, 24576\n"
    "s_waitcnt vmcnt(19)\n"
    "buffer_load_dwordx4 v29, s[40:43], s72 offen lds\n"
    "v_cmp_gt_u32 vcc, 0xb000000a, v47\n"
    "s_cbranch_vccz LF10_%=\n"
    "LS10_%=:\n"
    "ds_read_b32 v47, v46\n"
    "s_waitcnt lgkmcnt(0)\n"
    "s_sub_u32 s95, s95, 1\n"
    "s_cbranch_scc1 LE%=\n"
    "v_cmp_gt_u32 vcc, 0xb000000a, v47\n"
    "s_cbranch_vccnz LS10_%=\n"
    "LF10_%=:\n"
    "s_add_u32 m0, s84, 65536\n"
    "s_mul_i32 s72, s53, 6144\n"
    "s_add_u32 s72, s72, s67\n"
    "s_add_u32 s72, s72, 256\n"
    "s_waitcnt vmcnt(19)\n"
    "buffer_load_dwordx4 v26, s[40:43], s72 offen lds\n"
    "s_add_u32 m0, s85, 65536\n"
    "s_waitcnt vmcnt(19)\n"
    "buffer_load_dwordx4 v27, s[40:43], s72 offen lds\n"
    "s_add_u32 m0, s86, 65536\n"
    "s_waitcnt vmcnt(19)\n"
    "buffer_load_dwordx4 v28, s[40:43], s72 offen lds\n"
    "s_add_u32 m0, s87, 65536\n"
    "s_waitcnt vmcnt(19)\n"
    "buffer_load_dwordx4 v29, s[40:43], s72 offen lds\n"
    "s_mov_b32 m0, s5\n"
    "s_nop 0\n"
    "buffer_load_dword off, s[12:15], 0 offset:32 lds\n"
    "s_add_u32 m0, s80, 32768\n"
    "s_add_u32 s72, s64, s54\n"
    "s_waitcnt vmcnt(18)\n"
    "buffer_load_dwordx4 v10, s[28:31], s72 offen lds\n"
    "buffer_load_dwordx4 v11, s[28:31], s72 offen offset:1024 lds\n"
    "s_add_u32 m0, s81, 32768\n"
    "s_waitcnt vmcnt(18)\n"
    "buffer_load_dwordx4 v12, s[28:31], s72 offen lds\n"
    "buffer_load_dwordx4 v13, s[28:31], s72 offen offset:1024 lds\n"
    "s_add_u32 m0, s82, 32768\n"
    "s_waitcnt vmcnt(18)\n"
    "buffer_load_dwordx4 v14, s[28:31], s72 offen lds\n"
    "buffer_load_dwordx4 v15, s[28:31], s72 offen offset:1024 lds\n"
    "s_add_u32 m0, s83, 32768\n"
    "s_waitcnt vmcnt(18)\n"
    "buffer_load_dwordx4 v16, s[28:31], s72 offen lds\n"
    "buffer_load_dwordx4 v17, s[28:31], s72 offen offset:1024 lds\n"
    "v_cmp_gt_u32 vcc, 0xb000000b, v47\n"
    "s_cbranch_vccz LF11_%=\n"
    "LS11_%=:\n"
    "ds_read_b32 v47, v46\n"
    "s_waitcnt lgkmcnt(0)\n"
    "s_sub_u32 s95, s95, 1\n"
    "s_cbranch_scc1 LE%=\n"
    "v_cmp_gt_u32 vcc, 0xb000000b, v47\n"
    "s_cbranch_vccnz LS11_%=\n"
    "LF11_%=:\n"
    "s_add_u32 m0, s84, 49152\n"
    "s_mul_i32 s72, s54, 6144\n"
    "s_add_u32 s72, s72, s67\n"
    "s_waitcnt vmcnt(19)\n"
    "buffer_load_dwordx4 v26, s[40:43], s72 offen lds\n"
    "s_add_u32 m0, s85, 49152\n"
    "s_waitcnt vmcnt(19)\n"
    "buffer_load_dwordx4 v27, s[40:43], s72 offen lds\n"
    "s_add_u32 m0, s86, 49152\n"
    "s_waitcnt vmcnt(19)\n"
    "buffer_load_dwordx4 v28, s[40:43], s72 offen lds\n"
    "s_add_u32 m0, s87, 49152\n"
    "s_waitcnt vmcnt(19)\n"
    "buffer_load_dwordx4 v29, s[40:43], s72 offen lds\n"
    "s_add_u32 m0, s84, 57344\n"
    "s_mul_i32 s72, s54, 6144\n"
    "s_add_u32 s72, s72, s67\n"
    "s_add_u32 s72, s72, 128\n"
    "s_waitcnt vmcnt(19)\n"
    "buffer_load_dwordx4 v26, s[40:43], s72 offen lds\n"
    "s_add_u32 m0, s85, 57344\n"
    "s_waitcnt vmcnt(19)\n"
    "buffer_load_dwordx4 v27, s[40:43], s72 offen lds\n"
    "s_add_u32 m0, s86, 57344\n"
    "s_waitcnt vmcnt(19)\n"
    "buffer_load_dwordx4 v28, s[40:43], s72 offen lds\n"
    "s_add_u32 m0, s87, 57344\n"
    "s_waitcnt vmcnt(19)\n"
    "buffer_load_dwordx4 v29, s[40:43], s72 offen lds\n"
    "v_cmp_gt_u32 vcc, 0xb000000c, v47\n"
    "s_cbranch_vccz LF12_%=\n"
    "LS12_%=:\n"
    "ds_read_b32 v47, v46\n"
    "s_waitcnt lgkmcnt(0)\n"
    "s_sub_u32 s95, s95, 1\n"
    "s_cbranch_scc1 LE%=\n"
    "v_cmp_gt_u32 vcc, 0xb000000c, v47\n"
    "s_cbranch_vccnz LS12_%=\n"
    "LF12_%=:\n"
    "s_add_u32 m0, s84, 73728\n"
    "s_mul_i32 s72, s54, 6144\n"
    "s_add_u32 s72, s72, s67\n"
    "s_add_u32 s72, s72, 256\n"
    "s_waitcnt vmcnt(19)\n"
    "buffer_load_dwordx4 v26, s[40:43], s72 offen lds\n"
    "s_add_u32 m0, s85, 73728\n"
    "s_waitcnt vmcnt(19)\n"
    "buffer_load_dwordx4 v27, s[40:43], s72 offen lds\n"
    "s_add_u32 m0, s86, 73728\n"
    "s_waitcnt vmcnt(19)\n"
    "buffer_load_dwordx4 v28, s[40:43], s72 offen lds\n"
    "s_add_u32 m0, s87, 73728\n"
    "s_waitcnt vmcnt(19)\n"
    "buffer_load_dwordx4 v29, s[40:43], s72 offen lds\n"
    "s_mov_b32 m0, s5\n"
    "s_nop 0\n"
    "buffer_load_dword off, s[12:15], 0 offset:36 lds\n"
    "s_add_u32 m0, s80, 81920\n"
    "s_add_u32 s72, s64, s55\n"
    "s_waitcnt vmcnt(18)\n"
    "buffer_load_dwordx4 v10, s[28:31], s72 offen lds\n"
    "buffer_load_dwordx4 v11, s[28:31], s72 offen offset:1024 lds\n"
    "s_add_u32 m0, s81, 81920\n"
    "s_waitcnt vmcnt(18)\n"
    "buffer_load_dwordx4 v12, s[28:31], s72 offen lds\n"
    "buffer_load_dwordx4 v13, s[28:31], s72 offen offset:1024 lds\n"
    "s_add_u32 m0, s82, 81920\n"
    "s_waitcnt vmcnt(18)\n"
    "buffer_load_dwordx4 v14, s[28:31], s72 offen lds\n"
    "buffer_load_dwordx4 v15, s[28:31], s72 offen offset:1024 lds\n"
    "s_add_u32 m0, s83, 81920\n"
    "s_waitcnt vmcnt(18)\n"
    "buffer_load_dwordx4 v16, s[28:31], s72 offen lds\n"
    "buffer_load_dwordx4 v17, s[28:31], s72 offen offset:1024 lds\n"
    "v_cmp_gt_u32 vcc, 0xb000000d, v47\n"
    "s_cbranch_vccz LF13_%=\n"
    "LS13_%=:\n"
    "ds_read_b32 v47, v46\n"
    "s_waitcnt lgkmcnt(0)\n"
    "s_sub_u32 s95, s95, 1\n"
    "s_cbranch_scc1 LE%=\n"
    "v_cmp_gt_u32 vcc, 0xb000000d, v47\n"
    "s_cbranch_vccnz LS13_%=\n"
    "LF13_%=:\n"
    "s_add_u32 m0, s84, 98304\n"
    "s_mul_i32 s72, s55, 6144\n"
    "s_add_u32 s72, s72, s67\n"
    "s_waitcnt vmcnt(19)\n"
    "buffer_load_dwordx4 v26, s[40:43], s72 offen lds\n"
    "s_add_u32 m0, s85, 98304\n"
    "s_waitcnt vmcnt(19)\n"
    "buffer_load_dwordx4 v27, s[40:43], s72 offen lds\n"
    "s_add_u32 m0, s86, 98304\n"
    "s_waitcnt vmcnt(19)\n"
    "buffer_load_dwordx4 v28, s[40:43], s72 offen lds\n"
    "s_add_u32 m0, s87, 98304\n"
    "s_waitcnt vmcnt(19)\n"
    "buffer_load_dwordx4 v29, s[40:43], s72 offen lds\n"
    "s_add_u32 m0, s84, 106496\n"
    "s_mul_i32 s72, s55, 6144\n"
    "s_add_u32 s72, s72, s67\n"
    "s_add_u32 s72, s72, 128\n"
    "s_waitcnt vmcnt(19)\n"
    "buffer_load_dwordx4 v26, s[40:43], s72 offen lds\n"
    "s_add_u32 m0, s85, 106496\n"
    "s_waitcnt vmcnt(19)\n"
    "buffer_load_dwordx4 v27, s[40:43], s72 offen lds\n"
    "s_add_u32 m0, s86, 106496\n"
    "s_waitcnt vmcnt(19)\n"
    "buffer_load_dwordx4 v28, s[40:43], s72 offen lds\n"
    "s_add_u32 m0, s87, 106496\n"
    "s_waitcnt vmcnt(19)\n"
    "buffer_load_dwordx4 v29, s[40:43], s72 offen lds\n"
    "v_cmp_gt_u32 vcc, 0xb000000e, v47\n"
    "s_cbranch_vccz LF14_%=\n"
    "LS14_%=:\n"
    "ds_read_b32 v47, v46\n"
    "s_waitcnt lgkmcnt(0)\n"
    "s_sub_u32 s95, s95, 1\n"
    "s_cbranch_scc1 LE%=\n"
    "v_cmp_gt_u32 vcc, 0xb000000e, v47\n"
    "s_cbranch_vccnz LS14_%=\n"
    "LF14_%=:\n"
    "s_add_u32 m0, s84, 147456\n"
    "s_mul_i32 s72, s55, 6144\n"
    "s_add_u32 s72, s72, s67\n"
    "s_add_u32 s72, s72, 256\n"
    "s_waitcnt vmcnt(19)\n"
    "buffer_load_dwordx4 v26, s[40:43], s72 offen lds\n"
    "s_add_u32 m0, s85, 147456\n"
    "s_waitcnt vmcnt(19)\n"
    "buffer_load_dwordx4 v27, s[40:43], s72 offen lds\n"
    "s_add_u32 m0, s86, 147456\n"
    "s_waitcnt vmcnt(19)\n"
    "buffer_load_dwordx4 v28, s[40:43], s72 offen lds\n"
    "s_add_u32 m0, s87, 147456\n"
    "s_waitcnt vmcnt(19)\n"
    "buffer_load_dwordx4 v29, s[40:43], s72 offen lds\n"
    "s_add_u32 m0, s80, 131072\n"
    "s_add_u32 s72, s65, 256\n"
    "s_waitcnt vmcnt(18)\n"
    "buffer_load_dwordx4 v18, s[76:79], s72 offen lds\n"
    "buffer_load_dwordx4 v19, s[76:79], s72 offen offset:1024 lds\n"
    "s_add_u32 m0, s81, 131072\n"
    "s_waitcnt vmcnt(18)\n"
    "buffer_load_dwordx4 v20, s[76:79], s72 offen lds\n"
    "buffer_load_dwordx4 v21, s[76:79], s72 offen offset:1024 lds\n"
    "s_add_u32 m0, s82, 131072\n"
    "s_waitcnt vmcnt(18)\n"
    "buffer_load_dwordx4 v22, s[76:79], s72 offen lds\n"
    "buffer_load_dwordx4 v23, s[76:79], s72 offen offset:1024 lds\n"
    "s_add_u32 m0, s83, 131072\n"
    "s_waitcnt vmcnt(18)\n"
    "buffer_load_dwordx4 v24, s[76:79], s72 offen lds\n"
    "buffer_load_dwordx4 v25, s[76:79], s72 offen offset:1024 lds\n"
    "s_mov_b32 m0, s5\n"
    "s_nop 0\n"
    "buffer_load_dword off, s[12:15], 0 offset:40 lds\n"
    "v_cmp_gt_u32 vcc, 0xb000000f, v47\n"
    "s_cbranch_vccz LF15_%=\n"
    "LS15_%=:\n"
    "ds_read_b32 v47, v46\n"
    "s_waitcnt lgkmcnt(0)\n"
    "s_sub_u32 s95, s95, 1\n"
    "s_cbranch_scc1 LE%=\n"
    "v_cmp_gt_u32 vcc, 0xb000000f, v47\n"
    "s_cbranch_vccnz LS15_%=\n"
    "LF15_%=:\n"
    "s_add_u32 m0, s80, 114688\n"
    "s_add_u32 s72, s88, 0\n"
    "s_waitcnt vmcnt(18)\n"
    "buffer_load_dwordx4 v18, s[36:39], s72 offen lds\n"
    "buffer_load_dwordx4 v19, s[36:39], s72 offen offset:1024 lds\n"
    "s_add_u32 m0, s81, 114688\n"
    "s_waitcnt vmcnt(18)\n"
    "buffer_load_dwordx4 v20, s[36:39], s72 offen lds\n"
    "buffer_load_dwordx4 v21, s[36:39], s72 offen offset:1024 lds\n"
    "s_add_u32 m0, s82, 114688\n"
    "s_waitcnt vmcnt(18)\n"
    "buffer_load_dwordx4 v22, s[36:39], s72 offen lds\n"
    "buffer_load_dwordx4 v23, s[36:39], s72 offen offset:1024 lds\n"
    "s_add_u32 m0, s83, 114688\n"
    "s_waitcnt vmcnt(18)\n"
    "buffer_load_dwordx4 v24, s[36:39], s72 offen lds\n"
    "buffer_load_dwordx4 v25, s[36:39], s72 offen offset:1024 lds\n"
    "v_cmp_gt_u32 vcc, 0xb0000010, v47\n"
    "s_cbranch_vccz LF16_%=\n"
    "LS16_%=:\n"
    "ds_read_b32 v47, v46\n"
    "s_waitcnt lgkmcnt(0)\n"
    "s_sub_u32 s95, s95, 1\n"
    "s_cbranch_scc1 LE%=\n"
    "v_cmp_gt_u32 vcc, 0xb0000010, v47\n"
    "s_cbranch_vccnz LS16_%=\n"
    "LF16_%=:\n"
    "s_add_u32 m0, s84, 65536\n"
    "s_add_u32 s72, s92, s96\n"
    "s_waitcnt vmcnt(19)\n"
    "buffer_load_dwordx4 v30, s[44:47], s72 offen lds\n"
    "s_add_u32 m0, s85, 65536\n"
    "s_waitcnt vmcnt(19)\n"
    "buffer_load_dwordx4 v31, s[44:47], s72 offen lds\n"
    "s_add_u32 m0, s86, 65536\n"
    "s_waitcnt vmcnt(19)\n"
    "buffer_load_dwordx4 v32, s[44:47], s72 offen lds\n"
    "s_add_u32 m0, s87, 65536\n"
    "s_waitcnt vmcnt(19)\n"
    "buffer_load_dwordx4 v33, s[44:47], s72 offen lds\n"
    "s_mov_b32 m0, s5\n"
    "s_nop 0\n"
    "buffer_load_dword off, s[12:15], 0 offset:44 lds\n"
    "s_add_u32 m0, s80, 0\n"
    "s_add_u32 s72, s88, 128\n"
    "s_waitcnt vmcnt(18)\n"
    "buffer_load_dwordx4 v18, s[36:39], s72 offen lds\n"
    "buffer_load_dwordx4 v19, s[36:39], s72 offen offset:1024 lds\n"
    "s_add_u32 m0, s81, 0\n"
    "s_waitcnt vmcnt(18)\n"
    "buffer_load_dwordx4 v20, s[36:39], s72 offen lds\n"
    "buffer_load_dwordx4 v21, s[36:39], s72 offen offset:1024 lds\n"
    "s_add_u32 m0, s82, 0\n"
    "s_waitcnt vmcnt(18)\n"
    "buffer_load_dwordx4 v22, s[36:39], s72 offen lds\n"
    "buffer_load_dwordx4 v23, s[36:39], s72 offen offset:1024 lds\n"
    "s_add_u32 m0, s83, 0\n"
    "s_waitcnt vmcnt(18)\n"
    "buffer_load_dwordx4 v24, s[36:39], s72 offen lds\n"
    "buffer_load_dwordx4 v25, s[36:39], s72 offen offset:1024 lds\n"
    "s_add_u32 m0, s84, 16384\n"
    "s_add_u32 s72, s92, s96\n"
    "s_add_u32 s72, s72, 81920\n"
    "s_waitcnt vmcnt(19)\n"
    "buffer_load_dwordx4 v30, s[44:47], s72 offen lds\n"
    "s_add_u32 m0, s85, 16384\n"
    "s_waitcnt vmcnt(19)\n"
    "buffer_load_dwordx4 v31, s[44:47], s72 offen lds\n"
    "s_add_u32 m0, s86, 16384\n"
    "s_waitcnt vmcnt(19)\n"
    "buffer_load_dwordx4 v32, s[44:47], s72 offen lds\n"
    "s_add_u32 m0, s87, 16384\n"
    "s_waitcnt vmcnt(19)\n"
    "buffer_load_dwordx4 v33, s[44:47], s72 offen lds\n"
    "s_mov_b32 m0, s5\n"
    "s_nop 0\n"
    "buffer_load_dword off, s[12:15], 0 offset:48 lds\n"
    "v_cmp_gt_u32 vcc, 0xb0000011, v47\n"
    "s_cbranch_vccz LF17_%=\n"
    "LS17_%=:\n"
    "ds_read_b32 v47, v46\n"
    "s_waitcnt lgkmcnt(0)\n"
    "s_sub_u32 s95, s95, 1\n"
    "s_cbranch_scc1 LE%=\n"
    "v_cmp_gt_u32 vcc, 0xb0000011, v47\n"
    "s_cbranch_vccnz LS17_%=\n"
    "LF17_%=:\n"
    "s_add_u32 m0, s80, 32768\n"
    "s_add_u32 s72, s88, 256\n"
    "s_waitcnt vmcnt(18)\n"
    "buffer_load_dwordx4 v18, s[36:39], s72 offen lds\n"
    "buffer_load_dwordx4 v19, s[36:39], s72 offen offset:1024 lds\n"
    "s_add_u32 m0, s81, 32768\n"
    "s_waitcnt vmcnt(18)\n"
    "buffer_load_dwordx4 v20, s[36:39], s72 offen lds\n"
    "buffer_load_dwordx4 v21, s[36:39], s72 offen offset:1024 lds\n"
    "s_add_u32 m0, s82, 32768\n"
    "s_waitcnt vmcnt(18)\n"
    "buffer_load_dwordx4 v22, s[36:39], s72 offen lds\n"
    "buffer_load_dwordx4 v23, s[36:39], s72 offen offset:1024 lds\n"
    "s_add_u32 m0, s83, 32768\n"
    "s_waitcnt vmcnt(18)\n"
    "buffer_load_dwordx4 v24, s[36:39], s72 offen lds\n"
    "buffer_load_dwordx4 v25, s[36:39], s72 offen offset:1024 lds\n"
    "s_add_u32 m0, s84, 24576\n"
    "s_add_u32 s72, s92, s96\n"
    "s_add_u32 s72, s72, 163840\n"
    "s_waitcnt vmcnt(19)\n"
    "buffer_load_dwordx4 v30, s[44:47], s72 offen lds\n"
    "s_add_u32 m0, s85, 24576\n"
    "s_waitcnt vmcnt(19)\n"
    "buffer_load_dwordx4 v31, s[44:47], s72 offen lds\n"
    "s_add_u32 m0, s86, 24576\n"
    "s_waitcnt vmcnt(19)\n"
    "buffer_load_dwordx4 v32, s[44:47], s72 offen lds\n"
    "s_add_u32 m0, s87, 24576\n"
    "s_waitcnt vmcnt(19)\n"
    "buffer_load_dwordx4 v33, s[44:47], s72 offen lds\n"
    "s_mov_b32 m0, s5\n"
    "s_nop 0\n"
    "buffer_load_dword off, s[12:15], 0 offset:52 lds\n"
    "v_cmp_gt_u32 vcc, 0xb0000012, v47\n"
    "s_cbranch_vccz LF18_%=\n"
    "LS18_%=:\n"
    "ds_read_b32 v47, v46\n"
    "s_waitcnt lgkmcnt(0)\n"
    "s_sub_u32 s95, s95, 1\n"
    "s_cbranch_scc1 LE%=\n"
    "v_cmp_gt_u32 vcc, 0xb0000012, v47\n"
    "s_cbranch_vccnz LS18_%=\n"
    "LF18_%=:\n"
    "s_add_u32 m0, s84, 73728\n"
    "s_add_u32 s72, s92, s97\n"
    "s_waitcnt vmcnt(19)\n"
    "buffer_load_dwordx4 v30, s[44:47], s72 offen lds\n"
    "s_add_u32 m0, s85, 73728\n"
    "s_waitcnt vmcnt(19)\n"
    "buffer_load_dwordx4 v31, s[44:47], s72 offen lds\n"
    "s_add_u32 m0, s86, 73728\n"
    "s_waitcnt vmcnt(19)\n"
    "buffer_load_dwordx4 v32, s[44:47], s72 offen lds\n"
    "s_add_u32 m0, s87, 73728\n"
    "s_waitcnt vmcnt(19)\n"
    "buffer_load_dwordx4 v33, s[44:47], s72 offen lds\n"
    "s_add_u32 m0, s84, 49152\n"
    "s_add_u32 s72, s92, s97\n"
    "s_add_u32 s72, s72, 81920\n"
    "s_waitcnt vmcnt(19)\n"
    "buffer_load_dwordx4 v30, s[44:47], s72 offen lds\n"
    "s_add_u32 m0, s85, 49152\n"
    "s_waitcnt vmcnt(19)\n"
    "buffer_load_dwordx4 v31, s[44:47], s72 offen lds\n"
    "s_add_u32 m0, s86, 49152\n"
    "s_waitcnt vmcnt(19)\n"
    "buffer_load_dwordx4 v32, s[44:47], s72 offen lds\n"
    "s_add_u32 m0, s87, 49152\n"
    "s_waitcnt vmcnt(19)\n"
    "buffer_load_dwordx4 v33, s[44:47], s72 offen lds\n"
    "s_add_u32 m0, s84, 57344\n"
    "s_add_u32 s72, s92, s97\n"
    "s_add_u32 s72, s72, 163840\n"
    "s_waitcnt vmcnt(19)\n"
    "buffer_load_dwordx4 v30, s[44:47], s72 offen lds\n"
    "s_add_u32 m0, s85, 57344\n"
    "s_waitcnt vmcnt(19)\n"
    "buffer_load_dwordx4 v31, s[44:47], s72 offen lds\n"
    "s_add_u32 m0, s86, 57344\n"
    "s_waitcnt vmcnt(19)\n"
    "buffer_load_dwordx4 v32, s[44:47], s72 offen lds\n"
    "s_add_u32 m0, s87, 57344\n"
    "s_waitcnt vmcnt(19)\n"
    "buffer_load_dwordx4 v33, s[44:47], s72 offen lds\n"
    "s_mov_b32 m0, s5\n"
    "s_nop 0\n"
    "buffer_load_dword off, s[12:15], 0 offset:56 lds\n"
    "v_cmp_gt_u32 vcc, 0xb0000013, v47\n"
    "s_cbranch_vccz LF19_%=\n"
    "LS19_%=:\n"
    "ds_read_b32 v47, v46\n"
    "s_waitcnt lgkmcnt(0)\n"
    "s_sub_u32 s95, s95, 1\n"
    "s_cbranch_scc1 LE%=\n"
    "v_cmp_gt_u32 vcc, 0xb0000013, v47\n"
    "s_cbranch_vccnz LS19_%=\n"
    "LF19_%=:\n"
    "s_add_u32 m0, s80, 81920\n"
    "s_add_u32 s72, s89, 0\n"
    "s_waitcnt vmcnt(18)\n"
    "buffer_load_dwordx4 v18, s[36:39], s72 offen lds\n"
    "buffer_load_dwordx4 v19, s[36:39], s72 offen offset:1024 lds\n"
    "s_add_u32 m0, s81, 81920\n"
    "s_waitcnt vmcnt(18)\n"
    "buffer_load_dwordx4 v20, s[36:39], s72 offen lds\n"
    "buffer_load_dwordx4 v21, s[36:39], s72 offen offset:1024 lds\n"
    "s_add_u32 m0, s82, 81920\n"
    "s_waitcnt vmcnt(18)\n"
    "buffer_load_dwordx4 v22, s[36:39], s72 offen lds\n"
    "buffer_load_dwordx4 v23, s[36:39], s72 offen offset:1024 lds\n"
    "s_add_u32 m0, s83, 81920\n"
    "s_waitcnt vmcnt(18)\n"
    "buffer_load_dwordx4 v24, s[36:39], s72 offen lds\n"
    "buffer_load_dwordx4 v25, s[36:39], s72 offen offset:1024 lds\n"
    "s_mov_b32 m0, s5\n"
    "s_nop 0\n"
    "buffer_load_dword off, s[12:15], 0 offset:60 lds\n"
    "v_cmp_gt_u32 vcc, 0xb0000014, v47\n"
    "s_cbranch_vccz LF20_%=\n"
    "LS20_%=:\n"
    "ds_read_b32 v47, v46\n"
    "s_waitcnt lgkmcnt(0)\n"
    "s_sub_u32 s95, s95, 1\n"
    "s_cbranch_scc1 LE%=\n"
    "v_cmp_gt_u32 vcc, 0xb0000014, v47\n"
    "s_cbranch_vccnz LS20_%=\n"
    "LF20_%=:\n"
    "s_add_u32 m0, s84, 147456\n"
    "s_add_u32 s72, s92, 256\n"
    "s_waitcnt vmcnt(19)\n"
    "buffer_load_dwordx4 v30, s[44:47], s72 offen lds\n"
    "s_add_u32 m0, s85, 147456\n"
    "s_waitcnt vmcnt(19)\n"
    "buffer_load_dwordx4 v31, s[44:47], s72 offen lds\n"
    "s_add_u32 m0, s86, 147456\n"
    "s_waitcnt vmcnt(19)\n"
    "buffer_load_dwordx4 v32, s[44:47], s72 offen lds\n"
    "s_add_u32 m0, s87, 147456\n"
    "s_waitcnt vmcnt(19)\n"
    "buffer_load_dwordx4 v33, s[44:47], s72 offen lds\n"
    "s_add_u32 m0, s84, 98304\n"
    "s_add_u32 s72, s92, 82176\n"
    "s_waitcnt vmcnt(19)\n"
    "buffer_load_dwordx4 v30, s[44:47], s72 offen lds\n"
    "s_add_u32 m0, s85, 98304\n"
    "s_waitcnt vmcnt(19)\n"
    "buffer_load_dwordx4 v31, s[44:47], s72 offen lds\n"
    "s_add_u32 m0, s86, 98304\n"
    "s_waitcnt vmcnt(19)\n"
    "buffer_load_dwordx4 v32, s[44:47], s72 offen lds\n"
    "s_add_u32 m0, s87, 98304\n"
    "s_waitcnt vmcnt(19)\n"
    "buffer_load_dwordx4 v33, s[44:47], s72 offen lds\n"
    "s_add_u32 m0, s84, 106496\n"
    "s_add_u32 s72, s92, 164096\n"
    "s_waitcnt vmcnt(19)\n"
    "buffer_load_dwordx4 v30, s[44:47], s72 offen lds\n"
    "s_add_u32 m0, s85, 106496\n"
    "s_waitcnt vmcnt(19)\n"
    "buffer_load_dwordx4 v31, s[44:47], s72 offen lds\n"
    "s_add_u32 m0, s86, 106496\n"
    "s_waitcnt vmcnt(19)\n"
    "buffer_load_dwordx4 v32, s[44:47], s72 offen lds\n"
    "s_add_u32 m0, s87, 106496\n"
    "s_waitcnt vmcnt(19)\n"
    "buffer_load_dwordx4 v33, s[44:47], s72 offen lds\n"
    "s_mov_b32 m0, s5\n"
    "s_nop 0\n"
    "buffer_load_dword off, s[12:15], 0 offset:64 lds\n"
    "s_add_u32 m0, s80, 131072\n"
    "s_add_u32 s72, s89, 128\n"
    "s_waitcnt vmcnt(18)\n"
    "buffer_load_dwordx4 v18, s[36:39], s72 offen lds\n"
    "buffer_load_dwordx4 v19, s[36:39], s72 offen offset:1024 lds\n"
    "s_add_u32 m0, s81, 131072\n"
    "s_waitcnt vmcnt(18)\n"
    "buffer_load_dwordx4 v20, s[36:39], s72 offen lds\n"
    "buffer_load_dwordx4 v21, s[36:39], s72 offen offset:1024 lds\n"
    "s_add_u32 m0, s82, 131072\n"
    "s_waitcnt vmcnt(18)\n"
    "buffer_load_dwordx4 v22, s[36:39], s72 offen lds\n"
    "buffer_load_dwordx4 v23, s[36:39], s72 offen offset:1024 lds\n"
    "s_add_u32 m0, s83, 131072\n"
    "s_waitcnt vmcnt(18)\n"
    "buffer_load_dwordx4 v24, s[36:39], s72 offen lds\n"
    "buffer_load_dwordx4 v25, s[36:39], s72 offen offset:1024 lds\n"
    "s_mov_b32 m0, s5\n"
    "s_nop 0\n"
    "buffer_load_dword off, s[12:15], 0 offset:68 lds\n"
    "v_cmp_gt_u32 vcc, 0xb0000015, v47\n"
    "s_cbranch_vccz LF21_%=\n"
    "LS21_%=:\n"
    "ds_read_b32 v47, v46\n"
    "s_waitcnt lgkmcnt(0)\n"
    "s_sub_u32 s95, s95, 1\n"
    "s_cbranch_scc1 LE%=\n"
    "v_cmp_gt_u32 vcc, 0xb0000015, v47\n"
    "s_cbranch_vccnz LS21_%=\n"
    "LF21_%=:\n"
    "s_add_u32 m0, s80, 114688\n"
    "s_add_u32 s72, s89, 256\n"
    "s_waitcnt vmcnt(18)\n"
    "buffer_load_dwordx4 v18, s[36:39], s72 offen lds\n"
    "buffer_load_dwordx4 v19, s[36:39], s72 offen offset:1024 lds\n"
    "s_add_u32 m0, s81, 114688\n"
    "s_waitcnt vmcnt(18)\n"
    "buffer_load_dwordx4 v20, s[36:39], s72 offen lds\n"
    "buffer_load_dwordx4 v21, s[36:39], s72 offen offset:1024 lds\n"
    "s_add_u32 m0, s82, 114688\n"
    "s_waitcnt vmcnt(18)\n"
    "buffer_load_dwordx4 v22, s[36:39], s72 offen lds\n"
    "buffer_load_dwordx4 v23, s[36:39], s72 offen offset:1024 lds\n"
    "s_add_u32 m0, s83, 114688\n"
    "s_waitcnt vmcnt(18)\n"
    "buffer_load_dwordx4 v24, s[36:39], s72 offen lds\n"
    "buffer_load_dwordx4 v25, s[36:39], s72 offen offset:1024 lds\n"
    "s_mov_b32 m0, s5\n"
    "s_nop 0\n"
    "buffer_load_dword off, s[12:15], 0 offset:72 lds\n"
    "v_cmp_gt_u32 vcc, 0xb0000016, v47\n"
    "s_cbranch_vccz LF22_%=\n"
    "LS22_%=:\n"
    "ds_read_b32 v47, v46\n"
    "s_waitcnt lgkmcnt(0)\n"
    "s_sub_u32 s95, s95, 1\n"
    "s_cbranch_scc1 LE%=\n"
    "v_cmp_gt_u32 vcc, 0xb0000016, v47\n"
    "s_cbranch_vccnz LS22_%=\n"
    "LF22_%=:\n"
    "s_add_u32 m0, s84, 0\n"
    "s_add_u32 s72, s93, s96\n"
    "s_waitcnt vmcnt(19)\n"
    "buffer_load_dwordx4 v30, s[44:47], s72 offen lds\n"
    "s_add_u32 m0, s85, 0\n"
    "s_waitcnt vmcnt(19)\n"
    "buffer_load_dwordx4 v31, s[44:47], s72 offen lds\n"
    "s_add_u32 m0, s86, 0\n"
    "s_waitcnt vmcnt(19)\n"
    "buffer_load_dwordx4 v32, s[44:47], s72 offen lds\n"
    "s_add_u32 m0, s87, 0\n"
    "s_waitcnt vmcnt(19)\n"
    "buffer_load_dwordx4 v33, s[44:47], s72 offen lds\n"
    "s_add_u32 m0, s84, 8192\n"
    "s_add_u32 s72, s93, s96\n"
    "s_add_u32 s72, s72, 81920\n"
    "s_waitcnt vmcnt(19)\n"
    "buffer_load_dwordx4 v30, s[44:47], s72 offen lds\n"
    "s_add_u32 m0, s85, 8192\n"
    "s_waitcnt vmcnt(19)\n"
    "buffer_load_dwordx4 v31, s[44:47], s72 offen lds\n"
    "s_add_u32 m0, s86, 8192\n"
    "s_waitcnt vmcnt(19)\n"
    "buffer_load_dwordx4 v32, s[44:47], s72 offen lds\n"
    "s_add_u32 m0, s87, 8192\n"
    "s_waitcnt vmcnt(19)\n"
    "buffer_load_dwordx4 v33, s[44:47], s72 offen lds\n"
    "v_cmp_gt_u32 vcc, 0xb0000017, v47\n"
    "s_cbranch_vccz LF23_%=\n"
    "LS23_%=:\n"
    "ds_read_b32 v47, v46\n"
    "s_waitcnt lgkmcnt(0)\n"
    "s_sub_u32 s95, s95, 1\n"
    "s_cbranch_scc1 LE%=\n"
    "v_cmp_gt_u32 vcc, 0xb0000017, v47\n"
    "s_cbranch_vccnz LS23_%=\n"
    "LF23_%=:\n"
    "s_add_u32 m0, s84, 32768\n"
    "s_add_u32 s72, s93, s96\n"
    "s_add_u32 s72, s72, 163840\n"
    "s_waitcnt vmcnt(19)\n"
    "buffer_load_dwordx4 v30, s[44:47], s72 offen lds\n"
    "s_add_u32 m0, s85, 32768\n"
    "s_waitcnt vmcnt(19)\n"
    "buffer_load_dwordx4 v31, s[44:47], s72 offen lds\n"
    "s_add_u32 m0, s86, 32768\n"
    "s_waitcnt vmcnt(19)\n"
    "buffer_load_dwordx4 v32, s[44:47], s72 offen lds\n"
    "s_add_u32 m0, s87, 32768\n"
    "s_waitcnt vmcnt(19)\n"
    "buffer_load_dwordx4 v33, s[44:47], s72 offen lds\n"
    "s_mov_b32 m0, s5\n"
    "s_nop 0\n"
    "buffer_load_dword off, s[12:15], 0 offset:76 lds\n"
    "s_add_u32 m0, s84, 40960\n"
    "s_add_u32 s72, s93, s97\n"
    "s_waitcnt vmcnt(19)\n"
    "buffer_load_dwordx4 v30, s[44:47], s72 offen lds\n"
    "s_add_u32 m0, s85, 40960\n"
    "s_waitcnt vmcnt(19)\n"
    "buffer_load_dwordx4 v31, s[44:47], s72 offen lds\n"
    "s_add_u32 m0, s86, 40960\n"
    "s_waitcnt vmcnt(19)\n"
    "buffer_load_dwordx4 v32, s[44:47], s72 offen lds\n"
    "s_add_u32 m0, s87, 40960\n"
    "s_waitcnt vmcnt(19)\n"
    "buffer_load_dwordx4 v33, s[44:47], s72 offen lds\n"
    "v_cmp_gt_u32 vcc, 0xb0000018, v47\n"
    "s_cbranch_vccz LF24_%=\n"
    "LS24_%=:\n"
    "ds_read_b32 v47, v46\n"
    "s_waitcnt lgkmcnt(0)\n"
    "s_sub_u32 s95, s95, 1\n"
    "s_cbranch_scc1 LE%=\n"
    "v_cmp_gt_u32 vcc, 0xb0000018, v47\n"
    "s_cbranch_vccnz LS24_%=\n"
    "LF24_%=:\n"
    "s_add_u32 m0, s84, 65536\n"
    "s_add_u32 s72, s93, s97\n"
    "s_add_u32 s72, s72, 81920\n"
    "s_waitcnt vmcnt(19)\n"
    "buffer_load_dwordx4 v30, s[44:47], s72 offen lds\n"
    "s_add_u32 m0, s85, 65536\n"
    "s_waitcnt vmcnt(19)\n"
    "buffer_load_dwordx4 v31, s[44:47], s72 offen lds\n"
    "s_add_u32 m0, s86, 65536\n"
    "s_waitcnt vmcnt(19)\n"
    "buffer_load_dwordx4 v32, s[44:47], s72 offen lds\n"
    "s_add_u32 m0, s87, 65536\n"
    "s_waitcnt vmcnt(19)\n"
    "buffer_load_dwordx4 v33, s[44:47], s72 offen lds\n"
    "v_cmp_gt_u32 vcc, 0xb0000019, v47\n"
    "s_cbranch_vccz LF25_%=\n"
    "LS25_%=:\n"
    "ds_read_b32 v47, v46\n"
    "s_waitcnt lgkmcnt(0)\n"
    "s_sub_u32 s95, s95, 1\n"
    "s_cbranch_scc1 LE%=\n"
    "v_cmp_gt_u32 vcc, 0xb0000019, v47\n"
    "s_cbranch_vccnz LS25_%=\n"
    "LF25_%=:\n"
    "s_add_u32 m0, s84, 16384\n"
    "s_add_u32 s72, s93, s97\n"
    "s_add_u32 s72, s72, 163840\n"
    "s_waitcnt vmcnt(19)\n"
    "buffer_load_dwordx4 v30, s[44:47], s72 offen lds\n"
    "s_add_u32 m0, s85, 16384\n"
    "s_waitcnt vmcnt(19)\n"
    "buffer_load_dwordx4 v31, s[44:47], s72 offen lds\n"
    "s_add_u32 m0, s86, 16384\n"
    "s_waitcnt vmcnt(19)\n"
    "buffer_load_dwordx4 v32, s[44:47], s72 offen lds\n"
    "s_add_u32 m0, s87, 16384\n"
    "s_waitcnt vmcnt(19)\n"
    "buffer_load_dwordx4 v33, s[44:47], s72 offen lds\n"
    "s_mov_b32 m0, s5\n"
    "s_nop 0\n"
    "buffer_load_dword off, s[12:15], 0 offset:80 lds\n"
    "v_cmp_gt_u32 vcc, 0xb000001b, v47\n"
    "s_cbranch_vccz LF26_%=\n"
    "LS26_%=:\n"
    "ds_read_b32 v47, v46\n"
    "s_waitcnt lgkmcnt(0)\n"
    "s_sub_u32 s95, s95, 1\n"
    "s_cbranch_scc1 LE%=\n"
    "v_cmp_gt_u32 vcc, 0xb000001b, v47\n"
    "s_cbranch_vccnz LS26_%=\n"
    "LF26_%=:\n"
    "s_add_u32 m0, s80, 81920\n"
    "s_add_u32 s72, s90, 0\n"
    "s_waitcnt vmcnt(18)\n"
    "buffer_load_dwordx4 v18, s[36:39], s72 offen lds\n"
    "buffer_load_dwordx4 v19, s[36:39], s72 offen offset:1024 lds\n"
    "s_add_u32 m0, s81, 81920\n"
    "s_waitcnt vmcnt(18)\n"
    "buffer_load_dwordx4 v20, s[36:39], s72 offen lds\n"
    "buffer_load_dwordx4 v21, s[36:39], s72 offen offset:1024 lds\n"
    "s_add_u32 m0, s82, 81920\n"
    "s_waitcnt vmcnt(18)\n"
    "buffer_load_dwordx4 v22, s[36:39], s72 offen lds\n"
    "buffer_load_dwordx4 v23, s[36:39], s72 offen offset:1024 lds\n"
    "s_add_u32 m0, s83, 81920\n"
    "s_waitcnt vmcnt(18)\n"
    "buffer_load_dwordx4 v24, s[36:39], s72 offen lds\n"
    "buffer_load_dwordx4 v25, s[36:39], s72 offen offset:1024 lds\n"
    "s_mov_b32 m0, s5\n"
    "s_nop 0\n"
    "buffer_load_dword off, s[12:15], 0 offset:84 lds\n"
    "v_cmp_gt_u32 vcc, 0xb000001e, v47\n"
    "s_cbranch_vccz LF27_%=\n"
    "LS27_%=:\n"
    "ds_read_b32 v47, v46\n"
    "s_waitcnt lgkmcnt(0)\n"
    "s_sub_u32 s95, s95, 1\n"
    "s_cbranch_scc1 LE%=\n"
    "v_cmp_gt_u32 vcc, 0xb000001e, v47\n"
    "s_cbranch_vccnz LS27_%=\n"
    "LF27_%=:\n"
    "s_add_u32 m0, s80, 49152\n"
    "s_add_u32 s72, s90, 128\n"
    "s_waitcnt vmcnt(18)\n"
    "buffer_load_dwordx4 v18, s[36:39], s72 offen lds\n"
    "buffer_load_dwordx4 v19, s[36:39], s72 offen offset:1024 lds\n"
    "s_add_u32 m0, s81, 49152\n"
    "s_waitcnt vmcnt(18)\n"
    "buffer_load_dwordx4 v20, s[36:39], s72 offen lds\n"
    "buffer_load_dwordx4 v21, s[36:39], s72 offen offset:1024 lds\n"
    "s_add_u32 m0, s82, 49152\n"
    "s_waitcnt vmcnt(18)\n"
    "buffer_load_dwordx4 v22, s[36:39], s72 offen lds\n"
    "buffer_load_dwordx4 v23, s[36:39], s72 offen offset:1024 lds\n"
    "s_add_u32 m0, s83, 49152\n"
    "s_waitcnt vmcnt(18)\n"
    "buffer_load_dwordx4 v24, s[36:39], s72 offen lds\n"
    "buffer_load_dwordx4 v25, s[36:39], s72 offen offset:1024 lds\n"
    "s_mov_b32 m0, s5\n"
    "s_nop 0\n"
    "buffer_load_dword off, s[12:15], 0 offset:88 lds\n"
    "v_cmp_gt_u32 vcc, 0xb000001f, v47\n"
    "s_cbranch_vccz LF28_%=\n"
    "LS28_%=:\n"
    "ds_read_b32 v47, v46\n"
    "s_waitcnt lgkmcnt(0)\n"
    "s_sub_u32 s95, s95, 1\n"
    "s_cbranch_scc1 LE%=\n"
    "v_cmp_gt_u32 vcc, 0xb000001f, v47\n"
    "s_cbranch_vccnz LS28_%=\n"
    "LF28_%=:\n"
    "s_add_u32 m0, s80, 131072\n"
    "s_add_u32 s72, s90, 256\n"
    "s_waitcnt vmcnt(18)\n"
    "buffer_load_dwordx4 v18, s[36:39], s72 offen lds\n"
    "buffer_load_dwordx4 v19, s[36:39], s72 offen offset:1024 lds\n"
    "s_add_u32 m0, s81, 131072\n"
    "s_waitcnt vmcnt(18)\n"
    "buffer_load_dwordx4 v20, s[36:39], s72 offen lds\n"
    "buffer_load_dwordx4 v21, s[36:39], s72 offen offset:1024 lds\n"
    "s_add_u32 m0, s82, 131072\n"
    "s_waitcnt vmcnt(18)\n"
    "buffer_load_dwordx4 v22, s[36:39], s72 offen lds\n"
    "buffer_load_dwordx4 v23, s[36:39], s72 offen offset:1024 lds\n"
    "s_add_u32 m0, s83, 131072\n"
    "s_waitcnt vmcnt(18)\n"
    "buffer_load_dwordx4 v24, s[36:39], s72 offen lds\n"
    "buffer_load_dwordx4 v25, s[36:39], s72 offen offset:1024 lds\n"
    "s_mov_b32 m0, s5\n"
    "s_nop 0\n"
    "buffer_load_dword off, s[12:15], 0 offset:92 lds\n"
    "s_add_u32 m0, s84, 24576\n"
    "s_add_u32 s72, s94, 256\n"
    "s_waitcnt vmcnt(19)\n"
    "buffer_load_dwordx4 v30, s[44:47], s72 offen lds\n"
    "s_add_u32 m0, s85, 24576\n"
    "s_waitcnt vmcnt(19)\n"
    "buffer_load_dwordx4 v31, s[44:47], s72 offen lds\n"
    "s_add_u32 m0, s86, 24576\n"
    "s_waitcnt vmcnt(19)\n"
    "buffer_load_dwordx4 v32, s[44:47], s72 offen lds\n"
    "s_add_u32 m0, s87, 24576\n"
    "s_waitcnt vmcnt(19)\n"
    "buffer_load_dwordx4 v33, s[44:47], s72 offen lds\n"
    "s_add_u32 m0, s84, 73728\n"
    "s_add_u32 s72, s94, 82176\n"
    "s_waitcnt vmcnt(19)\n"
    "buffer_load_dwordx4 v30, s[44:47], s72 offen lds\n"
    "s_add_u32 m0, s85, 73728\n"
    "s_waitcnt vmcnt(19)\n"
    "buffer_load_dwordx4 v31, s[44:47], s72 offen lds\n"
    "s_add_u32 m0, s86, 73728\n"
    "s_waitcnt vmcnt(19)\n"
    "buffer_load_dwordx4 v32, s[44:47], s72 offen lds\n"
    "s_add_u32 m0, s87, 73728\n"
    "s_waitcnt vmcnt(19)\n"
    "buffer_load_dwordx4 v33, s[44:47], s72 offen lds\n"
    "v_cmp_gt_u32 vcc, 0xb0000020, v47\n"
    "s_cbranch_vccz LF29_%=\n"
    "LS29_%=:\n"
    "ds_read_b32 v47, v46\n"
    "s_waitcnt lgkmcnt(0)\n"
    "s_sub_u32 s95, s95, 1\n"
    "s_cbranch_scc1 LE%=\n"
    "v_cmp_gt_u32 vcc, 0xb0000020, v47\n"
    "s_cbranch_vccnz LS29_%=\n"
    "LF29_%=:\n"
    "s_add_u32 m0, s84, 114688\n"
    "s_add_u32 s72, s94, 164096\n"
    "s_waitcnt vmcnt(19)\n"
    "buffer_load_dwordx4 v30, s[44:47], s72 offen lds\n"
    "s_add_u32 m0, s85, 114688\n"
    "s_waitcnt vmcnt(19)\n"
    "buffer_load_dwordx4 v31, s[44:47], s72 offen lds\n"
    "s_add_u32 m0, s86, 114688\n"
    "s_waitcnt vmcnt(19)\n"
    "buffer_load_dwordx4 v32, s[44:47], s72 offen lds\n"
    "s_add_u32 m0, s87, 114688\n"
    "s_waitcnt vmcnt(19)\n"
    "buffer_load_dwordx4 v33, s[44:47], s72 offen lds\n"
    "s_mov_b32 m0, s5\n"
    "s_nop 0\n"
    "buffer_load_dword off, s[12:15], 0 offset:96 lds\n"
    "s_add_u32 m0, s84, 122880\n"
    "s_add_u32 s72, s94, s96\n"
    "s_waitcnt vmcnt(19)\n"
    "buffer_load_dwordx4 v30, s[44:47], s72 offen lds\n"
    "s_add_u32 m0, s85, 122880\n"
    "s_waitcnt vmcnt(19)\n"
    "buffer_load_dwordx4 v31, s[44:47], s72 offen lds\n"
    "s_add_u32 m0, s86, 122880\n"
    "s_waitcnt vmcnt(19)\n"
    "buffer_load_dwordx4 v32, s[44:47], s72 offen lds\n"
    "s_add_u32 m0, s87, 122880\n"
    "s_waitcnt vmcnt(19)\n"
    "buffer_load_dwordx4 v33, s[44:47], s72 offen lds\n"
    "v_cmp_gt_u32 vcc, 0xb0000021, v47\n"
    "s_cbranch_vccz LF30_%=\n"
    "LS30_%=:\n"
    "ds_read_b32 v47, v46\n"
    "s_waitcnt lgkmcnt(0)\n"
    "s_sub_u32 s95, s95, 1\n"
    "s_cbranch_scc1 LE%=\n"
    "v_cmp_gt_u32 vcc, 0xb0000021, v47\n"
    "s_cbranch_vccnz LS30_%=\n"
    "LF30_%=:\n"
    "s_add_u32 m0, s84, 147456\n"
    "s_add_u32 s72, s94, s96\n"
    "s_add_u32 s72, s72, 81920\n"
    "s_waitcnt vmcnt(19)\n"
    "buffer_load_dwordx4 v30, s[44:47], s72 offen lds\n"
    "s_add_u32 m0, s85, 147456\n"
    "s_waitcnt vmcnt(19)\n"
    "buffer_load_dwordx4 v31, s[44:47], s72 offen lds\n"
    "s_add_u32 m0, s86, 147456\n"
    "s_waitcnt vmcnt(19)\n"
    "buffer_load_dwordx4 v32, s[44:47], s72 offen lds\n"
    "s_add_u32 m0, s87, 147456\n"
    "s_waitcnt vmcnt(19)\n"
    "buffer_load_dwordx4 v33, s[44:47], s72 offen lds\n"
    "v_cmp_gt_u32 vcc, 0xb0000022, v47\n"
    "s_cbranch_vccz LF31_%=\n"
    "LS31_%=:\n"
    "ds_read_b32 v47, v46\n"
    "s_waitcnt lgkmcnt(0)\n"
    "s_sub_u32 s95, s95, 1\n"
    "s_cbranch_scc1 LE%=\n"
    "v_cmp_gt_u32 vcc, 0xb0000022, v47\n"
    "s_cbranch_vccnz LS31_%=\n"
    "LF31_%=:\n"
    "s_add_u32 m0, s84, 98304\n"
    "s_add_u32 s72, s94, s96\n"
    "s_add_u32 s72, s72, 163840\n"
    "s_waitcnt vmcnt(19)\n"
    "buffer_load_dwordx4 v30, s[44:47], s72 offen lds\n"
    "s_add_u32 m0, s85, 98304\n"
    "s_waitcnt vmcnt(19)\n"
    "buffer_load_dwordx4 v31, s[44:47], s72 offen lds\n"
    "s_add_u32 m0, s86, 98304\n"
    "s_waitcnt vmcnt(19)\n"
    "buffer_load_dwordx4 v32, s[44:47], s72 offen lds\n"
    "s_add_u32 m0, s87, 98304\n"
    "s_waitcnt vmcnt(19)\n"
    "buffer_load_dwordx4 v33, s[44:47], s72 offen lds\n"
    "s_mov_b32 m0, s5\n"
    "s_nop 0\n"
    "buffer_load_dword off, s[12:15], 0 offset:100 lds\n"
    "v_cmp_gt_u32 vcc, 0xb0000025, v47\n"
    "s_cbranch_vccz LF32_%=\n"
    "LS32_%=:\n"
    "ds_read_b32 v47, v46\n"
    "s_waitcnt lgkmcnt(0)\n"
    "s_sub_u32 s95, s95, 1\n"
    "s_cbranch_scc1 LE%=\n"
    "v_cmp_gt_u32 vcc, 0xb0000025, v47\n"
    "s_cbranch_vccnz LS32_%=\n"
    "LF32_%=:\n"
    "s_add_u32 m0, s80, 0\n"
    "s_add_u32 s72, s91, 0\n"
    "s_waitcnt vmcnt(18)\n"
    "buffer_load_dwordx4 v18, s[36:39], s72 offen lds\n"
    "buffer_load_dwordx4 v19, s[36:39], s72 offen offset:1024 lds\n"
    "s_add_u32 m0, s81, 0\n"
    "s_waitcnt vmcnt(18)\n"
    "buffer_load_dwordx4 v20, s[36:39], s72 offen lds\n"
    "buffer_load_dwordx4 v21, s[36:39], s72 offen offset:1024 lds\n"
    "s_add_u32 m0, s82, 0\n"
    "s_waitcnt vmcnt(18)\n"
    "buffer_load_dwordx4 v22, s[36:39], s72 offen lds\n"
    "buffer_load_dwordx4 v23, s[36:39], s72 offen offset:1024 lds\n"
    "s_add_u32 m0, s83, 0\n"
    "s_waitcnt vmcnt(18)\n"
    "buffer_load_dwordx4 v24, s[36:39], s72 offen lds\n"
    "buffer_load_dwordx4 v25, s[36:39], s72 offen offset:1024 lds\n"
    "s_mov_b32 m0, s5\n"
    "s_nop 0\n"
    "buffer_load_dword off, s[12:15], 0 offset:104 lds\n"
    "v_cmp_gt_u32 vcc, 0xb0000027, v47\n"
    "s_cbranch_vccz LF33_%=\n"
    "LS33_%=:\n"
    "ds_read_b32 v47, v46\n"
    "s_waitcnt lgkmcnt(0)\n"
    "s_sub_u32 s95, s95, 1\n"
    "s_cbranch_scc1 LE%=\n"
    "v_cmp_gt_u32 vcc, 0xb0000027, v47\n"
    "s_cbranch_vccnz LS33_%=\n"
    "LF33_%=:\n"
    "s_add_u32 m0, s80, 81920\n"
    "s_add_u32 s72, s91, 128\n"
    "s_waitcnt vmcnt(18)\n"
    "buffer_load_dwordx4 v18, s[36:39], s72 offen lds\n"
    "buffer_load_dwordx4 v19, s[36:39], s72 offen offset:1024 lds\n"
    "s_add_u32 m0, s81, 81920\n"
    "s_waitcnt vmcnt(18)\n"
    "buffer_load_dwordx4 v20, s[36:39], s72 offen lds\n"
    "buffer_load_dwordx4 v21, s[36:39], s72 offen offset:1024 lds\n"
    "s_add_u32 m0, s82, 81920\n"
    "s_waitcnt vmcnt(18)\n"
    "buffer_load_dwordx4 v22, s[36:39], s72 offen lds\n"
    "buffer_load_dwordx4 v23, s[36:39], s72 offen offset:1024 lds\n"
    "s_add_u32 m0, s83, 81920\n"
    "s_waitcnt vmcnt(18)\n"
    "buffer_load_dwordx4 v24, s[36:39], s72 offen lds\n"
    "buffer_load_dwordx4 v25, s[36:39], s72 offen offset:1024 lds\n"
    "s_mov_b32 m0, s5\n"
    "s_nop 0\n"
    "buffer_load_dword off, s[12:15], 0 offset:108 lds\n"
    "s_add_u32 m0, s84, 106496\n"
    "s_add_u32 s72, s69, s96\n"
    "s_waitcnt vmcnt(19)\n"
    "buffer_load_dwordx4 v30, s[44:47], s72 offen lds\n"
    "s_add_u32 m0, s85, 106496\n"
    "s_waitcnt vmcnt(19)\n"
    "buffer_load_dwordx4 v31, s[44:47], s72 offen lds\n"
    "s_add_u32 m0, s86, 106496\n"
    "s_waitcnt vmcnt(19)\n"
    "buffer_load_dwordx4 v32, s[44:47], s72 offen lds\n"
    "s_add_u32 m0, s87, 106496\n"
    "s_waitcnt vmcnt(19)\n"
    "buffer_load_dwordx4 v33, s[44:47], s72 offen lds\n"
    "s_add_u32 m0, s84, 32768\n"
    "s_add_u32 s72, s69, s96\n"
    "s_add_u32 s72, s72, 81920\n"
    "s_waitcnt vmcnt(19)\n"
    "buffer_load_dwordx4 v30, s[44:47], s72 offen lds\n"
    "s_add_u32 m0, s85, 32768\n"
    "s_waitcnt vmcnt(19)\n"
    "buffer_load_dwordx4 v31, s[44:47], s72 offen lds\n"
    "s_add_u32 m0, s86, 32768\n"
    "s_waitcnt vmcnt(19)\n"
    "buffer_load_dwordx4 v32, s[44:47], s72 offen lds\n"
    "s_add_u32 m0, s87, 32768\n"
    "s_waitcnt vmcnt(19)\n"
    "buffer_load_dwordx4 v33, s[44:47], s72 offen lds\n"
    "v_cmp_gt_u32 vcc, 0xb0000028, v47\n"
    "s_cbranch_vccz LF34_%=\n"
    "LS34_%=:\n"
    "ds_read_b32 v47, v46\n"
    "s_waitcnt lgkmcnt(0)\n"
    "s_sub_u32 s95, s95, 1\n"
    "s_cbranch_scc1 LE%=\n"
    "v_cmp_gt_u32 vcc, 0xb0000028, v47\n"
    "s_cbranch_vccnz LS34_%=\n"
    "LF34_%=:\n"
    "s_add_u32 m0, s84, 40960\n"
    "s_add_u32 s72, s69, s96\n"
    "s_add_u32 s72, s72, 163840\n"
    "s_waitcnt vmcnt(19)\n"
    "buffer_load_dwordx4 v30, s[44:47], s72 offen lds\n"
    "s_add_u32 m0, s85, 40960\n"
    "s_waitcnt vmcnt(19)\n"
    "buffer_load_dwordx4 v31, s[44:47], s72 offen lds\n"
    "s_add_u32 m0, s86, 40960\n"
    "s_waitcnt vmcnt(19)\n"
    "buffer_load_dwordx4 v32, s[44:47], s72 offen lds\n"
    "s_add_u32 m0, s87, 40960\n"
    "s_waitcnt vmcnt(19)\n"
    "buffer_load_dwordx4 v33, s[44:47], s72 offen lds\n"
    "s_mov_b32 m0, s5\n"
    "s_nop 0\n"
    "buffer_load_dword off, s[12:15], 0 offset:112 lds\n"
    "v_cmp_gt_u32 vcc, 0xb000002b, v47\n"
    "s_cbranch_vccz LF35_%=\n"
    "LS35_%=:\n"
    "ds_read_b32 v47, v46\n"
    "s_waitcnt lgkmcnt(0)\n"
    "s_sub_u32 s95, s95, 1\n"
    "s_cbranch_scc1 LE%=\n"
    "v_cmp_gt_u32 vcc, 0xb000002b, v47\n"
    "s_cbranch_vccnz LS35_%=\n"
    "LF35_%=:\n"
    "s_add_u32 m0, s80, 49152\n"
    "s_add_u32 s72, s91, 256\n"
    "s_waitcnt vmcnt(18)\n"
    "buffer_load_dwordx4 v18, s[36:39], s72 offen lds\n"
    "buffer_load_dwordx4 v19, s[36:39], s72 offen offset:1024 lds\n"
    "s_add_u32 m0, s81, 49152\n"
    "s_waitcnt vmcnt(18)\n"
    "buffer_load_dwordx4 v20, s[36:39], s72 offen lds\n"
    "buffer_load_dwordx4 v21, s[36:39], s72 offen offset:1024 lds\n"
    "s_add_u32 m0, s82, 49152\n"
    "s_waitcnt vmcnt(18)\n"
    "buffer_load_dwordx4 v22, s[36:39], s72 offen lds\n"
    "buffer_load_dwordx4 v23, s[36:39], s72 offen offset:1024 lds\n"
    "s_add_u32 m0, s83, 49152\n"
    "s_waitcnt vmcnt(18)\n"
    "buffer_load_dwordx4 v24, s[36:39], s72 offen lds\n"
    "buffer_load_dwordx4 v25, s[36:39], s72 offen offset:1024 lds\n"
    "s_mov_b32 m0, s5\n"
    "s_nop 0\n"
    "buffer_load_dword off, s[12:15], 0 offset:116 lds\n"
    "s_add_u32 m0, s84, 65536\n"
    "s_add_u32 s72, s94, s97\n"
    "s_waitcnt vmcnt(19)\n"
    "buffer_load_dwordx4 v30, s[44:47], s72 offen lds\n"
    "s_add_u32 m0, s85, 65536\n"
    "s_waitcnt vmcnt(19)\n"
    "buffer_load_dwordx4 v31, s[44:47], s72 offen lds\n"
    "s_add_u32 m0, s86, 65536\n"
    "s_waitcnt vmcnt(19)\n"
    "buffer_load_dwordx4 v32, s[44:47], s72 offen lds\n"
    "s_add_u32 m0, s87, 65536\n"
    "s_waitcnt vmcnt(19)\n"
    "buffer_load_dwordx4 v33, s[44:47], s72 offen lds\n"
    "s_add_u32 m0, s84, 16384\n"
    "s_add_u32 s72, s94, s97\n"
    "s_add_u32 s72, s72, 81920\n"
    "s_waitcnt vmcnt(19)\n"
    "buffer_load_dwordx4 v30, s[44:47], s72 offen lds\n"
    "s_add_u32 m0, s85, 16384\n"
    "s_waitcnt vmcnt(19)\n"
    "buffer_load_dwordx4 v31, s[44:47], s72 offen lds\n"
    "s_add_u32 m0, s86, 16384\n"
    "s_waitcnt vmcnt(19)\n"
    "buffer_load_dwordx4 v32, s[44:47], s72 offen lds\n"
    "s_add_u32 m0, s87, 16384\n"
    "s_waitcnt vmcnt(19)\n"
    "buffer_load_dwordx4 v33, s[44:47], s72 offen lds\n"
    "v_cmp_gt_u32 vcc, 0xb000002c, v47\n"
    "s_cbranch_vccz LF36_%=\n"
    "LS36_%=:\n"
    "ds_read_b32 v47, v46\n"
    "s_waitcnt lgkmcnt(0)\n"
    "s_sub_u32 s95, s95, 1\n"
    "s_cbranch_scc1 LE%=\n"
    "v_cmp_gt_u32 vcc, 0xb000002c, v47\n"
    "s_cbranch_vccnz LS36_%=\n"
    "LF36_%=:\n"
    "s_add_u32 m0, s84, 131072\n"
    "s_add_u32 s72, s94, s97\n"
    "s_add_u32 s72, s72, 163840\n"
    "s_waitcnt vmcnt(19)\n"
    "buffer_load_dwordx4 v30, s[44:47], s72 offen lds\n"
    "s_add_u32 m0, s85, 131072\n"
    "s_waitcnt vmcnt(19)\n"
    "buffer_load_dwordx4 v31, s[44:47], s72 offen lds\n"
    "s_add_u32 m0, s86, 131072\n"
    "s_waitcnt vmcnt(19)\n"
    "buffer_load_dwordx4 v32, s[44:47], s72 offen lds\n"
    "s_add_u32 m0, s87, 131072\n"
    "s_waitcnt vmcnt(19)\n"
    "buffer_load_dwordx4 v33, s[44:47], s72 offen lds\n"
    "s_mov_b32 m0, s5\n"
    "s_nop 0\n"
    "buffer_load_dword off, s[12:15], 0 offset:120 lds\n"
    "s_add_u32 m0, s84, 139264\n"
    "s_add_u32 s72, s69, s97\n"
    "s_waitcnt vmcnt(19)\n"
    "buffer_load_dwordx4 v30, s[44:47], s72 offen lds\n"
    "s_add_u32 m0, s85, 139264\n"
    "s_waitcnt vmcnt(19)\n"
    "buffer_load_dwordx4 v31, s[44:47], s72 offen lds\n"
    "s_add_u32 m0, s86, 139264\n"
    "s_waitcnt vmcnt(19)\n"
    "buffer_load_dwordx4 v32, s[44:47], s72 offen lds\n"
    "s_add_u32 m0, s87, 139264\n"
    "s_waitcnt vmcnt(19)\n"
    "buffer_load_dwordx4 v33, s[44:47], s72 offen lds\n"
    "v_cmp_gt_u32 vcc, 0xb000002d, v47\n"
    "s_cbranch_vccz LF37_%=\n"
    "LS37_%=:\n"
    "ds_read_b32 v47, v46\n"
    "s_waitcnt lgkmcnt(0)\n"
    "s_sub_u32 s95, s95, 1\n"
    "s_cbranch_scc1 LE%=\n"
    "v_cmp_gt_u32 vcc, 0xb000002d, v47\n"
    "s_cbranch_vccnz LS37_%=\n"
    "LF37_%=:\n"
    "s_add_u32 m0, s84, 24576\n"
    "s_add_u32 s72, s69, s97\n"
    "s_add_u32 s72, s72, 81920\n"
    "s_waitcnt vmcnt(19)\n"
    "buffer_load_dwordx4 v30, s[44:47], s72 offen lds\n"
    "s_add_u32 m0, s85, 24576\n"
    "s_waitcnt vmcnt(19)\n"
    "buffer_load_dwordx4 v31, s[44:47], s72 offen lds\n"
    "s_add_u32 m0, s86, 24576\n"
    "s_waitcnt vmcnt(19)\n"
    "buffer_load_dwordx4 v32, s[44:47], s72 offen lds\n"
    "s_add_u32 m0, s87, 24576\n"
    "s_waitcnt vmcnt(19)\n"
    "buffer_load_dwordx4 v33, s[44:47], s72 offen lds\n"
    "v_cmp_gt_u32 vcc, 0xb000002e, v47\n"
    "s_cbranch_vccz LF38_%=\n"
    "LS38_%=:\n"
    "ds_read_b32 v47, v46\n"
    "s_waitcnt lgkmcnt(0)\n"
    "s_sub_u32 s95, s95, 1\n"
    "s_cbranch_scc1 LE%=\n"
    "v_cmp_gt_u32 vcc, 0xb000002e, v47\n"
    "s_cbranch_vccnz LS38_%=\n"
    "LF38_%=:\n"
    "s_add_u32 m0, s84, 73728\n"
    "s_add_u32 s72, s69, s97\n"
    "s_add_u32 s72, s72, 163840\n"
    "s_waitcnt vmcnt(19)\n"
    "buffer_load_dwordx4 v30, s[44:47], s72 offen lds\n"
    "s_add_u32 m0, s85, 73728\n"
    "s_waitcnt vmcnt(19)\n"
    "buffer_load_dwordx4 v31, s[44:47], s72 offen lds\n"
    "s_add_u32 m0, s86, 73728\n"
    "s_waitcnt vmcnt(19)\n"
    "buffer_load_dwordx4 v32, s[44:47], s72 offen lds\n"
    "s_add_u32 m0, s87, 73728\n"
    "s_waitcnt vmcnt(19)\n"
    "buffer_load_dwordx4 v33, s[44:47], s72 offen lds\n"
    "s_mov_b32 m0, s5\n"
    "s_nop 0\n"
    "buffer_load_dword off, s[12:15], 0 offset:124 lds\n"
    "s_waitcnt lgkmcnt(0)\n"
    "s_mul_i32 s6, s6, 0x9e3779b1\n"
    "s_add_u32 s6, s6, 0x7f4a7c15\n"
    "s_mul_i32 s7, s7, 0x85ebca6d\n"
    "s_add_u32 s7, s7, 0x2545f491\n"
    "s_branch LR2%=\n"
    "LN%=:\n"
    "s_cmp_ge_u32 s9, 4\n"
    "s_cbranch_scc1 LC%=\n"
    "s_lshr_b32 s60, s59, 2\n"
    "s_and_b32 s63, s62, 0xfc\n"
    "s_xor_b32 s70, s58, s61\n"
    "s_and_b32 s70, s70, 1\n"
    "s_lshr_b32 s71, s58, 1\n"
    "s_mov_b32 s95, 2000000\n"
    "s_lshl_b32 s5, s9, 9\n"
    "s_add_u32 s5, s5, 155648\n"
    "s_getpc_b64 s[34:35]\n"
    "TPCS_%=:\n"
    "s_add_u32 s34, s34, TAB_%=-TPCS_%=\n"
    "s_addc_u32 s35, s35, 0\n"
    "s_waitcnt lgkmcnt(0)\n"
    "s_lshl_b32 s11, s56, 2\n"
    "s_add_u32 s11, s11, s58\n"
    "s_lshl_b32 s11, s11, 21\n"
    "s_add_u32 s26, s26, s11\n"
    "s_addc_u32 s27, s27, 0\n"
    "s_load_dwordx2 s[6:7], s[26:27], 0\n"
    "s_mov_b32 s88, s34\n"
    "s_and_b32 s89, s35, 0xffff\n"
    "s_mov_b32 s90, 256\n"
    "s_mov_b32 s91, 0x20000\n"
    "s_waitcnt lgkmcnt(0)\n"
    "s_mov_b32 m0, s5\n"
    "s_nop 0\n"
    "buffer_load_dword off, s[88:91], 0 offset:248 lds\n"
    "s_mul_i32 s6, s6, 0x9e3779b1\n"
    "s_add_u32 s6, s6, 0x7f4a7c15\n"
    "s_mul_i32 s7, s7, 0x85ebca6d\n"
    "s_add_u32 s7, s7, 0x2545f491\n"
    "s_waitcnt vmcnt(0) lgkmcnt(0)\n"
    "LR2%=:\n"
    "s_mov_b32 s48, s26\n"
    "s_and_b32 s49, s27, 0xffff\n"
    "s_mov_b32 s50, 2097152\n"
    "s_mov_b32 s51, 0x20000\n"
    "s_lshl_b32 s75, s57, 1\n"
    "s_add_u32 s75, s75, s60\n"
    "s_mov_b32 s52, s24\n"
    "s_and_b32 s53, s25, 0xffff\n"
    "s_mov_b32 s54, 1310720\n"
    "s_mov_b32 s55, 0x20000\n"
    "s_lshl_b32 s12, s61, 3\n"
    "s_add_u32 s12, s12, s75\n"
    "s_lshl_b32 s13, s12, 13\n"
    "s_lshl_b32 s23, s12, 14\n"
    "s_add_u32 s13, s13, 524288\n"
    "s_add_u32 s23, s23, 1572864\n"
    "s_lshl_b32 s14, s12, 10\n"
    "s_lshl_b32 s15, s58, 7\n"
    "s_lshl_b32 s11, s75, 4\n"
    "s_add_u32 s15, s15, s11\n"
    "s_mul_i32 s15, s15, 2560\n"
    "s_mul_i32 s11, s56, 1280\n"
    "s_add_u32 s15, s15, s11\n"
    "s_lshr_b32 s11, s61, 1\n"
    "s_mul_i32 s11, s11, 640\n"
    "s_add_u32 s15, s15, s11\n"
    "s_and_b32 s21, s61, 1\n"
    "s_lshl_b32 s16, s21, 6\n"
    "s_add_u32 s16, s16, s15\n"
    "s_add_u32 s16, s16, 256\n"
    "s_mul_i32 s11, s21, 384\n"
    "s_add_u32 s15, s15, s11\n"
    "s_lshl_b32 s11, s71, 7\n"
    "s_add_u32 s17, s15, s11\n"
    "s_xor_b32 s11, s11, 128\n"
    "s_add_u32 s18, s15, s11\n"
    "v_and_b32 v3, 7, v0\n"
    "v_lshlrev_b32 v3, 7, v3\n"
    "v_and_b32 v4, 15, v0\n"
    "v_lshlrev_b32 v4, 7, v4\n"
    "v_and_b32 v7, 3, v0\n"
    "v_lshlrev_b32 v7, 9, v7\n"
    "v_add_u32 v7, 157948, v7\n"
    "s_and_b32 s20, s9, 1\n"
    "s_and_b32 s22, s9, 3\n"
    "s_cmp_ge_u32 s9, 4\n"
    "s_cbranch_scc1 RC_%=\n"
    "s_cmp_lt_u32 s9, 2\n"
    "s_cbranch_scc1 RL_%=\n"
    "RW1_%=:\n"
    "ds_read_b32 v9, v7\n"
    "s_waitcnt lgkmcnt(0)\n"
    "s_sub_u32 s95, s95, 1\n"
    "s_cbranch_scc1 LE%=\n"
    "v_cmp_gt_u32 vcc, 0xb0000038, v9\n"
    "s_cbranch_vccz RX1_%=\n"
    "s_sleep 3\n"
    "s_branch RW1_%=\n"
    "RX1_%=:\n"
    "v_lshrrev_b32 v1, 4, v0\n"
    "v_lshlrev_b32 v1, 8, v1\n"
    "v_and_b32 v5, 15, v0\n"
    "v_lshl_add_u32 v1, v5, 3, v1\n"
    "s_lshl_b32 s11, s20, 7\n"
    "v_add_u32 v1, s11, v1\n"
    "v_and_b32 v6, 3, v0\n"
    "v_lshlrev_b32 v6, 4, v6\n"
    "v_lshrrev_b32 v5, 2, v0\n"
    "v_add_lshl_u32 v6, v6, v5, 2\n"
    "v_lshrrev_b32 v2, 3, v0\n"
    "s_lshl_b32 s11, s20, 3\n"
    "v_add_u32 v2, s11, v2\n"
    "v_mul_u32_u24 v2, 2560, v2\n"
    "v_and_b32 v5, 7, v0\n"
    "v_lshl_add_u32 v2, v5, 3, v2\n"
    "s_lshl_b32 s72, s14, 1\n"
    "s_add_u32 s72, s72, 32768\n"
    "v_mov_b32 v8, 159744\n"
    "s_cmp_eq_u32 s9, 2\n"
    "s_cbranch_scc1 RP2_%=\n"
    "RZ2_%=:\n"
    "ds_read_b32 v9, v8\n"
    "s_waitcnt lgkmcnt(0)\n"
    "s_sub_u32 s95, s95, 1\n"
    "s_cbranch_scc1 LE%=\n"
    "v_cmp_ne_u32 vcc, 1, v9\n"
    "s_cbranch_vccnz RZ2_%=\n"
    "s_branch RY2_%=\n"
    "RP2_%=:\n"
    "buffer_load_dwordx2 v[10:11], v4, s[48:51], s72 offen sc1\n"
    "s_waitcnt vmcnt(0)\n"
    "s_sub_u32 s95, s95, 1\n"
    "s_cbranch_scc1 LE%=\n"
    "v_cmp_ne_u64 vcc, s[6:7], v[10:11]\n"
    "s_cbranch_vccz RQ2_%=\n"
    "s_sleep 3\n"
    "s_branch RP2_%=\n"
    "RQ2_%=:\n"
    "v_mov_b32 v9, 1\n"
    "ds_write_b32 v8, v9\n"
    "RY2_%=:\n"
    "s_add_u32 s73, s23, 0\n"
    "buffer_load_dwordx2 v[32:33], v1, s[48:51], s73 offen offset:0 sc1\n"
    "buffer_load_dwordx2 v[34:35], v1, s[48:51], s73 offen offset:1024 sc1\n"
    "buffer_load_dwordx2 v[36:37], v1, s[48:51], s73 offen offset:2048 sc1\n"
    "buffer_load_dwordx2 v[38:39], v1, s[48:51], s73 offen offset:3072 sc1\n"
    "s_add_u32 s73, s23, 4096\n"
    "buffer_load_dwordx2 v[40:41], v1, s[48:51], s73 offen offset:0 sc1\n"
    "buffer_load_dwordx2 v[42:43], v1, s[48:51], s73 offen offset:1024 sc1\n"
    "buffer_load_dwordx2 v[44:45], v1, s[48:51], s73 offen offset:2048 sc1\n"
    "buffer_load_dwordx2 v[46:47], v1, s[48:51], s73 offen offset:3072 sc1\n"
    "s_add_u32 s73, s23, 8192\n"
    "buffer_load_dwordx2 v[48:49], v1, s[48:51], s73 offen offset:0 sc1\n"
    "buffer_load_dwordx2 v[50:51], v1, s[48:51], s73 offen offset:1024 sc1\n"
    "buffer_load_dwordx2 v[52:53], v1, s[48:51], s73 offen offset:2048 sc1\n"
    "buffer_load_dwordx2 v[54:55], v1, s[48:51], s73 offen offset:3072 sc1\n"
    "s_add_u32 s73, s23, 12288\n"
    "buffer_load_dwordx2 v[56:57], v1, s[48:51], s73 offen offset:0 sc1\n"
    "buffer_load_dwordx2 v[58:59], v1, s[48:51], s73 offen offset:1024 sc1\n"
    "buffer_load_dwordx2 v[60:61], v1, s[48:51], s73 offen offset:2048 sc1\n"
    "buffer_load_dwordx2 v[62:63], v1, s[48:51], s73 offen offset:3072 sc1\n"
    "s_waitcnt vmcnt(15)\n"
    "v_lshlrev_b32 v16, 16, v32\n"
    "v_and_b32 v17, 0xffff0000, v32\n"
    "v_lshlrev_b32 v18, 16, v33\n"
    "v_and_b32 v19, 0xffff0000, v33\n"
    "s_waitcnt vmcnt(14)\n"
    "v_lshlrev_b32 v12, 16, v34\n"
    "v_and_b32 v13, 0xffff0000, v34\n"
    "v_pk_add_f32 v[16:17], v[16:17], v[12:13]\n"
    "v_lshlrev_b32 v14, 16, v35\n"
    "v_and_b32 v15, 0xffff0000, v35\n"
    "v_pk_add_f32 v[18:19], v[18:19], v[14:15]\n"
    "s_waitcnt vmcnt(13)\n"
    "v_lshlrev_b32 v12, 16, v36\n"
    "v_and_b32 v13, 0xffff0000, v36\n"
    "v_pk_add_f32 v[16:17], v[16:17], v[12:13]\n"
    "v_lshlrev_b32 v14, 16, v37\n"
    "v_and_b32 v15, 0xffff0000, v37\n"
    "v_pk_add_f32 v[18:19], v[18:19], v[14:15]\n"
    "s_waitcnt vmcnt(12)\n"
    "v_lshlrev_b32 v12, 16, v38\n"
    "v_and_b32 v13, 0xffff0000, v38\n"
    "v_pk_add_f32 v[16:17], v[16:17], v[12:13]\n"
    "v_lshlrev_b32 v14, 16, v39\n"
    "v_and_b32 v15, 0xffff0000, v39\n"
    "v_pk_add_f32 v[18:19], v[18:19], v[14:15]\n"
    "s_waitcnt vmcnt(11)\n"
    "v_lshlrev_b32 v12, 16, v40\n"
    "v_and_b32 v13, 0xffff0000, v40\n"
    "v_pk_add_f32 v[16:17], v[16:17], v[12:13]\n"
    "v_lshlrev_b32 v14, 16, v41\n"
    "v_and_b32 v15, 0xffff0000, v41\n"
    "v_pk_add_f32 v[18:19], v[18:19], v[14:15]\n"
    "s_waitcnt vmcnt(10)\n"
    "v_lshlrev_b32 v12, 16, v42\n"
    "v_and_b32 v13, 0xffff0000, v42\n"
    "v_pk_add_f32 v[16:17], v[16:17], v[12:13]\n"
    "v_lshlrev_b32 v14, 16, v43\n"
    "v_and_b32 v15, 0xffff0000, v43\n"
    "v_pk_add_f32 v[18:19], v[18:19], v[14:15]\n"
    "s_waitcnt vmcnt(9)\n"
    "v_lshlrev_b32 v12, 16, v44\n"
    "v_and_b32 v13, 0xffff0000, v44\n"
    "v_pk_add_f32 v[16:17], v[16:17], v[12:13]\n"
    "v_lshlrev_b32 v14, 16, v45\n"
    "v_and_b32 v15, 0xffff0000, v45\n"
    "v_pk_add_f32 v[18:19], v[18:19], v[14:15]\n"
    "s_waitcnt vmcnt(8)\n"
    "v_lshlrev_b32 v12, 16, v46\n"
    "v_and_b32 v13, 0xffff0000, v46\n"
    "v_pk_add_f32 v[16:17], v[16:17], v[12:13]\n"
    "v_lshlrev_b32 v14, 16, v47\n"
    "v_and_b32 v15, 0xffff0000, v47\n"
    "v_pk_add_f32 v[18:19], v[18:19], v[14:15]\n"
    "s_waitcnt vmcnt(7)\n"
    "v_lshlrev_b32 v12, 16, v48\n"
    "v_and_b32 v13, 0xffff0000, v48\n"
    "v_pk_add_f32 v[16:17], v[16:17], v[12:13]\n"
    "v_lshlrev_b32 v14, 16, v49\n"
    "v_and_b32 v15, 0xffff0000, v49\n"
    "v_pk_add_f32 v[18:19], v[18:19], v[14:15]\n"
    "s_waitcnt vmcnt(6)\n"
    "v_lshlrev_b32 v12, 16, v50\n"
    "v_and_b32 v13, 0xffff0000, v50\n"
    "v_pk_add_f32 v[16:17], v[16:17], v[12:13]\n"
    "v_lshlrev_b32 v14, 16, v51\n"
    "v_and_b32 v15, 0xffff0000, v51\n"
    "v_pk_add_f32 v[18:19], v[18:19], v[14:15]\n"
    "s_waitcnt vmcnt(5)\n"
    "v_lshlrev_b32 v12, 16, v52\n"
    "v_and_b32 v13, 0xffff0000, v52\n"
    "v_pk_add_f32 v[16:17], v[16:17], v[12:13]\n"
    "v_lshlrev_b32 v14, 16, v53\n"
    "v_and_b32 v15, 0xffff0000, v53\n"
    "v_pk_add_f32 v[18:19], v[18:19], v[14:15]\n"
    "s_waitcnt vmcnt(4)\n"
    "v_lshlrev_b32 v12, 16, v54\n"
    "v_and_b32 v13, 0xffff0000, v54\n"
    "v_pk_add_f32 v[16:17], v[16:17], v[12:13]\n"
    "v_lshlrev_b32 v14, 16, v55\n"
    "v_and_b32 v15, 0xffff0000, v55\n"
    "v_pk_add_f32 v[18:19], v[18:19], v[14:15]\n"
    "s_waitcnt vmcnt(3)\n"
    "v_lshlrev_b32 v12, 16, v56\n"
    "v_and_b32 v13, 0xffff0000, v56\n"
    "v_pk_add_f32 v[16:17], v[16:17], v[12:13]\n"
    "v_lshlrev_b32 v14, 16, v57\n"
    "v_and_b32 v15, 0xffff0000, v57\n"
    "v_pk_add_f32 v[18:19], v[18:19], v[14:15]\n"
    "s_waitcnt vmcnt(2)\n"
    "v_lshlrev_b32 v12, 16, v58\n"
    "v_and_b32 v13, 0xffff0000, v58\n"
    "v_pk_add_f32 v[16:17], v[16:17], v[12:13]\n"
    "v_lshlrev_b32 v14, 16, v59\n"
    "v_and_b32 v15, 0xffff0000, v59\n"
    "v_pk_add_f32 v[18:19], v[18:19], v[14:15]\n"
    "s_waitcnt vmcnt(1)\n"
    "v_lshlrev_b32 v12, 16, v60\n"
    "v_and_b32 v13, 0xffff0000, v60\n"
    "v_pk_add_f32 v[16:17], v[16:17], v[12:13]\n"
    "v_lshlrev_b32 v14, 16, v61\n"
    "v_and_b32 v15, 0xffff0000, v61\n"
    "v_pk_add_f32 v[18:19], v[18:19], v[14:15]\n"
    "s_waitcnt vmcnt(0)\n"
    "v_lshlrev_b32 v12, 16, v62\n"
    "v_and_b32 v13, 0xffff0000, v62\n"
    "v_pk_add_f32 v[16:17], v[16:17], v[12:13]\n"
    "v_lshlrev_b32 v14, 16, v63\n"
    "v_and_b32 v15, 0xffff0000, v63\n"
    "v_pk_add_f32 v[18:19], v[18:19], v[14:15]\n"
    "v_cvt_pk_bf16_f32 v24, v16, v17\n"
    "v_cvt_pk_bf16_f32 v25, v18, v19\n"
    "ds_bpermute_b32 v26, v6, v24\n"
    "ds_bpermute_b32 v27, v6, v25\n"
    "s_waitcnt lgkmcnt(0)\n"
    "buffer_store_dwordx2 v[26:27], v2, s[52:55], s16 offen\n"
    "s_or_b32 s11, s61, s75\n"
    "s_or_b32 s11, s11, s20\n"
    "s_cmp_lg_u32 s11, 0\n"
    "s_cbranch_scc1 RE_%=\n"
    "v_mov_b32 v14, s6\n"
    "v_mov_b32 v15, s7\n"
    "v_lshlrev_b32 v13, 3, v0\n"
    "s_mov_b64 exec, 0xffff\n"
    "buffer_store_dwordx2 v[14:15], v13, s[48:51], 0 offen sc1\n"
    "s_mov_b64 exec, -1\n"
    "RE_%=:\n"
    "s_branch RF_%=\n"
    "RL_%=:\n"
    "s_waitcnt vmcnt(0) lgkmcnt(0)\n"
    "RW3_%=:\n"
    "ds_read_b32 v9, v7\n"
    "s_waitcnt lgkmcnt(0)\n"
    "s_sub_u32 s95, s95, 1\n"
    "s_cbranch_scc1 LE%=\n"
    "v_cmp_gt_u32 vcc, 0xb0000038, v9\n"
    "s_cbranch_vccz RX3_%=\n"
    "s_sleep 3\n"
    "s_branch RW3_%=\n"
    "RX3_%=:\n"
    "s_sleep 12\n"
    "v_lshrrev_b32 v1, 5, v0\n"
    "v_lshlrev_b32 v1, 18, v1\n"
    "v_bfe_u32 v5, v0, 3, 2\n"
    "v_lshl_add_u32 v1, v5, 8, v1\n"
    "v_and_b32 v5, 7, v0\n"
    "v_lshl_add_u32 v1, v5, 3, v1\n"
    "s_lshl_b32 s11, s20, 7\n"
    "v_add_u32 v1, s11, v1\n"
    "v_bfe_u32 v6, v0, 3, 1\n"
    "v_lshlrev_b32 v6, 5, v6\n"
    "v_and_b32 v5, 3, v0\n"
    "v_lshl_add_u32 v6, v5, 3, v6\n"
    "v_lshrrev_b32 v5, 4, v0\n"
    "v_lshl_add_u32 v6, v5, 1, v6\n"
    "v_bfe_u32 v5, v0, 2, 1\n"
    "v_add_lshl_u32 v6, v6, v5, 2\n"
    "v_lshrrev_b32 v2, 4, v0\n"
    "s_lshl_b32 s11, s20, 3\n"
    "v_add_u32 v2, s11, v2\n"
    "v_mul_u32_u24 v2, 2560, v2\n"
    "v_and_b32 v5, 15, v0\n"
    "v_lshl_add_u32 v2, v5, 3, v2\n"
    "s_add_u32 s72, s14, 98304\n"
    "v_mov_b32 v8, 159748\n"
    "s_cmp_eq_u32 s9, 0\n"
    "s_cbranch_scc1 RP4_%=\n"
    "RZ4_%=:\n"
    "ds_read_b32 v9, v8\n"
    "s_waitcnt lgkmcnt(0)\n"
    "s_sub_u32 s95, s95, 1\n"
    "s_cbranch_scc1 LE%=\n"
    "v_cmp_ne_u32 vcc, 1, v9\n"
    "s_cbranch_vccnz RZ4_%=\n"
    "s_branch RY4_%=\n"
    "RP4_%=:\n"
    "buffer_load_dwordx2 v[10:11], v3, s[48:51], s72 offen sc1\n"
    "s_waitcnt vmcnt(0)\n"
    "s_sub_u32 s95, s95, 1\n"
    "s_cbranch_scc1 LE%=\n"
    "v_cmp_ne_u64 vcc, s[6:7], v[10:11]\n"
    "s_cbranch_vccz RQ4_%=\n"
    "s_sleep 6\n"
    "s_branch RP4_%=\n"
    "RQ4_%=:\n"
    "v_mov_b32 v9, 1\n"
    "ds_write_b32 v8, v9\n"
    "RY4_%=:\n"
    "s_add_u32 s73, s13, 0\n"
    "buffer_load_dwordx2 v[32:33], v1, s[48:51], s73 offen offset:0 sc1\n"
    "buffer_load_dwordx2 v[34:35], v1, s[48:51], s73 offen offset:1024 sc1\n"
    "buffer_load_dwordx2 v[36:37], v1, s[48:51], s73 offen offset:2048 sc1\n"
    "buffer_load_dwordx2 v[38:39], v1, s[48:51], s73 offen offset:3072 sc1\n"
    "s_add_u32 s73, s13, 4096\n"
    "buffer_load_dwordx2 v[40:41], v1, s[48:51], s73 offen offset:0 sc1\n"
    "buffer_load_dwordx2 v[42:43], v1, s[48:51], s73 offen offset:1024 sc1\n"
    "buffer_load_dwordx2 v[44:45], v1, s[48:51], s73 offen offset:2048 sc1\n"
    "buffer_load_dwordx2 v[46:47], v1, s[48:51], s73 offen offset:3072 sc1\n"
    "s_add_u32 s73, s13, 64\n"
    "buffer_load_dwordx2 v[48:49], v1, s[48:51], s73 offen offset:0 sc1\n"
    "buffer_load_dwordx2 v[50:51], v1, s[48:51], s73 offen offset:1024 sc1\n"
    "buffer_load_dwordx2 v[52:53], v1, s[48:51], s73 offen offset:2048 sc1\n"
    "buffer_load_dwordx2 v[54:55], v1, s[48:51], s73 offen offset:3072 sc1\n"
    "s_add_u32 s73, s13, 4160\n"
    "buffer_load_dwordx2 v[56:57], v1, s[48:51], s73 offen offset:0 sc1\n"
    "buffer_load_dwordx2 v[58:59], v1, s[48:51], s73 offen offset:1024 sc1\n"
    "buffer_load_dwordx2 v[60:61], v1, s[48:51], s73 offen offset:2048 sc1\n"
    "buffer_load_dwordx2 v[62:63], v1, s[48:51], s73 offen offset:3072 sc1\n"
    "s_waitcnt vmcnt(15)\n"
    "v_lshlrev_b32 v16, 16, v32\n"
    "v_and_b32 v17, 0xffff0000, v32\n"
    "v_lshlrev_b32 v18, 16, v33\n"
    "v_and_b32 v19, 0xffff0000, v33\n"
    "s_waitcnt vmcnt(14)\n"
    "v_lshlrev_b32 v12, 16, v34\n"
    "v_and_b32 v13, 0xffff0000, v34\n"
    "v_pk_add_f32 v[16:17], v[16:17], v[12:13]\n"
    "v_lshlrev_b32 v14, 16, v35\n"
    "v_and_b32 v15, 0xffff0000, v35\n"
    "v_pk_add_f32 v[18:19], v[18:19], v[14:15]\n"
    "s_waitcnt vmcnt(13)\n"
    "v_lshlrev_b32 v12, 16, v36\n"
    "v_and_b32 v13, 0xffff0000, v36\n"
    "v_pk_add_f32 v[16:17], v[16:17], v[12:13]\n"
    "v_lshlrev_b32 v14, 16, v37\n"
    "v_and_b32 v15, 0xffff0000, v37\n"
    "v_pk_add_f32 v[18:19], v[18:19], v[14:15]\n"
    "s_waitcnt vmcnt(12)\n"
    "v_lshlrev_b32 v12, 16, v38\n"
    "v_and_b32 v13, 0xffff0000, v38\n"
    "v_pk_add_f32 v[16:17], v[16:17], v[12:13]\n"
    "v_lshlrev_b32 v14, 16, v39\n"
    "v_and_b32 v15, 0xffff0000, v39\n"
    "v_pk_add_f32 v[18:19], v[18:19], v[14:15]\n"
    "s_waitcnt vmcnt(11)\n"
    "v_lshlrev_b32 v12, 16, v40\n"
    "v_and_b32 v13, 0xffff0000, v40\n"
    "v_pk_add_f32 v[16:17], v[16:17], v[12:13]\n"
    "v_lshlrev_b32 v14, 16, v41\n"
    "v_and_b32 v15, 0xffff0000, v41\n"
    "v_pk_add_f32 v[18:19], v[18:19], v[14:15]\n"
    "s_waitcnt vmcnt(10)\n"
    "v_lshlrev_b32 v12, 16, v42\n"
    "v_and_b32 v13, 0xffff0000, v42\n"
    "v_pk_add_f32 v[16:17], v[16:17], v[12:13]\n"
    "v_lshlrev_b32 v14, 16, v43\n"
    "v_and_b32 v15, 0xffff0000, v43\n"
    "v_pk_add_f32 v[18:19], v[18:19], v[14:15]\n"
    "s_waitcnt vmcnt(9)\n"
    "v_lshlrev_b32 v12, 16, v44\n"
    "v_and_b32 v13, 0xffff0000, v44\n"
    "v_pk_add_f32 v[16:17], v[16:17], v[12:13]\n"
    "v_lshlrev_b32 v14, 16, v45\n"
    "v_and_b32 v15, 0xffff0000, v45\n"
    "v_pk_add_f32 v[18:19], v[18:19], v[14:15]\n"
    "s_waitcnt vmcnt(8)\n"
    "v_lshlrev_b32 v12, 16, v46\n"
    "v_and_b32 v13, 0xffff0000, v46\n"
    "v_pk_add_f32 v[16:17], v[16:17], v[12:13]\n"
    "v_lshlrev_b32 v14, 16, v47\n"
    "v_and_b32 v15, 0xffff0000, v47\n"
    "v_pk_add_f32 v[18:19], v[18:19], v[14:15]\n"
    "v_cvt_pk_bf16_f32 v24, v16, v17\n"
    "v_cvt_pk_bf16_f32 v25, v18, v19\n"
    "ds_bpermute_b32 v26, v6, v24\n"
    "ds_bpermute_b32 v27, v6, v25\n"
    "s_waitcnt lgkmcnt(0)\n"
    "buffer_store_dwordx2 v[26:27], v2, s[52:55], s17 offen\n"
    "s_waitcnt vmcnt(8)\n"
    "v_lshlrev_b32 v16, 16, v48\n"
    "v_and_b32 v17, 0xffff0000, v48\n"
    "v_lshlrev_b32 v18, 16, v49\n"
    "v_and_b32 v19, 0xffff0000, v49\n"
    "s_waitcnt vmcnt(7)\n"
    "v_lshlrev_b32 v12, 16, v50\n"
    "v_and_b32 v13, 0xffff0000, v50\n"
    "v_pk_add_f32 v[16:17], v[16:17], v[12:13]\n"
    "v_lshlrev_b32 v14, 16, v51\n"
    "v_and_b32 v15, 0xffff0000, v51\n"
    "v_pk_add_f32 v[18:19], v[18:19], v[14:15]\n"
    "s_waitcnt vmcnt(6)\n"
    "v_lshlrev_b32 v12, 16, v52\n"
    "v_and_b32 v13, 0xffff0000, v52\n"
    "v_pk_add_f32 v[16:17], v[16:17], v[12:13]\n"
    "v_lshlrev_b32 v14, 16, v53\n"
    "v_and_b32 v15, 0xffff0000, v53\n"
    "v_pk_add_f32 v[18:19], v[18:19], v[14:15]\n"
    "s_waitcnt vmcnt(5)\n"
    "v_lshlrev_b32 v12, 16, v54\n"
    "v_and_b32 v13, 0xffff0000, v54\n"
    "v_pk_add_f32 v[16:17], v[16:17], v[12:13]\n"
    "v_lshlrev_b32 v14, 16, v55\n"
    "v_and_b32 v15, 0xffff0000, v55\n"
    "v_pk_add_f32 v[18:19], v[18:19], v[14:15]\n"
    "s_waitcnt vmcnt(4)\n"
    "v_lshlrev_b32 v12, 16, v56\n"
    "v_and_b32 v13, 0xffff0000, v56\n"
    "v_pk_add_f32 v[16:17], v[16:17], v[12:13]\n"
    "v_lshlrev_b32 v14, 16, v57\n"
    "v_and_b32 v15, 0xffff0000, v57\n"
    "v_pk_add_f32 v[18:19], v[18:19], v[14:15]\n"
    "s_waitcnt vmcnt(3)\n"
    "v_lshlrev_b32 v12, 16, v58\n"
    "v_and_b32 v13, 0xffff0000, v58\n"
    "v_pk_add_f32 v[16:17], v[16:17], v[12:13]\n"
    "v_lshlrev_b32 v14, 16, v59\n"
    "v_and_b32 v15, 0xffff0000, v59\n"
    "v_pk_add_f32 v[18:19], v[18:19], v[14:15]\n"
    "s_waitcnt vmcnt(2)\n"
    "v_lshlrev_b32 v12, 16, v60\n"
    "v_and_b32 v13, 0xffff0000, v60\n"
    "v_pk_add_f32 v[16:17], v[16:17], v[12:13]\n"
    "v_lshlrev_b32 v14, 16, v61\n"
    "v_and_b32 v15, 0xffff0000, v61\n"
    "v_pk_add_f32 v[18:19], v[18:19], v[14:15]\n"
    "s_waitcnt vmcnt(1)\n"
    "v_lshlrev_b32 v12, 16, v62\n"
    "v_and_b32 v13, 0xffff0000, v62\n"
    "v_pk_add_f32 v[16:17], v[16:17], v[12:13]\n"
    "v_lshlrev_b32 v14, 16, v63\n"
    "v_and_b32 v15, 0xffff0000, v63\n"
    "v_pk_add_f32 v[18:19], v[18:19], v[14:15]\n"
    "v_cvt_pk_bf16_f32 v28, v16, v17\n"
    "v_cvt_pk_bf16_f32 v29, v18, v19\n"
    "ds_bpermute_b32 v30, v6, v28\n"
    "ds_bpermute_b32 v31, v6, v29\n"
    "s_add_u32 s73, s17, 10240\n"
    "s_waitcnt lgkmcnt(0)\n"
    "buffer_store_dwordx2 v[30:31], v2, s[52:55], s73 offen\n"
    "s_branch RF_%=\n"
    "RC_%=:\n"
    "s_waitcnt lgkmcnt(0)\n"
    "s_sleep 20\n"
    "v_lshrrev_b32 v1, 5, v0\n"
    "v_lshlrev_b32 v1, 18, v1\n"
    "v_bfe_u32 v5, v0, 3, 2\n"
    "v_lshl_add_u32 v1, v5, 8, v1\n"
    "v_and_b32 v5, 7, v0\n"
    "v_lshl_add_u32 v1, v5, 3, v1\n"
    "s_lshl_b32 s11, s22, 6\n"
    "v_add_u32 v1, s11, v1\n"
    "v_bfe_u32 v6, v0, 3, 1\n"
    "v_lshlrev_b32 v6, 5, v6\n"
    "v_and_b32 v5, 3, v0\n"
    "v_lshl_add_u32 v6, v5, 3, v6\n"
    "v_lshrrev_b32 v5, 4, v0\n"
    "v_lshl_add_u32 v6, v5, 1, v6\n"
    "v_bfe_u32 v5, v0, 2, 1\n"
    "v_add_lshl_u32 v6, v6, v5, 2\n"
    "v_lshrrev_b32 v2, 4, v0\n"
    "s_lshl_b32 s11, s22, 2\n"
    "v_add_u32 v2, s11, v2\n"
    "v_mul_u32_u24 v2, 2560, v2\n"
    "v_and_b32 v5, 15, v0\n"
    "v_lshl_add_u32 v2, v5, 3, v2\n"
    "s_add_u32 s72, s14, 131072\n"
    "v_mov_b32 v8, 159752\n"
    "s_cmp_eq_u32 s9, 4\n"
    "s_cbranch_scc1 RP5_%=\n"
    "RZ5_%=:\n"
    "ds_read_b32 v9, v8\n"
    "s_waitcnt lgkmcnt(0)\n"
    "s_sub_u32 s95, s95, 1\n"
    "s_cbranch_scc1 LE%=\n"
    "v_cmp_ne_u32 vcc, 1, v9\n"
    "s_cbranch_vccnz RZ5_%=\n"
    "s_branch RY5_%=\n"
    "RP5_%=:\n"
    "buffer_load_dwordx2 v[10:11], v3, s[48:51], s72 offen sc1\n"
    "s_waitcnt vmcnt(0)\n"
    "s_sub_u32 s95, s95, 1\n"
    "s_cbranch_scc1 LE%=\n"
    "v_cmp_ne_u64 vcc, s[6:7], v[10:11]\n"
    "s_cbranch_vccz RQ5_%=\n"
    "s_sleep 4\n"
    "s_branch RP5_%=\n"
    "RQ5_%=:\n"
    "v_mov_b32 v9, 1\n"
    "ds_write_b32 v8, v9\n"
    "RY5_%=:\n"
    "s_add_u32 s73, s13, 524288\n"
    "buffer_load_dwordx2 v[32:33], v1, s[48:51], s73 offen offset:0 sc1\n"
    "buffer_load_dwordx2 v[34:35], v1, s[48:51], s73 offen offset:1024 sc1\n"
    "buffer_load_dwordx2 v[36:37], v1, s[48:51], s73 offen offset:2048 sc1\n"
    "buffer_load_dwordx2 v[38:39], v1, s[48:51], s73 offen offset:3072 sc1\n"
    "s_add_u32 s73, s13, 528384\n"
    "buffer_load_dwordx2 v[40:41], v1, s[48:51], s73 offen offset:0 sc1\n"
    "buffer_load_dwordx2 v[42:43], v1, s[48:51], s73 offen offset:1024 sc1\n"
    "buffer_load_dwordx2 v[44:45], v1, s[48:51], s73 offen offset:2048 sc1\n"
    "buffer_load_dwordx2 v[46:47], v1, s[48:51], s73 offen offset:3072 sc1\n"
    "s_waitcnt vmcnt(7)\n"
    "v_lshlrev_b32 v16, 16, v32\n"
    "v_and_b32 v17, 0xffff0000, v32\n"
    "v_lshlrev_b32 v18, 16, v33\n"
    "v_and_b32 v19, 0xffff0000, v33\n"
    "s_waitcnt vmcnt(6)\n"
    "v_lshlrev_b32 v12, 16, v34\n"
    "v_and_b32 v13, 0xffff0000, v34\n"
    "v_pk_add_f32 v[16:17], v[16:17], v[12:13]\n"
    "v_lshlrev_b32 v14, 16, v35\n"
    "v_and_b32 v15, 0xffff0000, v35\n"
    "v_pk_add_f32 v[18:19], v[18:19], v[14:15]\n"
    "s_waitcnt vmcnt(5)\n"
    "v_lshlrev_b32 v12, 16, v36\n"
    "v_and_b32 v13, 0xffff0000, v36\n"
    "v_pk_add_f32 v[16:17], v[16:17], v[12:13]\n"
    "v_lshlrev_b32 v14, 16, v37\n"
    "v_and_b32 v15, 0xffff0000, v37\n"
    "v_pk_add_f32 v[18:19], v[18:19], v[14:15]\n"
    "s_waitcnt vmcnt(4)\n"
    "v_lshlrev_b32 v12, 16, v38\n"
    "v_and_b32 v13, 0xffff0000, v38\n"
    "v_pk_add_f32 v[16:17], v[16:17], v[12:13]\n"
    "v_lshlrev_b32 v14, 16, v39\n"
    "v_and_b32 v15, 0xffff0000, v39\n"
    "v_pk_add_f32 v[18:19], v[18:19], v[14:15]\n"
    "s_waitcnt vmcnt(3)\n"
    "v_lshlrev_b32 v12, 16, v40\n"
    "v_and_b32 v13, 0xffff0000, v40\n"
    "v_pk_add_f32 v[16:17], v[16:17], v[12:13]\n"
    "v_lshlrev_b32 v14, 16, v41\n"
    "v_and_b32 v15, 0xffff0000, v41\n"
    "v_pk_add_f32 v[18:19], v[18:19], v[14:15]\n"
    "s_waitcnt vmcnt(2)\n"
    "v_lshlrev_b32 v12, 16, v42\n"
    "v_and_b32 v13, 0xffff0000, v42\n"
    "v_pk_add_f32 v[16:17], v[16:17], v[12:13]\n"
    "v_lshlrev_b32 v14, 16, v43\n"
    "v_and_b32 v15, 0xffff0000, v43\n"
    "v_pk_add_f32 v[18:19], v[18:19], v[14:15]\n"
    "s_waitcnt vmcnt(1)\n"
    "v_lshlrev_b32 v12, 16, v44\n"
    "v_and_b32 v13, 0xffff0000, v44\n"
    "v_pk_add_f32 v[16:17], v[16:17], v[12:13]\n"
    "v_lshlrev_b32 v14, 16, v45\n"
    "v_and_b32 v15, 0xffff0000, v45\n"
    "v_pk_add_f32 v[18:19], v[18:19], v[14:15]\n"
    "s_waitcnt vmcnt(0)\n"
    "v_lshlrev_b32 v12, 16, v46\n"
    "v_and_b32 v13, 0xffff0000, v46\n"
    "v_pk_add_f32 v[16:17], v[16:17], v[12:13]\n"
    "v_lshlrev_b32 v14, 16, v47\n"
    "v_and_b32 v15, 0xffff0000, v47\n"
    "v_pk_add_f32 v[18:19], v[18:19], v[14:15]\n"
    "v_cvt_pk_bf16_f32 v24, v16, v17\n"
    "v_cvt_pk_bf16_f32 v25, v18, v19\n"
    "ds_bpermute_b32 v26, v6, v24\n"
    "ds_bpermute_b32 v27, v6, v25\n"
    "s_waitcnt lgkmcnt(0)\n"
    "buffer_store_dwordx2 v[26:27], v2, s[52:55], s18 offen\n"
    "RF_%=:\n"
    "s_endpgm\n"
    "LC%=:\n"
    "s_setprio 3\n"
    "s_barrier\n"
    "s_lshr_b32 s60, s59, 2\n"
    "s_and_b32 s63, s62, 0xfc\n"
    "s_xor_b32 s70, s58, s61\n"
    "s_and_b32 s70, s70, 1\n"
    "s_lshr_b32 s71, s58, 1\n"
    "s_mov_b32 s95, 2000000\n"
    "s_lshl_b32 s5, s9, 9\n"
    "s_add_u32 s5, s5, 155648\n"
    "s_getpc_b64 s[34:35]\n"
    "TPCC_%=:\n"
    "s_add_u32 s34, s34, TAB_%=-TPCC_%=\n"
    "s_addc_u32 s35, s35, 0\n"
    "s_sub_u32 s10, s9, 4\n"
    "v_and_b32 v7, 1, v0\n"
    "v_lshlrev_b32 v7, 9, v7\n"
    "v_add_u32 v7, 155900, v7\n"
    "s_mov_b32 m0, s5\n"
    "s_mov_b32 s88, s34\n"
    "s_and_b32 s89, s35, 0xffff\n"
    "s_mov_b32 s90, 256\n"
    "s_mov_b32 s91, 0x20000\n"
    "v_lshrrev_b32 v13, 4, v0\n"
    "v_lshlrev_b32 v1, 10, v13\n"
    "v_bfe_u32 v14, v0, 2, 2\n"
    "v_lshl_add_u32 v1, v14, 6, v1\n"
    "v_and_b32 v14, 1, v13\n"
    "v_lshl_add_u32 v1, v14, 5, v1\n"
    "v_and_b32 v15, 3, v0\n"
    "v_lshl_add_u32 v1, v15, 3, v1\n"
    "v_add_u32 v2, 0x10000, v1\n"
    "v_add_u32 v3, 0x20000, v1\n"
    "v_xor_b32 v4, 32, v1\n"
    "v_xor_b32 v5, 32, v2\n"
    "v_xor_b32 v6, 32, v3\n"
    "v_bfe_u32 v15, v0, 3, 1\n"
    "v_and_b32 v8, 7, v0\n"
    "v_lshlrev_b32 v8, 6, v8\n"
    "v_lshl_add_u32 v8, v15, 10, v8\n"
    "s_lshl_b32 s11, s10, 12\n"
    "v_add_u32 v8, s11, v8\n"
    "v_lshlrev_b32 v15, 1, v15\n"
    "v_lshrrev_b32 v12, 1, v13\n"
    "v_xor_b32 v12, v12, v15\n"
    "v_lshl_add_u32 v12, v12, 4, v8\n"
    "v_lshl_add_u32 v12, v14, 3, v12\n"
    "v_xor_b32 v13, v13, v15\n"
    "v_lshl_add_u32 v8, v13, 4, v8\n"
    "v_lshlrev_b32 v11, 4, v0\n"
    "s_waitcnt lgkmcnt(0)\n"
    "s_lshl_b32 s11, s56, 2\n"
    "s_add_u32 s11, s11, s58\n"
    "s_lshl_b32 s11, s11, 21\n"
    "s_add_u32 s26, s26, s11\n"
    "s_addc_u32 s27, s27, 0\n"
    "s_load_dwordx2 s[6:7], s[26:27], 0\n"
    "s_mov_b32 s48, s26\n"
    "s_and_b32 s49, s27, 0xffff\n"
    "s_mov_b32 s50, 2097152\n"
    "s_mov_b32 s51, 0x20000\n"
    "s_lshl_b32 s75, s57, 1\n"
    "s_add_u32 s75, s75, s60\n"
    "s_lshl_b32 s12, s10, 5\n"
    "s_add_u32 s13, s12, s62\n"
    "s_lshl_b32 s14, s13, 11\n"
    "s_add_u32 s14, s14, 262144\n"
    "s_lshl_b32 s15, s13, 7\n"
    "s_add_u32 s15, s15, 4096\n"
    "s_add_u32 s12, s12, s63\n"
    "s_lshl_b32 s16, s12, 7\n"
    "s_add_u32 s16, s16, 4096\n"
    "s_xor_b32 s13, s70, 0\n"
    "s_add_u32 s13, s13, s12\n"
    "s_lshl_b32 s17, s13, 11\n"
    "s_add_u32 s17, s17, 262144\n"
    "s_xor_b32 s13, s70, 1\n"
    "s_add_u32 s13, s13, s12\n"
    "s_lshl_b32 s18, s13, 11\n"
    "s_add_u32 s18, s18, 262144\n"
    "s_xor_b32 s13, s70, 2\n"
    "s_add_u32 s13, s13, s12\n"
    "s_lshl_b32 s19, s13, 11\n"
    "s_add_u32 s19, s19, 262144\n"
    "s_xor_b32 s13, s70, 3\n"
    "s_add_u32 s13, s13, s12\n"
    "s_lshl_b32 s20, s13, 11\n"
    "s_add_u32 s20, s20, 262144\n"
    "s_lshl_b32 s13, s61, 3\n"
    "s_lshl_b32 s21, s10, 1\n"
    "s_add_u32 s13, s13, s21\n"
    "s_lshl_b32 s22, s13, 13\n"
    "s_lshl_b32 s72, s75, 10\n"
    "s_add_u32 s22, s22, s72\n"
    "s_add_u32 s22, s22, 524288\n"
    "s_lshl_b32 s23, s13, 10\n"
    "s_lshl_b32 s73, s75, 7\n"
    "s_add_u32 s23, s23, s73\n"
    "s_add_u32 s23, s23, 98304\n"
    "s_and_b32 s13, s61, 2\n"
    "s_lshl_b32 s13, s13, 3\n"
    "s_add_u32 s13, s13, s21\n"
    "s_and_b32 s72, s61, 1\n"
    "s_lshl_b32 s73, s75, 1\n"
    "s_add_u32 s72, s72, s73\n"
    "s_lshl_b32 s52, s13, 14\n"
    "s_lshl_b32 s73, s72, 10\n"
    "s_add_u32 s52, s52, s73\n"
    "s_add_u32 s52, s52, 1572864\n"
    "s_lshl_b32 s53, s13, 11\n"
    "s_lshl_b32 s73, s72, 7\n"
    "s_add_u32 s53, s53, s73\n"
    "s_add_u32 s53, s53, 32768\n"
    "s_waitcnt vmcnt(0) lgkmcnt(0)\n"
    "s_mul_i32 s6, s6, 0x9e3779b1\n"
    "s_add_u32 s6, s6, 0x7f4a7c15\n"
    "s_mul_i32 s7, s7, 0x85ebca6d\n"
    "s_add_u32 s7, s7, 0x2545f491\n"
    "ds_read_b32 v9, v7\n"
    "s_waitcnt lgkmcnt(0)\n"
    "v_cmp_gt_u32 vcc, 0xb0000001, v9\n"
    "s_cbranch_vccnz CS1_%=\n"
    "CB1_%=:\n"
    "v_add_u32 v10, 0, v8\n"
    "ds_read_b128 v[112:115], v10 offset:0\n"
    "ds_read_b128 v[116:119], v10 offset:2048\n"
    "ds_read_b64_tr_b16 v[64:65], v3 offset:16384\n"
    "ds_read_b64_tr_b16 v[66:67], v3 offset:16640\n"
    "ds_read_b64_tr_b16 v[68:69], v6 offset:16384\n"
    "ds_read_b64_tr_b16 v[70:71], v6 offset:16640\n"
    "ds_read_b64_tr_b16 v[72:73], v3 offset:16896\n"
    "ds_read_b64_tr_b16 v[74:75], v3 offset:17152\n"
    "ds_read_b64_tr_b16 v[76:77], v6 offset:16896\n"
    "ds_read_b64_tr_b16 v[78:79], v6 offset:17152\n"
    "ds_read_b64_tr_b16 v[80:81], v1 offset:16384\n"
    "ds_read_b64_tr_b16 v[82:83], v1 offset:16640\n"
    "ds_read_b64_tr_b16 v[84:85], v4 offset:16384\n"
    "ds_read_b64_tr_b16 v[86:87], v4 offset:16640\n"
    "s_waitcnt lgkmcnt(8)\n"
    "v_mfma_f32_16x16x32_bf16 v[128:131], v[64:67], v[112:115], 0\n"
    "ds_read_b64_tr_b16 v[88:89], v1 offset:16896\n"
    "ds_read_b128 v[120:123], v10 offset:512\n"
    "v_mfma_f32_16x16x32_bf16 v[136:139], v[64:67], v[116:119], 0\n"
    "ds_read_b64_tr_b16 v[90:91], v1 offset:17152\n"
    "ds_read_b128 v[124:127], v10 offset:2560\n"
    "s_nop 3\n"
    "v_mfma_f32_16x16x32_bf16 v[132:135], v[68:71], v[112:115], 0\n"
    "ds_read_b64_tr_b16 v[92:93], v4 offset:16896\n"
    "v_mfma_f32_16x16x32_bf16 v[140:143], v[68:71], v[116:119], 0\n"
    "ds_read_b64_tr_b16 v[94:95], v4 offset:17152\n"
    "s_nop 3\n"
    "s_waitcnt lgkmcnt(10)\n"
    "v_mfma_f32_16x16x32_bf16 v[144:147], v[72:75], v[112:115], 0\n"
    "ds_read_b64_tr_b16 v[96:97], v1 offset:24576\n"
    "v_mfma_f32_16x16x32_bf16 v[152:155], v[72:75], v[116:119], 0\n"
    "ds_read_b64_tr_b16 v[98:99], v1 offset:24832\n"
    "s_nop 3\n"
    "v_mfma_f32_16x16x32_bf16 v[148:151], v[76:79], v[112:115], 0\n"
    "ds_read_b64_tr_b16 v[100:101], v4 offset:24576\n"
    "v_mfma_f32_16x16x32_bf16 v[156:159], v[76:79], v[116:119], 0\n"
    "ds_read_b64_tr_b16 v[102:103], v4 offset:24832\n"
    "s_nop 3\n"
    "s_waitcnt lgkmcnt(10)\n"
    "v_mfma_f32_16x16x32_bf16 v[160:163], v[80:83], v[112:115], 0\n"
    "ds_read_b64_tr_b16 v[104:105], v1 offset:25088\n"
    "v_mfma_f32_16x16x32_bf16 v[168:171], v[80:83], v[116:119], 0\n"
    "ds_read_b64_tr_b16 v[106:107], v1 offset:25344\n"
    "s_nop 3\n"
    "v_mfma_f32_16x16x32_bf16 v[164:167], v[84:87], v[112:115], 0\n"
    "ds_read_b64_tr_b16 v[108:109], v4 offset:25088\n"
    "v_mfma_f32_16x16x32_bf16 v[172:175], v[84:87], v[116:119], 0\n"
    "ds_read_b64_tr_b16 v[110:111], v4 offset:25344\n"
    "s_nop 3\n"
    "s_waitcnt lgkmcnt(8)\n"
    "v_mfma_f32_16x16x32_bf16 v[176:179], v[88:91], v[112:115], 0\n"
    "ds_read_b64_tr_b16 v[64:65], v3 offset:20480\n"
    "buffer_load_dword off, s[88:91], 0 offset:4 lds\n"
    "v_mfma_f32_16x16x32_bf16 v[184:187], v[88:91], v[116:119], 0\n"
    "ds_read_b64_tr_b16 v[66:67], v3 offset:20736\n"
    "s_nop 3\n"
    "v_mfma_f32_16x16x32_bf16 v[180:183], v[92:95], v[112:115], 0\n"
    "ds_read_b64_tr_b16 v[68:69], v6 offset:20480\n"
    "v_mfma_f32_16x16x32_bf16 v[188:191], v[92:95], v[116:119], 0\n"
    "ds_read_b64_tr_b16 v[70:71], v6 offset:20736\n"
    "s_nop 3\n"
    "s_waitcnt lgkmcnt(8)\n"
    "v_mfma_f32_16x16x32_bf16 v[192:195], v[96:99], v[112:115], 0\n"
    "ds_read_b64_tr_b16 v[72:73], v3 offset:20992\n"
    "v_mfma_f32_16x16x32_bf16 v[200:203], v[96:99], v[116:119], 0\n"
    "ds_read_b64_tr_b16 v[74:75], v3 offset:21248\n"
    "s_nop 3\n"
    "v_mfma_f32_16x16x32_bf16 v[196:199], v[100:103], v[112:115], 0\n"
    "ds_read_b64_tr_b16 v[76:77], v6 offset:20992\n"
    "v_mfma_f32_16x16x32_bf16 v[204:207], v[100:103], v[116:119], 0\n"
    "ds_read_b64_tr_b16 v[78:79], v6 offset:21248\n"
    "s_nop 3\n"
    "s_waitcnt lgkmcnt(8)\n"
    "v_mfma_f32_16x16x32_bf16 v[208:211], v[104:107], v[112:115], 0\n"
    "ds_read_b64_tr_b16 v[80:81], v1 offset:20480\n"
    "v_mfma_f32_16x16x32_bf16 v[216:219], v[104:107], v[116:119], 0\n"
    "ds_read_b64_tr_b16 v[82:83], v1 offset:20736\n"
    "s_nop 3\n"
    "v_mfma_f32_16x16x32_bf16 v[212:215], v[108:111], v[112:115], 0\n"
    "ds_read_b64_tr_b16 v[84:85], v4 offset:20480\n"
    "ds_read_b32 v9, v7\n"
    "v_mfma_f32_16x16x32_bf16 v[220:223], v[108:111], v[116:119], 0\n"
    "ds_read_b64_tr_b16 v[86:87], v4 offset:20736\n"
    "s_nop 3\n"
    "s_waitcnt lgkmcnt(9)\n"
    "v_mfma_f32_16x16x32_bf16 v[128:131], v[64:67], v[120:123], v[128:131]\n"
    "ds_read_b64_tr_b16 v[88:89], v1 offset:20992\n"
    "v_mfma_f32_16x16x32_bf16 v[136:139], v[64:67], v[124:127], v[136:139]\n"
    "ds_read_b64_tr_b16 v[90:91], v1 offset:21248\n"
    "s_nop 3\n"
    "v_mfma_f32_16x16x32_bf16 v[132:135], v[68:71], v[120:123], v[132:135]\n"
    "ds_read_b64_tr_b16 v[92:93], v4 offset:20992\n"
    "v_mfma_f32_16x16x32_bf16 v[140:143], v[68:71], v[124:127], v[140:143]\n"
    "ds_read_b64_tr_b16 v[94:95], v4 offset:21248\n"
    "s_nop 3\n"
    "s_waitcnt lgkmcnt(9)\n"
    "v_mfma_f32_16x16x32_bf16 v[144:147], v[72:75], v[120:123], v[144:147]\n"
    "ds_read_b64_tr_b16 v[96:97], v1 offset:28672\n"
    "v_mfma_f32_16x16x32_bf16 v[152:155], v[72:75], v[124:127], v[152:155]\n"
    "ds_read_b64_tr_b16 v[98:99], v1 offset:28928\n"
    "s_nop 3\n"
    "v_mfma_f32_16x16x32_bf16 v[148:151], v[76:79], v[120:123], v[148:151]\n"
    "ds_read_b64_tr_b16 v[100:101], v4 offset:28672\n"
    "v_mfma_f32_16x16x32_bf16 v[156:159], v[76:79], v[124:127], v[156:159]\n"
    "ds_read_b64_tr_b16 v[102:103], v4 offset:28928\n"
    "s_nop 3\n"
    "s_waitcnt lgkmcnt(8)\n"
    "v_mfma_f32_16x16x32_bf16 v[160:163], v[80:83], v[120:123], v[160:163]\n"
    "ds_read_b64_tr_b16 v[104:105], v1 offset:29184\n"
    "v_mfma_f32_16x16x32_bf16 v[168:171], v[80:83], v[124:127], v[168:171]\n"
    "ds_read_b64_tr_b16 v[106:107], v1 offset:29440\n"
    "s_nop 3\n"
    "v_mfma_f32_16x16x32_bf16 v[164:167], v[84:87], v[120:123], v[164:167]\n"
    "ds_read_b64_tr_b16 v[108:109], v4 offset:29184\n"
    "v_mfma_f32_16x16x32_bf16 v[172:175], v[84:87], v[124:127], v[172:175]\n"
    "ds_read_b64_tr_b16 v[110:111], v4 offset:29440\n"
    "v_cmp_gt_u32 vcc, 0xb0000002, v9\n"
    "s_nop 3\n"
    "s_waitcnt lgkmcnt(8)\n"
    "v_mfma_f32_16x16x32_bf16 v[176:179], v[88:91], v[120:123], v[176:179]\n"
    "s_cbranch_vccnz CS2_%=\n"
    "CB2_%=:\n"
    "ds_read_b64_tr_b16 v[64:65], v1 offset:49152\n"
    "v_mfma_f32_16x16x32_bf16 v[184:187], v[88:91], v[124:127], v[184:187]\n"
    "ds_read_b64_tr_b16 v[66:67], v1 offset:49408\n"
    "v_add_u32 v10, 32768, v8\n"
    "s_nop 3\n"
    "v_mfma_f32_16x16x32_bf16 v[180:183], v[92:95], v[120:123], v[180:183]\n"
    "ds_read_b64_tr_b16 v[68:69], v4 offset:49152\n"
    "ds_read_b128 v[224:227], v10 offset:0\n"
    "v_mfma_f32_16x16x32_bf16 v[188:191], v[92:95], v[124:127], v[188:191]\n"
    "ds_read_b64_tr_b16 v[70:71], v4 offset:49408\n"
    "s_nop 3\n"
    "s_waitcnt lgkmcnt(9)\n"
    "v_mfma_f32_16x16x32_bf16 v[192:195], v[96:99], v[120:123], v[192:195]\n"
    "ds_read_b64_tr_b16 v[72:73], v1 offset:49664\n"
    "ds_read_b128 v[228:231], v10 offset:2048\n"
    "v_mfma_f32_16x16x32_bf16 v[200:203], v[96:99], v[124:127], v[200:203]\n"
    "ds_read_b64_tr_b16 v[74:75], v1 offset:49920\n"
    "ds_read_b128 v[232:235], v10 offset:512\n"
    "s_nop 3\n"
    "v_mfma_f32_16x16x32_bf16 v[196:199], v[100:103], v[120:123], v[196:199]\n"
    "ds_read_b64_tr_b16 v[76:77], v4 offset:49664\n"
    "ds_read_b128 v[236:239], v10 offset:2560\n"
    "v_mfma_f32_16x16x32_bf16 v[204:207], v[100:103], v[124:127], v[204:207]\n"
    "s_waitcnt lgkmcnt(14)\n"
    "ds_read_b64_tr_b16 v[78:79], v4 offset:49920\n"
    "s_nop 3\n"
    "s_waitcnt lgkmcnt(12)\n"
    "v_mfma_f32_16x16x32_bf16 v[208:211], v[104:107], v[120:123], v[208:211]\n"
    "ds_read_b64_tr_b16 v[80:81], v1 offset:57344\n"
    "buffer_load_dword off, s[88:91], 0 offset:8 lds\n"
    "v_mfma_f32_16x16x32_bf16 v[216:219], v[104:107], v[124:127], v[216:219]\n"
    "ds_read_b64_tr_b16 v[82:83], v1 offset:57600\n"
    "s_nop 3\n"
    "v_mfma_f32_16x16x32_bf16 v[212:215], v[108:111], v[120:123], v[212:215]\n"
    "ds_read_b64_tr_b16 v[84:85], v4 offset:57344\n"
    "v_mfma_f32_16x16x32_bf16 v[220:223], v[108:111], v[124:127], v[220:223]\n"
    "s_waitcnt lgkmcnt(14)\n"
    "ds_read_b64_tr_b16 v[86:87], v4 offset:57600\n"
    "s_nop 3\n"
    "s_waitcnt lgkmcnt(11)\n"
    "v_mfma_f32_16x16x32_bf16 v[128:131], v[64:67], v[224:227], v[128:131]\n"
    "ds_read_b64_tr_b16 v[88:89], v1 offset:57856\n"
    "s_waitcnt lgkmcnt(10)\n"
    "v_mfma_f32_16x16x32_bf16 v[136:139], v[64:67], v[228:231], v[136:139]\n"
    "ds_read_b64_tr_b16 v[90:91], v1 offset:58112\n"
    "s_nop 3\n"
    "v_mfma_f32_16x16x32_bf16 v[132:135], v[68:71], v[224:227], v[132:135]\n"
    "ds_read_b64_tr_b16 v[92:93], v4 offset:57856\n"
    "v_mfma_f32_16x16x32_bf16 v[140:143], v[68:71], v[228:231], v[140:143]\n"
    "ds_read_b64_tr_b16 v[94:95], v4 offset:58112\n"
    "s_nop 3\n"
    "s_waitcnt lgkmcnt(8)\n"
    "v_mfma_f32_16x16x32_bf16 v[144:147], v[72:75], v[224:227], v[144:147]\n"
    "ds_read_b64_tr_b16 v[96:97], v2 offset:0\n"
    "v_mfma_f32_16x16x32_bf16 v[152:155], v[72:75], v[228:231], v[152:155]\n"
    "ds_read_b64_tr_b16 v[98:99], v2 offset:256\n"
    "s_nop 3\n"
    "v_mfma_f32_16x16x32_bf16 v[148:151], v[76:79], v[224:227], v[148:151]\n"
    "ds_read_b64_tr_b16 v[100:101], v5 offset:0\n"
    "buffer_load_dword off, s[88:91], 0 offset:12 lds\n"
    "v_mfma_f32_16x16x32_bf16 v[156:159], v[76:79], v[228:231], v[156:159]\n"
    "ds_read_b64_tr_b16 v[102:103], v5 offset:256\n"
    "s_nop 3\n"
    "s_waitcnt lgkmcnt(8)\n"
    "v_mfma_f32_16x16x32_bf16 v[160:163], v[80:83], v[224:227], v[160:163]\n"
    "ds_read_b64_tr_b16 v[104:105], v2 offset:512\n"
    "v_mfma_f32_16x16x32_bf16 v[168:171], v[80:83], v[228:231], v[168:171]\n"
    "ds_read_b64_tr_b16 v[106:107], v2 offset:768\n"
    "s_nop 3\n"
    "v_mfma_f32_16x16x32_bf16 v[164:167], v[84:87], v[224:227], v[164:167]\n"
    "ds_read_b64_tr_b16 v[108:109], v5 offset:512\n"
    "v_mfma_f32_16x16x32_bf16 v[172:175], v[84:87], v[228:231], v[172:175]\n"
    "ds_read_b64_tr_b16 v[110:111], v5 offset:768\n"
    "s_nop 3\n"
    "s_waitcnt lgkmcnt(8)\n"
    "v_mfma_f32_16x16x32_bf16 v[176:179], v[88:91], v[224:227], v[176:179]\n"
    "ds_read_b64_tr_b16 v[64:65], v1 offset:53248\n"
    "v_mfma_f32_16x16x32_bf16 v[184:187], v[88:91], v[228:231], v[184:187]\n"
    "ds_read_b64_tr_b16 v[66:67], v1 offset:53504\n"
    "s_nop 3\n"
    "v_mfma_f32_16x16x32_bf16 v[180:183], v[92:95], v[224:227], v[180:183]\n"
    "ds_read_b64_tr_b16 v[68:69], v4 offset:53248\n"
    "v_mfma_f32_16x16x32_bf16 v[188:191], v[92:95], v[228:231], v[188:191]\n"
    "ds_read_b64_tr_b16 v[70:71], v4 offset:53504\n"
    "s_nop 3\n"
    "s_waitcnt lgkmcnt(8)\n"
    "v_mfma_f32_16x16x32_bf16 v[192:195], v[96:99], v[224:227], v[192:195]\n"
    "ds_read_b64_tr_b16 v[72:73], v1 offset:53760\n"
    "v_mfma_f32_16x16x32_bf16 v[200:203], v[96:99], v[228:231], v[200:203]\n"
    "ds_read_b64_tr_b16 v[74:75], v1 offset:54016\n"
    "s_nop 3\n"
    "v_mfma_f32_16x16x32_bf16 v[196:199], v[100:103], v[224:227], v[196:199]\n"
    "ds_read_b64_tr_b16 v[76:77], v4 offset:53760\n"
    "v_mfma_f32_16x16x32_bf16 v[204:207], v[100:103], v[228:231], v[204:207]\n"
    "ds_read_b64_tr_b16 v[78:79], v4 offset:54016\n"
    "s_nop 3\n"
    "s_waitcnt lgkmcnt(8)\n"
    "v_mfma_f32_16x16x32_bf16 v[208:211], v[104:107], v[224:227], v[208:211]\n"
    "ds_read_b64_tr_b16 v[80:81], v1 offset:61440\n"
    "v_mfma_f32_16x16x32_bf16 v[216:219], v[104:107], v[228:231], v[216:219]\n"
    "ds_read_b64_tr_b16 v[82:83], v1 offset:61696\n"
    "s_nop 3\n"
    "v_mfma_f32_16x16x32_bf16 v[212:215], v[108:111], v[224:227], v[212:215]\n"
    "ds_read_b64_tr_b16 v[84:85], v4 offset:61440\n"
    "ds_read_b32 v9, v7\n"
    "v_mfma_f32_16x16x32_bf16 v[220:223], v[108:111], v[228:231], v[220:223]\n"
    "ds_read_b64_tr_b16 v[86:87], v4 offset:61696\n"
    "s_nop 3\n"
    "s_waitcnt lgkmcnt(9)\n"
    "v_mfma_f32_16x16x32_bf16 v[128:131], v[64:67], v[232:235], v[128:131]\n"
    "ds_read_b64_tr_b16 v[88:89], v1 offset:61952\n"
    "v_mfma_f32_16x16x32_bf16 v[136:139], v[64:67], v[236:239], v[136:139]\n"
    "ds_read_b64_tr_b16 v[90:91], v1 offset:62208\n"
    "s_nop 3\n"
    "v_mfma_f32_16x16x32_bf16 v[132:135], v[68:71], v[232:235], v[132:135]\n"
    "ds_read_b64_tr_b16 v[92:93], v4 offset:61952\n"
    "v_mfma_f32_16x16x32_bf16 v[140:143], v[68:71], v[236:239], v[140:143]\n"
    "ds_read_b64_tr_b16 v[94:95], v4 offset:62208\n"
    "s_nop 3\n"
    "s_waitcnt lgkmcnt(9)\n"
    "v_mfma_f32_16x16x32_bf16 v[144:147], v[72:75], v[232:235], v[144:147]\n"
    "ds_read_b64_tr_b16 v[96:97], v2 offset:4096\n"
    "v_mfma_f32_16x16x32_bf16 v[152:155], v[72:75], v[236:239], v[152:155]\n"
    "ds_read_b64_tr_b16 v[98:99], v2 offset:4352\n"
    "s_nop 3\n"
    "v_mfma_f32_16x16x32_bf16 v[148:151], v[76:79], v[232:235], v[148:151]\n"
    "ds_read_b64_tr_b16 v[100:101], v5 offset:4096\n"
    "v_mfma_f32_16x16x32_bf16 v[156:159], v[76:79], v[236:239], v[156:159]\n"
    "ds_read_b64_tr_b16 v[102:103], v5 offset:4352\n"
    "s_nop 3\n"
    "s_waitcnt lgkmcnt(8)\n"
    "v_mfma_f32_16x16x32_bf16 v[160:163], v[80:83], v[232:235], v[160:163]\n"
    "ds_read_b64_tr_b16 v[104:105], v2 offset:4608\n"
    "v_mfma_f32_16x16x32_bf16 v[168:171], v[80:83], v[236:239], v[168:171]\n"
    "ds_read_b64_tr_b16 v[106:107], v2 offset:4864\n"
    "s_nop 3\n"
    "v_mfma_f32_16x16x32_bf16 v[164:167], v[84:87], v[232:235], v[164:167]\n"
    "ds_read_b64_tr_b16 v[108:109], v5 offset:4608\n"
    "v_mfma_f32_16x16x32_bf16 v[172:175], v[84:87], v[236:239], v[172:175]\n"
    "ds_read_b64_tr_b16 v[110:111], v5 offset:4864\n"
    "v_cmp_gt_u32 vcc, 0xb0000003, v9\n"
    "s_nop 3\n"
    "s_waitcnt lgkmcnt(8)\n"
    "v_mfma_f32_16x16x32_bf16 v[176:179], v[88:91], v[232:235], v[176:179]\n"
    "s_cbranch_vccnz CS3_%=\n"
    "CB3_%=:\n"
    "ds_read_b64_tr_b16 v[64:65], v2 offset:8192\n"
    "v_mfma_f32_16x16x32_bf16 v[184:187], v[88:91], v[236:239], v[184:187]\n"
    "ds_read_b64_tr_b16 v[66:67], v2 offset:8448\n"
    "v_add_u32 v10, 81920, v8\n"
    "s_nop 3\n"
    "v_mfma_f32_16x16x32_bf16 v[180:183], v[92:95], v[232:235], v[180:183]\n"
    "ds_read_b64_tr_b16 v[68:69], v5 offset:8192\n"
    "ds_read_b128 v[112:115], v10 offset:0\n"
    "v_mfma_f32_16x16x32_bf16 v[188:191], v[92:95], v[236:239], v[188:191]\n"
    "ds_read_b64_tr_b16 v[70:71], v5 offset:8448\n"
    "s_nop 3\n"
    "s_waitcnt lgkmcnt(9)\n"
    "v_mfma_f32_16x16x32_bf16 v[192:195], v[96:99], v[232:235], v[192:195]\n"
    "ds_read_b64_tr_b16 v[72:73], v2 offset:8704\n"
    "ds_read_b128 v[116:119], v10 offset:2048\n"
    "v_mfma_f32_16x16x32_bf16 v[200:203], v[96:99], v[236:239], v[200:203]\n"
    "ds_read_b64_tr_b16 v[74:75], v2 offset:8960\n"
    "ds_read_b128 v[120:123], v10 offset:512\n"
    "s_nop 3\n"
    "v_mfma_f32_16x16x32_bf16 v[196:199], v[100:103], v[232:235], v[196:199]\n"
    "ds_read_b64_tr_b16 v[76:77], v5 offset:8704\n"
    "ds_read_b128 v[124:127], v10 offset:2560\n"
    "v_mfma_f32_16x16x32_bf16 v[204:207], v[100:103], v[236:239], v[204:207]\n"
    "s_waitcnt lgkmcnt(14)\n"
    "ds_read_b64_tr_b16 v[78:79], v5 offset:8960\n"
    "s_nop 3\n"
    "s_waitcnt lgkmcnt(12)\n"
    "v_mfma_f32_16x16x32_bf16 v[208:211], v[104:107], v[232:235], v[208:211]\n"
    "ds_read_b64_tr_b16 v[80:81], v2 offset:32768\n"
    "buffer_load_dword off, s[88:91], 0 offset:16 lds\n"
    "v_mfma_f32_16x16x32_bf16 v[216:219], v[104:107], v[236:239], v[216:219]\n"
    "ds_read_b64_tr_b16 v[82:83], v2 offset:33024\n"
    "s_nop 3\n"
    "v_mfma_f32_16x16x32_bf16 v[212:215], v[108:111], v[232:235], v[212:215]\n"
    "ds_read_b64_tr_b16 v[84:85], v5 offset:32768\n"
    "v_mfma_f32_16x16x32_bf16 v[220:223], v[108:111], v[236:239], v[220:223]\n"
    "s_waitcnt lgkmcnt(14)\n"
    "ds_read_b64_tr_b16 v[86:87], v5 offset:33024\n"
    "s_nop 3\n"
    "s_waitcnt lgkmcnt(11)\n"
    "v_mfma_f32_16x16x32_bf16 v[128:131], v[64:67], v[112:115], v[128:131]\n"
    "ds_read_b64_tr_b16 v[88:89], v2 offset:33280\n"
    "s_waitcnt lgkmcnt(10)\n"
    "v_mfma_f32_16x16x32_bf16 v[136:139], v[64:67], v[116:119], v[136:139]\n"
    "ds_read_b64_tr_b16 v[90:91], v2 offset:33536\n"
    "s_nop 3\n"
    "v_mfma_f32_16x16x32_bf16 v[132:135], v[68:71], v[112:115], v[132:135]\n"
    "ds_read_b64_tr_b16 v[92:93], v5 offset:33280\n"
    "v_mfma_f32_16x16x32_bf16 v[140:143], v[68:71], v[116:119], v[140:143]\n"
    "ds_read_b64_tr_b16 v[94:95], v5 offset:33536\n"
    "s_nop 3\n"
    "s_waitcnt lgkmcnt(8)\n"
    "v_mfma_f32_16x16x32_bf16 v[144:147], v[72:75], v[112:115], v[144:147]\n"
    "ds_read_b64_tr_b16 v[96:97], v2 offset:40960\n"
    "v_mfma_f32_16x16x32_bf16 v[152:155], v[72:75], v[116:119], v[152:155]\n"
    "ds_read_b64_tr_b16 v[98:99], v2 offset:41216\n"
    "s_nop 3\n"
    "v_mfma_f32_16x16x32_bf16 v[148:151], v[76:79], v[112:115], v[148:151]\n"
    "ds_read_b64_tr_b16 v[100:101], v5 offset:40960\n"
    "buffer_load_dword off, s[88:91], 0 offset:20 lds\n"
    "v_mfma_f32_16x16x32_bf16 v[156:159], v[76:79], v[116:119], v[156:159]\n"
    "ds_read_b64_tr_b16 v[102:103], v5 offset:41216\n"
    "s_nop 3\n"
    "s_waitcnt lgkmcnt(8)\n"
    "v_mfma_f32_16x16x32_bf16 v[160:163], v[80:83], v[112:115], v[160:163]\n"
    "ds_read_b64_tr_b16 v[104:105], v2 offset:41472\n"
    "v_mfma_f32_16x16x32_bf16 v[168:171], v[80:83], v[116:119], v[168:171]\n"
    "ds_read_b64_tr_b16 v[106:107], v2 offset:41728\n"
    "s_nop 3\n"
    "v_mfma_f32_16x16x32_bf16 v[164:167], v[84:87], v[112:115], v[164:167]\n"
    "ds_read_b64_tr_b16 v[108:109], v5 offset:41472\n"
    "v_mfma_f32_16x16x32_bf16 v[172:175], v[84:87], v[116:119], v[172:175]\n"
    "ds_read_b64_tr_b16 v[110:111], v5 offset:41728\n"
    "s_nop 3\n"
    "s_waitcnt lgkmcnt(8)\n"
    "v_mfma_f32_16x16x32_bf16 v[176:179], v[88:91], v[112:115], v[176:179]\n"
    "ds_read_b64_tr_b16 v[64:65], v2 offset:12288\n"
    "v_mfma_f32_16x16x32_bf16 v[184:187], v[88:91], v[116:119], v[184:187]\n"
    "ds_read_b64_tr_b16 v[66:67], v2 offset:12544\n"
    "s_nop 3\n"
    "v_mfma_f32_16x16x32_bf16 v[180:183], v[92:95], v[112:115], v[180:183]\n"
    "ds_read_b64_tr_b16 v[68:69], v5 offset:12288\n"
    "v_mfma_f32_16x16x32_bf16 v[188:191], v[92:95], v[116:119], v[188:191]\n"
    "ds_read_b64_tr_b16 v[70:71], v5 offset:12544\n"
    "s_nop 3\n"
    "s_waitcnt lgkmcnt(8)\n"
    "v_mfma_f32_16x16x32_bf16 v[192:195], v[96:99], v[112:115], v[192:195]\n"
    "ds_read_b64_tr_b16 v[72:73], v2 offset:12800\n"
    "v_mfma_f32_16x16x32_bf16 v[200:203], v[96:99], v[116:119], v[200:203]\n"
    "ds_read_b64_tr_b16 v[74:75], v2 offset:13056\n"
    "s_nop 3\n"
    "v_mfma_f32_16x16x32_bf16 v[196:199], v[100:103], v[112:115], v[196:199]\n"
    "ds_read_b64_tr_b16 v[76:77], v5 offset:12800\n"
    "v_mfma_f32_16x16x32_bf16 v[204:207], v[100:103], v[116:119], v[204:207]\n"
    "ds_read_b64_tr_b16 v[78:79], v5 offset:13056\n"
    "s_nop 3\n"
    "s_waitcnt lgkmcnt(8)\n"
    "v_mfma_f32_16x16x32_bf16 v[208:211], v[104:107], v[112:115], v[208:211]\n"
    "ds_read_b64_tr_b16 v[80:81], v2 offset:36864\n"
    "v_mfma_f32_16x16x32_bf16 v[216:219], v[104:107], v[116:119], v[216:219]\n"
    "ds_read_b64_tr_b16 v[82:83], v2 offset:37120\n"
    "s_nop 3\n"
    "v_mfma_f32_16x16x32_bf16 v[212:215], v[108:111], v[112:115], v[212:215]\n"
    "ds_read_b64_tr_b16 v[84:85], v5 offset:36864\n"
    "ds_read_b32 v9, v7\n"
    "v_mfma_f32_16x16x32_bf16 v[220:223], v[108:111], v[116:119], v[220:223]\n"
    "ds_read_b64_tr_b16 v[86:87], v5 offset:37120\n"
    "s_nop 3\n"
    "s_waitcnt lgkmcnt(9)\n"
    "v_mfma_f32_16x16x32_bf16 v[128:131], v[64:67], v[120:123], v[128:131]\n"
    "ds_read_b64_tr_b16 v[88:89], v2 offset:37376\n"
    "v_mfma_f32_16x16x32_bf16 v[136:139], v[64:67], v[124:127], v[136:139]\n"
    "ds_read_b64_tr_b16 v[90:91], v2 offset:37632\n"
    "s_nop 3\n"
    "v_mfma_f32_16x16x32_bf16 v[132:135], v[68:71], v[120:123], v[132:135]\n"
    "ds_read_b64_tr_b16 v[92:93], v5 offset:37376\n"
    "v_mfma_f32_16x16x32_bf16 v[140:143], v[68:71], v[124:127], v[140:143]\n"
    "ds_read_b64_tr_b16 v[94:95], v5 offset:37632\n"
    "s_nop 3\n"
    "s_waitcnt lgkmcnt(9)\n"
    "v_mfma_f32_16x16x32_bf16 v[144:147], v[72:75], v[120:123], v[144:147]\n"
    "ds_read_b64_tr_b16 v[96:97], v2 offset:45056\n"
    "v_mfma_f32_16x16x32_bf16 v[152:155], v[72:75], v[124:127], v[152:155]\n"
    "ds_read_b64_tr_b16 v[98:99], v2 offset:45312\n"
    "s_nop 3\n"
    "v_mfma_f32_16x16x32_bf16 v[148:151], v[76:79], v[120:123], v[148:151]\n"
    "ds_read_b64_tr_b16 v[100:101], v5 offset:45056\n"
    "v_mfma_f32_16x16x32_bf16 v[156:159], v[76:79], v[124:127], v[156:159]\n"
    "ds_read_b64_tr_b16 v[102:103], v5 offset:45312\n"
    "s_nop 3\n"
    "s_waitcnt lgkmcnt(8)\n"
    "v_mfma_f32_16x16x32_bf16 v[160:163], v[80:83], v[120:123], v[160:163]\n"
    "ds_read_b64_tr_b16 v[104:105], v2 offset:45568\n"
    "v_mfma_f32_16x16x32_bf16 v[168:171], v[80:83], v[124:127], v[168:171]\n"
    "ds_read_b64_tr_b16 v[106:107], v2 offset:45824\n"
    "s_nop 3\n"
    "v_mfma_f32_16x16x32_bf16 v[164:167], v[84:87], v[120:123], v[164:167]\n"
    "ds_read_b64_tr_b16 v[108:109], v5 offset:45568\n"
    "v_mfma_f32_16x16x32_bf16 v[172:175], v[84:87], v[124:127], v[172:175]\n"
    "ds_read_b64_tr_b16 v[110:111], v5 offset:45824\n"
    "v_cmp_gt_u32 vcc, 0xb0000004, v9\n"
    "s_nop 3\n"
    "s_waitcnt lgkmcnt(8)\n"
    "v_mfma_f32_16x16x32_bf16 v[176:179], v[88:91], v[120:123], v[176:179]\n"
    "s_cbranch_vccnz CS4_%=\n"
    "CB4_%=:\n"
    "ds_read_b64_tr_b16 v[64:65], v1 offset:0\n"
    "v_mfma_f32_16x16x32_bf16 v[184:187], v[88:91], v[124:127], v[184:187]\n"
    "ds_read_b64_tr_b16 v[66:67], v1 offset:256\n"
    "v_add_u32 v10, 131072, v8\n"
    "s_nop 3\n"
    "v_mfma_f32_16x16x32_bf16 v[180:183], v[92:95], v[120:123], v[180:183]\n"
    "ds_read_b64_tr_b16 v[68:69], v4 offset:0\n"
    "ds_read_b128 v[224:227], v10 offset:0\n"
    "v_mfma_f32_16x16x32_bf16 v[188:191], v[92:95], v[124:127], v[188:191]\n"
    "ds_read_b64_tr_b16 v[70:71], v4 offset:256\n"
    "s_nop 3\n"
    "s_waitcnt lgkmcnt(9)\n"
    "v_mfma_f32_16x16x32_bf16 v[192:195], v[96:99], v[120:123], v[192:195]\n"
    "ds_read_b64_tr_b16 v[72:73], v1 offset:512\n"
    "ds_read_b128 v[228:231], v10 offset:2048\n"
    "v_mfma_f32_16x16x32_bf16 v[200:203], v[96:99], v[124:127], v[200:203]\n"
    "ds_read_b64_tr_b16 v[74:75], v1 offset:768\n"
    "ds_read_b128 v[232:235], v10 offset:512\n"
    "s_nop 3\n"
    "v_mfma_f32_16x16x32_bf16 v[196:199], v[100:103], v[120:123], v[196:199]\n"
    "ds_read_b64_tr_b16 v[76:77], v4 offset:512\n"
    "ds_read_b128 v[236:239], v10 offset:2560\n"
    "v_mfma_f32_16x16x32_bf16 v[204:207], v[100:103], v[124:127], v[204:207]\n"
    "s_waitcnt lgkmcnt(14)\n"
    "ds_read_b64_tr_b16 v[78:79], v4 offset:768\n"
    "s_nop 3\n"
    "s_waitcnt lgkmcnt(12)\n"
    "v_mfma_f32_16x16x32_bf16 v[208:211], v[104:107], v[120:123], v[208:211]\n"
    "ds_read_b64_tr_b16 v[80:81], v1 offset:8192\n"
    "buffer_load_dword off, s[88:91], 0 offset:24 lds\n"
    "v_mfma_f32_16x16x32_bf16 v[216:219], v[104:107], v[124:127], v[216:219]\n"
    "ds_read_b64_tr_b16 v[82:83], v1 offset:8448\n"
    "s_nop 3\n"
    "v_mfma_f32_16x16x32_bf16 v[212:215], v[108:111], v[120:123], v[212:215]\n"
    "ds_read_b64_tr_b16 v[84:85], v4 offset:8192\n"
    "v_mfma_f32_16x16x32_bf16 v[220:223], v[108:111], v[124:127], v[220:223]\n"
    "s_waitcnt lgkmcnt(14)\n"
    "ds_read_b64_tr_b16 v[86:87], v4 offset:8448\n"
    "s_nop 3\n"
    "s_waitcnt lgkmcnt(11)\n"
    "v_mfma_f32_16x16x32_bf16 v[128:131], v[64:67], v[224:227], v[128:131]\n"
    "ds_read_b64_tr_b16 v[88:89], v1 offset:8704\n"
    "v_add_u32 v13, 114688, v12\n"
    "s_waitcnt lgkmcnt(10)\n"
    "v_mfma_f32_16x16x32_bf16 v[136:139], v[64:67], v[228:231], v[136:139]\n"
    "ds_read_b64_tr_b16 v[90:91], v1 offset:8960\n"
    "v_xor_b32 v14, 32, v13\n"
    "s_nop 3\n"
    "v_mfma_f32_16x16x32_bf16 v[132:135], v[68:71], v[224:227], v[132:135]\n"
    "ds_read_b64_tr_b16 v[92:93], v4 offset:8704\n"
    "ds_read_b64 v[16:17], v13 offset:0\n"
    "v_mfma_f32_16x16x32_bf16 v[140:143], v[68:71], v[228:231], v[140:143]\n"
    "ds_read_b64_tr_b16 v[94:95], v4 offset:8960\n"
    "s_nop 3\n"
    "s_waitcnt lgkmcnt(9)\n"
    "v_mfma_f32_16x16x32_bf16 v[144:147], v[72:75], v[224:227], v[144:147]\n"
    "ds_read_b64_tr_b16 v[96:97], v3 offset:16384\n"
    "ds_read_b64 v[18:19], v14 offset:0\n"
    "v_mfma_f32_16x16x32_bf16 v[152:155], v[72:75], v[228:231], v[152:155]\n"
    "ds_read_b64_tr_b16 v[98:99], v3 offset:16640\n"
    "s_nop 3\n"
    "v_mfma_f32_16x16x32_bf16 v[148:151], v[76:79], v[224:227], v[148:151]\n"
    "ds_read_b64_tr_b16 v[100:101], v6 offset:16384\n"
    "buffer_load_dword off, s[88:91], 0 offset:28 lds\n"
    "v_mfma_f32_16x16x32_bf16 v[156:159], v[76:79], v[228:231], v[156:159]\n"
    "ds_read_b64_tr_b16 v[102:103], v6 offset:16640\n"
    "s_nop 3\n"
    "s_waitcnt lgkmcnt(10)\n"
    "v_mfma_f32_16x16x32_bf16 v[160:163], v[80:83], v[224:227], v[160:163]\n"
    "ds_read_b64_tr_b16 v[104:105], v3 offset:16896\n"
    "ds_read_b64 v[20:21], v13 offset:2048\n"
    "v_mfma_f32_16x16x32_bf16 v[168:171], v[80:83], v[228:231], v[168:171]\n"
    "ds_read_b64_tr_b16 v[106:107], v3 offset:17152\n"
    "ds_read_b64 v[22:23], v14 offset:2048\n"
    "s_nop 3\n"
    "v_mfma_f32_16x16x32_bf16 v[164:167], v[84:87], v[224:227], v[164:167]\n"
    "ds_read_b64_tr_b16 v[108:109], v6 offset:16896\n"
    "s_waitcnt lgkmcnt(14)\n"
    "ds_read_b64 v[24:25], v13 offset:512\n"
    "v_mfma_f32_16x16x32_bf16 v[172:175], v[84:87], v[228:231], v[172:175]\n"
    "s_waitcnt lgkmcnt(14)\n"
    "ds_read_b64_tr_b16 v[110:111], v6 offset:17152\n"
    "s_nop 3\n"
    "s_waitcnt lgkmcnt(12)\n"
    "v_mfma_f32_16x16x32_bf16 v[176:179], v[88:91], v[224:227], v[176:179]\n"
    "ds_read_b64_tr_b16 v[64:65], v1 offset:4096\n"
    "ds_read_b64 v[26:27], v14 offset:512\n"
    "v_mfma_f32_16x16x32_bf16 v[184:187], v[88:91], v[228:231], v[184:187]\n"
    "ds_read_b64_tr_b16 v[66:67], v1 offset:4352\n"
    "s_nop 3\n"
    "v_mfma_f32_16x16x32_bf16 v[180:183], v[92:95], v[224:227], v[180:183]\n"
    "s_waitcnt lgkmcnt(14)\n"
    "ds_read_b64_tr_b16 v[68:69], v4 offset:4096\n"
    "s_waitcnt lgkmcnt(14)\n"
    "ds_read_b64 v[28:29], v13 offset:2560\n"
    "v_mfma_f32_16x16x32_bf16 v[188:191], v[92:95], v[228:231], v[188:191]\n"
    "s_waitcnt lgkmcnt(14)\n"
    "ds_read_b64_tr_b16 v[70:71], v4 offset:4352\n"
    "s_nop 3\n"
    "s_waitcnt lgkmcnt(13)\n"
    "v_mfma_f32_16x16x32_bf16 v[192:195], v[96:99], v[224:227], v[192:195]\n"
    "ds_read_b64_tr_b16 v[72:73], v1 offset:4608\n"
    "ds_read_b64 v[30:31], v14 offset:2560\n"
    "v_mfma_f32_16x16x32_bf16 v[200:203], v[96:99], v[228:231], v[200:203]\n"
    "s_waitcnt lgkmcnt(14)\n"
    "ds_read_b64_tr_b16 v[74:75], v1 offset:4864\n"
    "s_nop 3\n"
    "v_mfma_f32_16x16x32_bf16 v[196:199], v[100:103], v[224:227], v[196:199]\n"
    "s_waitcnt lgkmcnt(14)\n"
    "ds_read_b64_tr_b16 v[76:77], v4 offset:4608\n"
    "v_mfma_f32_16x16x32_bf16 v[204:207], v[100:103], v[228:231], v[204:207]\n"
    "s_waitcnt lgkmcnt(14)\n"
    "ds_read_b64_tr_b16 v[78:79], v4 offset:4864\n"
    "s_nop 3\n"
    "s_waitcnt lgkmcnt(11)\n"
    "v_mfma_f32_16x16x32_bf16 v[208:211], v[104:107], v[224:227], v[208:211]\n"
    "ds_read_b64_tr_b16 v[80:81], v1 offset:12288\n"
    "v_mfma_f32_16x16x32_bf16 v[216:219], v[104:107], v[228:231], v[216:219]\n"
    "ds_read_b64_tr_b16 v[82:83], v1 offset:12544\n"
    "s_nop 3\n"
    "v_mfma_f32_16x16x32_bf16 v[212:215], v[108:111], v[224:227], v[212:215]\n"
    "ds_read_b64_tr_b16 v[84:85], v4 offset:12288\n"
    "ds_read_b32 v9, v7\n"
    "v_mfma_f32_16x16x32_bf16 v[220:223], v[108:111], v[228:231], v[220:223]\n"
    "s_waitcnt lgkmcnt(14)\n"
    "ds_read_b64_tr_b16 v[86:87], v4 offset:12544\n"
    "s_nop 3\n"
    "s_waitcnt lgkmcnt(10)\n"
    "v_mfma_f32_16x16x32_bf16 v[128:131], v[64:67], v[232:235], v[128:131]\n"
    "ds_read_b64_tr_b16 v[88:89], v1 offset:12800\n"
    "v_mfma_f32_16x16x32_bf16 v[136:139], v[64:67], v[236:239], v[136:139]\n"
    "ds_read_b64_tr_b16 v[90:91], v1 offset:13056\n"
    "s_nop 3\n"
    "v_mfma_f32_16x16x32_bf16 v[132:135], v[68:71], v[232:235], v[132:135]\n"
    "ds_read_b64_tr_b16 v[92:93], v4 offset:12800\n"
    "v_mfma_f32_16x16x32_bf16 v[140:143], v[68:71], v[236:239], v[140:143]\n"
    "ds_read_b64_tr_b16 v[94:95], v4 offset:13056\n"
    "s_nop 3\n"
    "s_waitcnt lgkmcnt(9)\n"
    "v_mfma_f32_16x16x32_bf16 v[144:147], v[72:75], v[232:235], v[144:147]\n"
    "ds_read_b64_tr_b16 v[96:97], v3 offset:20480\n"
    "v_mfma_f32_16x16x32_bf16 v[152:155], v[72:75], v[236:239], v[152:155]\n"
    "ds_read_b64_tr_b16 v[98:99], v3 offset:20736\n"
    "s_nop 3\n"
    "v_mfma_f32_16x16x32_bf16 v[148:151], v[76:79], v[232:235], v[148:151]\n"
    "ds_read_b64_tr_b16 v[100:101], v6 offset:20480\n"
    "v_mfma_f32_16x16x32_bf16 v[156:159], v[76:79], v[236:239], v[156:159]\n"
    "ds_read_b64_tr_b16 v[102:103], v6 offset:20736\n"
    "s_nop 3\n"
    "s_waitcnt lgkmcnt(8)\n"
    "v_mfma_f32_16x16x32_bf16 v[160:163], v[80:83], v[232:235], v[160:163]\n"
    "ds_read_b64_tr_b16 v[104:105], v3 offset:20992\n"
    "v_mfma_f32_16x16x32_bf16 v[168:171], v[80:83], v[236:239], v[168:171]\n"
    "ds_read_b64_tr_b16 v[106:107], v3 offset:21248\n"
    "s_nop 3\n"
    "v_mfma_f32_16x16x32_bf16 v[164:167], v[84:87], v[232:235], v[164:167]\n"
    "ds_read_b64_tr_b16 v[108:109], v6 offset:20992\n"
    "v_mfma_f32_16x16x32_bf16 v[172:175], v[84:87], v[236:239], v[172:175]\n"
    "ds_read_b64_tr_b16 v[110:111], v6 offset:21248\n"
    "v_cmp_gt_u32 vcc, 0xb0000005, v9\n"
    "s_nop 3\n"
    "s_waitcnt lgkmcnt(8)\n"
    "v_mfma_f32_16x16x32_bf16 v[176:179], v[88:91], v[232:235], v[176:179]\n"
    "s_cbranch_vccnz CS5_%=\n"
    "CB5_%=:\n"
    "ds_read_b64_tr_b16 v[64:65], v1 offset:32768\n"
    "v_mfma_f32_16x16x32_bf16 v[184:187], v[88:91], v[236:239], v[184:187]\n"
    "ds_read_b64_tr_b16 v[66:67], v1 offset:33024\n"
    "v_add_u32 v10, 16384, v8\n"
    "s_nop 3\n"
    "v_mfma_f32_16x16x32_bf16 v[180:183], v[92:95], v[232:235], v[180:183]\n"
    "ds_read_b64_tr_b16 v[68:69], v4 offset:32768\n"
    "ds_read_b128 v[112:115], v10 offset:0\n"
    "v_mfma_f32_16x16x32_bf16 v[188:191], v[92:95], v[236:239], v[188:191]\n"
    "ds_read_b64_tr_b16 v[70:71], v4 offset:33024\n"
    "s_nop 3\n"
    "s_waitcnt lgkmcnt(9)\n"
    "v_mfma_f32_16x16x32_bf16 v[192:195], v[96:99], v[232:235], v[192:195]\n"
    "ds_read_b64_tr_b16 v[72:73], v1 offset:33280\n"
    "ds_read_b128 v[116:119], v10 offset:2048\n"
    "v_mfma_f32_16x16x32_bf16 v[200:203], v[96:99], v[236:239], v[200:203]\n"
    "ds_read_b64_tr_b16 v[74:75], v1 offset:33536\n"
    "ds_read_b128 v[120:123], v10 offset:512\n"
    "s_nop 3\n"
    "v_mfma_f32_16x16x32_bf16 v[196:199], v[100:103], v[232:235], v[196:199]\n"
    "ds_read_b64_tr_b16 v[76:77], v4 offset:33280\n"
    "ds_read_b128 v[124:127], v10 offset:2560\n"
    "v_mfma_f32_16x16x32_bf16 v[204:207], v[100:103], v[236:239], v[204:207]\n"
    "s_waitcnt lgkmcnt(14)\n"
    "ds_read_b64_tr_b16 v[78:79], v4 offset:33536\n"
    "s_nop 3\n"
    "s_waitcnt lgkmcnt(12)\n"
    "v_mfma_f32_16x16x32_bf16 v[208:211], v[104:107], v[232:235], v[208:211]\n"
    "ds_read_b64_tr_b16 v[80:81], v1 offset:40960\n"
    "buffer_load_dword off, s[88:91], 0 offset:32 lds\n"
    "v_mfma_f32_16x16x32_bf16 v[216:219], v[104:107], v[236:239], v[216:219]\n"
    "ds_read_b64_tr_b16 v[82:83], v1 offset:41216\n"
    "s_nop 3\n"
    "v_mfma_f32_16x16x32_bf16 v[212:215], v[108:111], v[232:235], v[212:215]\n"
    "ds_read_b64_tr_b16 v[84:85], v4 offset:40960\n"
    "v_mfma_f32_16x16x32_bf16 v[220:223], v[108:111], v[236:239], v[220:223]\n"
    "s_waitcnt lgkmcnt(14)\n"
    "ds_read_b64_tr_b16 v[86:87], v4 offset:41216\n"
    "s_nop 3\n"
    "s_waitcnt lgkmcnt(11)\n"
    "v_mfma_f32_16x16x32_bf16 v[128:131], v[64:67], v[112:115], v[128:131]\n"
    "ds_read_b64_tr_b16 v[88:89], v1 offset:41472\n"
    "s_waitcnt lgkmcnt(10)\n"
    "v_mfma_f32_16x16x32_bf16 v[136:139], v[64:67], v[116:119], v[136:139]\n"
    "ds_read_b64_tr_b16 v[90:91], v1 offset:41728\n"
    "s_nop 3\n"
    "v_mfma_f32_16x16x32_bf16 v[132:135], v[68:71], v[112:115], v[132:135]\n"
    "ds_read_b64_tr_b16 v[92:93], v4 offset:41472\n"
    "v_mfma_f32_16x16x32_bf16 v[140:143], v[68:71], v[116:119], v[140:143]\n"
    "ds_read_b64_tr_b16 v[94:95], v4 offset:41728\n"
    "s_nop 3\n"
    "s_waitcnt lgkmcnt(8)\n"
    "v_mfma_f32_16x16x32_bf16 v[144:147], v[72:75], v[112:115], v[144:147]\n"
    "ds_read_b64_tr_b16 v[96:97], v2 offset:0\n"
    "v_mfma_f32_16x16x32_bf16 v[152:155], v[72:75], v[116:119], v[152:155]\n"
    "ds_read_b64_tr_b16 v[98:99], v2 offset:256\n"
    "s_nop 3\n"
    "v_mfma_f32_16x16x32_bf16 v[148:151], v[76:79], v[112:115], v[148:151]\n"
    "ds_read_b64_tr_b16 v[100:101], v5 offset:0\n"
    "buffer_load_dword off, s[88:91], 0 offset:36 lds\n"
    "v_mfma_f32_16x16x32_bf16 v[156:159], v[76:79], v[116:119], v[156:159]\n"
    "ds_read_b64_tr_b16 v[102:103], v5 offset:256\n"
    "s_nop 3\n"
    "s_waitcnt lgkmcnt(8)\n"
    "v_mfma_f32_16x16x32_bf16 v[160:163], v[80:83], v[112:115], v[160:163]\n"
    "ds_read_b64_tr_b16 v[104:105], v2 offset:512\n"
    "v_mfma_f32_16x16x32_bf16 v[168:171], v[80:83], v[116:119], v[168:171]\n"
    "ds_read_b64_tr_b16 v[106:107], v2 offset:768\n"
    "s_nop 3\n"
    "v_mfma_f32_16x16x32_bf16 v[164:167], v[84:87], v[112:115], v[164:167]\n"
    "ds_read_b64_tr_b16 v[108:109], v5 offset:512\n"
    "v_mfma_f32_16x16x32_bf16 v[172:175], v[84:87], v[116:119], v[172:175]\n"
    "ds_read_b64_tr_b16 v[110:111], v5 offset:768\n"
    "s_nop 3\n"
    "s_waitcnt lgkmcnt(8)\n"
    "v_mfma_f32_16x16x32_bf16 v[176:179], v[88:91], v[112:115], v[176:179]\n"
    "ds_read_b64_tr_b16 v[64:65], v1 offset:36864\n"
    "v_mfma_f32_16x16x32_bf16 v[184:187], v[88:91], v[116:119], v[184:187]\n"
    "ds_read_b64_tr_b16 v[66:67], v1 offset:37120\n"
    "s_nop 3\n"
    "v_mfma_f32_16x16x32_bf16 v[180:183], v[92:95], v[112:115], v[180:183]\n"
    "ds_read_b64_tr_b16 v[68:69], v4 offset:36864\n"
    "v_mfma_f32_16x16x32_bf16 v[188:191], v[92:95], v[116:119], v[188:191]\n"
    "ds_read_b64_tr_b16 v[70:71], v4 offset:37120\n"
    "s_nop 3\n"
    "s_waitcnt lgkmcnt(8)\n"
    "v_mfma_f32_16x16x32_bf16 v[192:195], v[96:99], v[112:115], v[192:195]\n"
    "ds_read_b64_tr_b16 v[72:73], v1 offset:37376\n"
    "v_mfma_f32_16x16x32_bf16 v[200:203], v[96:99], v[116:119], v[200:203]\n"
    "ds_read_b64_tr_b16 v[74:75], v1 offset:37632\n"
    "s_nop 3\n"
    "v_mfma_f32_16x16x32_bf16 v[196:199], v[100:103], v[112:115], v[196:199]\n"
    "ds_read_b64_tr_b16 v[76:77], v4 offset:37376\n"
    "v_mfma_f32_16x16x32_bf16 v[204:207], v[100:103], v[116:119], v[204:207]\n"
    "ds_read_b64_tr_b16 v[78:79], v4 offset:37632\n"
    "s_nop 3\n"
    "s_waitcnt lgkmcnt(8)\n"
    "v_mfma_f32_16x16x32_bf16 v[208:211], v[104:107], v[112:115], v[208:211]\n"
    "ds_read_b64_tr_b16 v[80:81], v1 offset:45056\n"
    "v_mfma_f32_16x16x32_bf16 v[216:219], v[104:107], v[116:119], v[216:219]\n"
    "ds_read_b64_tr_b16 v[82:83], v1 offset:45312\n"
    "s_nop 3\n"
    "v_mfma_f32_16x16x32_bf16 v[212:215], v[108:111], v[112:115], v[212:215]\n"
    "ds_read_b64_tr_b16 v[84:85], v4 offset:45056\n"
    "ds_read_b32 v9, v7\n"
    "v_mfma_f32_16x16x32_bf16 v[220:223], v[108:111], v[116:119], v[220:223]\n"
    "ds_read_b64_tr_b16 v[86:87], v4 offset:45312\n"
    "s_nop 3\n"
    "s_waitcnt lgkmcnt(9)\n"
    "v_mfma_f32_16x16x32_bf16 v[128:131], v[64:67], v[120:123], v[128:131]\n"
    "ds_read_b64_tr_b16 v[88:89], v1 offset:45568\n"
    "v_mfma_f32_16x16x32_bf16 v[136:139], v[64:67], v[124:127], v[136:139]\n"
    "ds_read_b64_tr_b16 v[90:91], v1 offset:45824\n"
    "s_nop 3\n"
    "v_mfma_f32_16x16x32_bf16 v[132:135], v[68:71], v[120:123], v[132:135]\n"
    "ds_read_b64_tr_b16 v[92:93], v4 offset:45568\n"
    "v_mfma_f32_16x16x32_bf16 v[140:143], v[68:71], v[124:127], v[140:143]\n"
    "ds_read_b64_tr_b16 v[94:95], v4 offset:45824\n"
    "s_nop 3\n"
    "s_waitcnt lgkmcnt(9)\n"
    "v_mfma_f32_16x16x32_bf16 v[144:147], v[72:75], v[120:123], v[144:147]\n"
    "ds_read_b64_tr_b16 v[96:97], v2 offset:4096\n"
    "v_mfma_f32_16x16x32_bf16 v[152:155], v[72:75], v[124:127], v[152:155]\n"
    "ds_read_b64_tr_b16 v[98:99], v2 offset:4352\n"
    "s_nop 3\n"
    "v_mfma_f32_16x16x32_bf16 v[148:151], v[76:79], v[120:123], v[148:151]\n"
    "ds_read_b64_tr_b16 v[100:101], v5 offset:4096\n"
    "v_mfma_f32_16x16x32_bf16 v[156:159], v[76:79], v[124:127], v[156:159]\n"
    "ds_read_b64_tr_b16 v[102:103], v5 offset:4352\n"
    "s_nop 3\n"
    "s_waitcnt lgkmcnt(8)\n"
    "v_mfma_f32_16x16x32_bf16 v[160:163], v[80:83], v[120:123], v[160:163]\n"
    "ds_read_b64_tr_b16 v[104:105], v2 offset:4608\n"
    "v_mfma_f32_16x16x32_bf16 v[168:171], v[80:83], v[124:127], v[168:171]\n"
    "ds_read_b64_tr_b16 v[106:107], v2 offset:4864\n"
    "s_nop 3\n"
    "v_mfma_f32_16x16x32_bf16 v[164:167], v[84:87], v[120:123], v[164:167]\n"
    "ds_read_b64_tr_b16 v[108:109], v5 offset:4608\n"
    "v_mfma_f32_16x16x32_bf16 v[172:175], v[84:87], v[124:127], v[172:175]\n"
    "ds_read_b64_tr_b16 v[110:111], v5 offset:4864\n"
    "v_cmp_gt_u32 vcc, 0xb0000006, v9\n"
    "s_nop 3\n"
    "s_waitcnt lgkmcnt(8)\n"
    "v_mfma_f32_16x16x32_bf16 v[176:179], v[88:91], v[120:123], v[176:179]\n"
    "s_cbranch_vccnz CS6_%=\n"
    "CB6_%=:\n"
    "ds_read_b64_tr_b16 v[64:65], v2 offset:16384\n"
    "v_mfma_f32_16x16x32_bf16 v[184:187], v[88:91], v[124:127], v[184:187]\n"
    "ds_read_b64_tr_b16 v[66:67], v2 offset:16640\n"
    "v_add_u32 v10, 49152, v8\n"
    "s_nop 3\n"
    "v_mfma_f32_16x16x32_bf16 v[180:183], v[92:95], v[120:123], v[180:183]\n"
    "ds_read_b64_tr_b16 v[68:69], v5 offset:16384\n"
    "ds_read_b128 v[224:227], v10 offset:0\n"
    "v_mfma_f32_16x16x32_bf16 v[188:191], v[92:95], v[124:127], v[188:191]\n"
    "ds_read_b64_tr_b16 v[70:71], v5 offset:16640\n"
    "s_nop 3\n"
    "s_waitcnt lgkmcnt(9)\n"
    "v_mfma_f32_16x16x32_bf16 v[192:195], v[96:99], v[120:123], v[192:195]\n"
    "ds_read_b64_tr_b16 v[72:73], v2 offset:16896\n"
    "ds_read_b128 v[228:231], v10 offset:2048\n"
    "v_mfma_f32_16x16x32_bf16 v[200:203], v[96:99], v[124:127], v[200:203]\n"
    "ds_read_b64_tr_b16 v[74:75], v2 offset:17152\n"
    "ds_read_b128 v[232:235], v10 offset:512\n"
    "s_nop 3\n"
    "v_mfma_f32_16x16x32_bf16 v[196:199], v[100:103], v[120:123], v[196:199]\n"
    "ds_read_b64_tr_b16 v[76:77], v5 offset:16896\n"
    "ds_read_b128 v[236:239], v10 offset:2560\n"
    "v_mfma_f32_16x16x32_bf16 v[204:207], v[100:103], v[124:127], v[204:207]\n"
    "s_waitcnt lgkmcnt(14)\n"
    "ds_read_b64_tr_b16 v[78:79], v5 offset:17152\n"
    "s_nop 3\n"
    "s_waitcnt lgkmcnt(12)\n"
    "v_mfma_f32_16x16x32_bf16 v[208:211], v[104:107], v[120:123], v[208:211]\n"
    "ds_read_b64_tr_b16 v[80:81], v2 offset:24576\n"
    "buffer_load_dword off, s[88:91], 0 offset:40 lds\n"
    "v_mfma_f32_16x16x32_bf16 v[216:219], v[104:107], v[124:127], v[216:219]\n"
    "ds_read_b64_tr_b16 v[82:83], v2 offset:24832\n"
    "s_nop 3\n"
    "v_mfma_f32_16x16x32_bf16 v[212:215], v[108:111], v[120:123], v[212:215]\n"
    "ds_read_b64_tr_b16 v[84:85], v5 offset:24576\n"
    "v_mfma_f32_16x16x32_bf16 v[220:223], v[108:111], v[124:127], v[220:223]\n"
    "s_waitcnt lgkmcnt(14)\n"
    "ds_read_b64_tr_b16 v[86:87], v5 offset:24832\n"
    "s_nop 3\n"
    "s_waitcnt lgkmcnt(11)\n"
    "v_mfma_f32_16x16x32_bf16 v[128:131], v[64:67], v[224:227], v[128:131]\n"
    "ds_read_b64_tr_b16 v[88:89], v2 offset:25088\n"
    "s_waitcnt lgkmcnt(10)\n"
    "v_mfma_f32_16x16x32_bf16 v[136:139], v[64:67], v[228:231], v[136:139]\n"
    "ds_read_b64_tr_b16 v[90:91], v2 offset:25344\n"
    "s_nop 3\n"
    "v_mfma_f32_16x16x32_bf16 v[132:135], v[68:71], v[224:227], v[132:135]\n"
    "ds_read_b64_tr_b16 v[92:93], v5 offset:25088\n"
    "v_mfma_f32_16x16x32_bf16 v[140:143], v[68:71], v[228:231], v[140:143]\n"
    "ds_read_b64_tr_b16 v[94:95], v5 offset:25344\n"
    "s_nop 3\n"
    "s_waitcnt lgkmcnt(8)\n"
    "v_mfma_f32_16x16x32_bf16 v[144:147], v[72:75], v[224:227], v[144:147]\n"
    "ds_read_b64_tr_b16 v[96:97], v2 offset:8192\n"
    "v_mfma_f32_16x16x32_bf16 v[152:155], v[72:75], v[228:231], v[152:155]\n"
    "ds_read_b64_tr_b16 v[98:99], v2 offset:8448\n"
    "s_nop 3\n"
    "v_mfma_f32_16x16x32_bf16 v[148:151], v[76:79], v[224:227], v[148:151]\n"
    "ds_read_b64_tr_b16 v[100:101], v5 offset:8192\n"
    "buffer_load_dword off, s[88:91], 0 offset:44 lds\n"
    "v_mfma_f32_16x16x32_bf16 v[156:159], v[76:79], v[228:231], v[156:159]\n"
    "ds_read_b64_tr_b16 v[102:103], v5 offset:8448\n"
    "s_nop 3\n"
    "s_waitcnt lgkmcnt(8)\n"
    "v_mfma_f32_16x16x32_bf16 v[160:163], v[80:83], v[224:227], v[160:163]\n"
    "ds_read_b64_tr_b16 v[104:105], v2 offset:8704\n"
    "v_mfma_f32_16x16x32_bf16 v[168:171], v[80:83], v[228:231], v[168:171]\n"
    "ds_read_b64_tr_b16 v[106:107], v2 offset:8960\n"
    "s_nop 3\n"
    "v_mfma_f32_16x16x32_bf16 v[164:167], v[84:87], v[224:227], v[164:167]\n"
    "ds_read_b64_tr_b16 v[108:109], v5 offset:8704\n"
    "v_mfma_f32_16x16x32_bf16 v[172:175], v[84:87], v[228:231], v[172:175]\n"
    "ds_read_b64_tr_b16 v[110:111], v5 offset:8960\n"
    "s_nop 3\n"
    "s_waitcnt lgkmcnt(8)\n"
    "v_mfma_f32_16x16x32_bf16 v[176:179], v[88:91], v[224:227], v[176:179]\n"
    "ds_read_b64_tr_b16 v[64:65], v2 offset:20480\n"
    "v_mfma_f32_16x16x32_bf16 v[184:187], v[88:91], v[228:231], v[184:187]\n"
    "ds_read_b64_tr_b16 v[66:67], v2 offset:20736\n"
    "s_nop 3\n"
    "v_mfma_f32_16x16x32_bf16 v[180:183], v[92:95], v[224:227], v[180:183]\n"
    "ds_read_b64_tr_b16 v[68:69], v5 offset:20480\n"
    "v_mfma_f32_16x16x32_bf16 v[188:191], v[92:95], v[228:231], v[188:191]\n"
    "ds_read_b64_tr_b16 v[70:71], v5 offset:20736\n"
    "s_nop 3\n"
    "s_waitcnt lgkmcnt(8)\n"
    "v_mfma_f32_16x16x32_bf16 v[192:195], v[96:99], v[224:227], v[192:195]\n"
    "ds_read_b64_tr_b16 v[72:73], v2 offset:20992\n"
    "v_mfma_f32_16x16x32_bf16 v[200:203], v[96:99], v[228:231], v[200:203]\n"
    "ds_read_b64_tr_b16 v[74:75], v2 offset:21248\n"
    "s_nop 3\n"
    "v_mfma_f32_16x16x32_bf16 v[196:199], v[100:103], v[224:227], v[196:199]\n"
    "ds_read_b64_tr_b16 v[76:77], v5 offset:20992\n"
    "v_mfma_f32_16x16x32_bf16 v[204:207], v[100:103], v[228:231], v[204:207]\n"
    "ds_read_b64_tr_b16 v[78:79], v5 offset:21248\n"
    "s_nop 3\n"
    "s_waitcnt lgkmcnt(8)\n"
    "v_mfma_f32_16x16x32_bf16 v[208:211], v[104:107], v[224:227], v[208:211]\n"
    "ds_read_b64_tr_b16 v[80:81], v2 offset:28672\n"
    "v_mfma_f32_16x16x32_bf16 v[216:219], v[104:107], v[228:231], v[216:219]\n"
    "ds_read_b64_tr_b16 v[82:83], v2 offset:28928\n"
    "s_nop 3\n"
    "v_mfma_f32_16x16x32_bf16 v[212:215], v[108:111], v[224:227], v[212:215]\n"
    "ds_read_b64_tr_b16 v[84:85], v5 offset:28672\n"
    "ds_read_b32 v9, v7\n"
    "v_mfma_f32_16x16x32_bf16 v[220:223], v[108:111], v[228:231], v[220:223]\n"
    "ds_read_b64_tr_b16 v[86:87], v5 offset:28928\n"
    "s_nop 3\n"
    "s_waitcnt lgkmcnt(9)\n"
    "v_mfma_f32_16x16x32_bf16 v[128:131], v[64:67], v[232:235], v[128:131]\n"
    "ds_read_b64_tr_b16 v[88:89], v2 offset:29184\n"
    "v_mfma_f32_16x16x32_bf16 v[136:139], v[64:67], v[236:239], v[136:139]\n"
    "ds_read_b64_tr_b16 v[90:91], v2 offset:29440\n"
    "s_nop 3\n"
    "v_mfma_f32_16x16x32_bf16 v[132:135], v[68:71], v[232:235], v[132:135]\n"
    "ds_read_b64_tr_b16 v[92:93], v5 offset:29184\n"
    "v_mfma_f32_16x16x32_bf16 v[140:143], v[68:71], v[236:239], v[140:143]\n"
    "ds_read_b64_tr_b16 v[94:95], v5 offset:29440\n"
    "s_nop 3\n"
    "s_waitcnt lgkmcnt(9)\n"
    "v_mfma_f32_16x16x32_bf16 v[144:147], v[72:75], v[232:235], v[144:147]\n"
    "ds_read_b64_tr_b16 v[96:97], v2 offset:12288\n"
    "v_mfma_f32_16x16x32_bf16 v[152:155], v[72:75], v[236:239], v[152:155]\n"
    "ds_read_b64_tr_b16 v[98:99], v2 offset:12544\n"
    "s_nop 3\n"
    "v_mfma_f32_16x16x32_bf16 v[148:151], v[76:79], v[232:235], v[148:151]\n"
    "ds_read_b64_tr_b16 v[100:101], v5 offset:12288\n"
    "v_mfma_f32_16x16x32_bf16 v[156:159], v[76:79], v[236:239], v[156:159]\n"
    "ds_read_b64_tr_b16 v[102:103], v5 offset:12544\n"
    "s_nop 3\n"
    "s_waitcnt lgkmcnt(8)\n"
    "v_mfma_f32_16x16x32_bf16 v[160:163], v[80:83], v[232:235], v[160:163]\n"
    "ds_read_b64_tr_b16 v[104:105], v2 offset:12800\n"
    "v_mfma_f32_16x16x32_bf16 v[168:171], v[80:83], v[236:239], v[168:171]\n"
    "ds_read_b64_tr_b16 v[106:107], v2 offset:13056\n"
    "s_nop 3\n"
    "v_mfma_f32_16x16x32_bf16 v[164:167], v[84:87], v[232:235], v[164:167]\n"
    "ds_read_b64_tr_b16 v[108:109], v5 offset:12800\n"
    "v_mfma_f32_16x16x32_bf16 v[172:175], v[84:87], v[236:239], v[172:175]\n"
    "ds_read_b64_tr_b16 v[110:111], v5 offset:13056\n"
    "v_cmp_gt_u32 vcc, 0xb0000007, v9\n"
    "s_nop 3\n"
    "s_waitcnt lgkmcnt(8)\n"
    "v_mfma_f32_16x16x32_bf16 v[176:179], v[88:91], v[232:235], v[176:179]\n"
    "s_cbranch_vccnz CS7_%=\n"
    "CB7_%=:\n"
    "ds_read_b64_tr_b16 v[64:65], v3 offset:0\n"
    "v_mfma_f32_16x16x32_bf16 v[184:187], v[88:91], v[236:239], v[184:187]\n"
    "ds_read_b64_tr_b16 v[66:67], v3 offset:256\n"
    "v_add_u32 v10, 98304, v8\n"
    "s_nop 3\n"
    "v_mfma_f32_16x16x32_bf16 v[180:183], v[92:95], v[232:235], v[180:183]\n"
    "ds_read_b64_tr_b16 v[68:69], v6 offset:0\n"
    "ds_read_b128 v[112:115], v10 offset:0\n"
    "v_mfma_f32_16x16x32_bf16 v[188:191], v[92:95], v[236:239], v[188:191]\n"
    "ds_read_b64_tr_b16 v[70:71], v6 offset:256\n"
    "s_nop 3\n"
    "s_waitcnt lgkmcnt(9)\n"
    "v_mfma_f32_16x16x32_bf16 v[192:195], v[96:99], v[232:235], v[192:195]\n"
    "ds_read_b64_tr_b16 v[72:73], v3 offset:512\n"
    "ds_read_b128 v[116:119], v10 offset:2048\n"
    "v_mfma_f32_16x16x32_bf16 v[200:203], v[96:99], v[236:239], v[200:203]\n"
    "ds_read_b64_tr_b16 v[74:75], v3 offset:768\n"
    "ds_read_b128 v[120:123], v10 offset:512\n"
    "s_nop 3\n"
    "v_mfma_f32_16x16x32_bf16 v[196:199], v[100:103], v[232:235], v[196:199]\n"
    "ds_read_b64_tr_b16 v[76:77], v6 offset:512\n"
    "ds_read_b128 v[124:127], v10 offset:2560\n"
    "v_mfma_f32_16x16x32_bf16 v[204:207], v[100:103], v[236:239], v[204:207]\n"
    "s_waitcnt lgkmcnt(14)\n"
    "ds_read_b64_tr_b16 v[78:79], v6 offset:768\n"
    "s_nop 3\n"
    "s_waitcnt lgkmcnt(12)\n"
    "v_mfma_f32_16x16x32_bf16 v[208:211], v[104:107], v[232:235], v[208:211]\n"
    "ds_read_b64_tr_b16 v[80:81], v3 offset:8192\n"
    "buffer_load_dword off, s[88:91], 0 offset:48 lds\n"
    "v_mfma_f32_16x16x32_bf16 v[216:219], v[104:107], v[236:239], v[216:219]\n"
    "ds_read_b64_tr_b16 v[82:83], v3 offset:8448\n"
    "s_nop 3\n"
    "v_mfma_f32_16x16x32_bf16 v[212:215], v[108:111], v[232:235], v[212:215]\n"
    "ds_read_b64_tr_b16 v[84:85], v6 offset:8192\n"
    "v_mfma_f32_16x16x32_bf16 v[220:223], v[108:111], v[236:239], v[220:223]\n"
    "s_waitcnt lgkmcnt(14)\n"
    "ds_read_b64_tr_b16 v[86:87], v6 offset:8448\n"
    "s_nop 3\n"
    "s_waitcnt lgkmcnt(11)\n"
    "v_mfma_f32_16x16x32_bf16 v[128:131], v[64:67], v[112:115], v[128:131]\n"
    "ds_read_b64_tr_b16 v[88:89], v3 offset:8704\n"
    "s_waitcnt lgkmcnt(10)\n"
    "v_mfma_f32_16x16x32_bf16 v[136:139], v[64:67], v[116:119], v[136:139]\n"
    "ds_read_b64_tr_b16 v[90:91], v3 offset:8960\n"
    "s_nop 3\n"
    "v_mfma_f32_16x16x32_bf16 v[132:135], v[68:71], v[112:115], v[132:135]\n"
    "ds_read_b64_tr_b16 v[92:93], v6 offset:8704\n"
    "v_mfma_f32_16x16x32_bf16 v[140:143], v[68:71], v[116:119], v[140:143]\n"
    "ds_read_b64_tr_b16 v[94:95], v6 offset:8960\n"
    "s_nop 3\n"
    "s_waitcnt lgkmcnt(8)\n"
    "v_mfma_f32_16x16x32_bf16 v[144:147], v[72:75], v[112:115], v[144:147]\n"
    "ds_read_b64_tr_b16 v[96:97], v3 offset:16384\n"
    "v_mfma_f32_16x16x32_bf16 v[152:155], v[72:75], v[116:119], v[152:155]\n"
    "ds_read_b64_tr_b16 v[98:99], v3 offset:16640\n"
    "s_nop 3\n"
    "v_mfma_f32_16x16x32_bf16 v[148:151], v[76:79], v[112:115], v[148:151]\n"
    "ds_read_b64_tr_b16 v[100:101], v6 offset:16384\n"
    "buffer_load_dword off, s[88:91], 0 offset:52 lds\n"
    "v_mfma_f32_16x16x32_bf16 v[156:159], v[76:79], v[116:119], v[156:159]\n"
    "ds_read_b64_tr_b16 v[102:103], v6 offset:16640\n"
    "s_nop 3\n"
    "s_waitcnt lgkmcnt(8)\n"
    "v_mfma_f32_16x16x32_bf16 v[160:163], v[80:83], v[112:115], v[160:163]\n"
    "ds_read_b64_tr_b16 v[104:105], v3 offset:16896\n"
    "v_mfma_f32_16x16x32_bf16 v[168:171], v[80:83], v[116:119], v[168:171]\n"
    "ds_read_b64_tr_b16 v[106:107], v3 offset:17152\n"
    "s_nop 3\n"
    "v_mfma_f32_16x16x32_bf16 v[164:167], v[84:87], v[112:115], v[164:167]\n"
    "ds_read_b64_tr_b16 v[108:109], v6 offset:16896\n"
    "v_mfma_f32_16x16x32_bf16 v[172:175], v[84:87], v[116:119], v[172:175]\n"
    "ds_read_b64_tr_b16 v[110:111], v6 offset:17152\n"
    "s_nop 3\n"
    "s_waitcnt lgkmcnt(8)\n"
    "v_mfma_f32_16x16x32_bf16 v[176:179], v[88:91], v[112:115], v[176:179]\n"
    "ds_read_b64_tr_b16 v[64:65], v3 offset:4096\n"
    "v_mfma_f32_16x16x32_bf16 v[184:187], v[88:91], v[116:119], v[184:187]\n"
    "ds_read_b64_tr_b16 v[66:67], v3 offset:4352\n"
    "s_nop 3\n"
    "v_mfma_f32_16x16x32_bf16 v[180:183], v[92:95], v[112:115], v[180:183]\n"
    "ds_read_b64_tr_b16 v[68:69], v6 offset:4096\n"
    "v_mfma_f32_16x16x32_bf16 v[188:191], v[92:95], v[116:119], v[188:191]\n"
    "ds_read_b64_tr_b16 v[70:71], v6 offset:4352\n"
    "s_nop 3\n"
    "s_waitcnt lgkmcnt(8)\n"
    "v_mfma_f32_16x16x32_bf16 v[192:195], v[96:99], v[112:115], v[192:195]\n"
    "ds_read_b64_tr_b16 v[72:73], v3 offset:4608\n"
    "v_mfma_f32_16x16x32_bf16 v[200:203], v[96:99], v[116:119], v[200:203]\n"
    "ds_read_b64_tr_b16 v[74:75], v3 offset:4864\n"
    "s_nop 3\n"
    "v_mfma_f32_16x16x32_bf16 v[196:199], v[100:103], v[112:115], v[196:199]\n"
    "ds_read_b64_tr_b16 v[76:77], v6 offset:4608\n"
    "v_mfma_f32_16x16x32_bf16 v[204:207], v[100:103], v[116:119], v[204:207]\n"
    "ds_read_b64_tr_b16 v[78:79], v6 offset:4864\n"
    "s_nop 3\n"
    "s_waitcnt lgkmcnt(8)\n"
    "v_mfma_f32_16x16x32_bf16 v[208:211], v[104:107], v[112:115], v[208:211]\n"
    "ds_read_b64_tr_b16 v[80:81], v3 offset:12288\n"
    "v_mfma_f32_16x16x32_bf16 v[216:219], v[104:107], v[116:119], v[216:219]\n"
    "ds_read_b64_tr_b16 v[82:83], v3 offset:12544\n"
    "s_nop 3\n"
    "v_mfma_f32_16x16x32_bf16 v[212:215], v[108:111], v[112:115], v[212:215]\n"
    "ds_read_b64_tr_b16 v[84:85], v6 offset:12288\n"
    "ds_read_b32 v9, v7\n"
    "v_mfma_f32_16x16x32_bf16 v[220:223], v[108:111], v[116:119], v[220:223]\n"
    "ds_read_b64_tr_b16 v[86:87], v6 offset:12544\n"
    "s_nop 3\n"
    "s_waitcnt lgkmcnt(9)\n"
    "v_mfma_f32_16x16x32_bf16 v[128:131], v[64:67], v[120:123], v[128:131]\n"
    "ds_read_b64_tr_b16 v[88:89], v3 offset:12800\n"
    "v_mfma_f32_16x16x32_bf16 v[136:139], v[64:67], v[124:127], v[136:139]\n"
    "ds_read_b64_tr_b16 v[90:91], v3 offset:13056\n"
    "s_nop 3\n"
    "v_mfma_f32_16x16x32_bf16 v[132:135], v[68:71], v[120:123], v[132:135]\n"
    "ds_read_b64_tr_b16 v[92:93], v6 offset:12800\n"
    "v_mfma_f32_16x16x32_bf16 v[140:143], v[68:71], v[124:127], v[140:143]\n"
    "ds_read_b64_tr_b16 v[94:95], v6 offset:13056\n"
    "s_nop 3\n"
    "s_waitcnt lgkmcnt(9)\n"
    "v_mfma_f32_16x16x32_bf16 v[144:147], v[72:75], v[120:123], v[144:147]\n"
    "ds_read_b64_tr_b16 v[96:97], v3 offset:20480\n"
    "v_mfma_f32_16x16x32_bf16 v[152:155], v[72:75], v[124:127], v[152:155]\n"
    "ds_read_b64_tr_b16 v[98:99], v3 offset:20736\n"
    "s_nop 3\n"
    "v_mfma_f32_16x16x32_bf16 v[148:151], v[76:79], v[120:123], v[148:151]\n"
    "ds_read_b64_tr_b16 v[100:101], v6 offset:20480\n"
    "v_mfma_f32_16x16x32_bf16 v[156:159], v[76:79], v[124:127], v[156:159]\n"
    "ds_read_b64_tr_b16 v[102:103], v6 offset:20736\n"
    "s_nop 3\n"
    "s_waitcnt lgkmcnt(8)\n"
    "v_mfma_f32_16x16x32_bf16 v[160:163], v[80:83], v[120:123], v[160:163]\n"
    "ds_read_b64_tr_b16 v[104:105], v3 offset:20992\n"
    "v_mfma_f32_16x16x32_bf16 v[168:171], v[80:83], v[124:127], v[168:171]\n"
    "ds_read_b64_tr_b16 v[106:107], v3 offset:21248\n"
    "s_nop 3\n"
    "v_mfma_f32_16x16x32_bf16 v[164:167], v[84:87], v[120:123], v[164:167]\n"
    "ds_read_b64_tr_b16 v[108:109], v6 offset:20992\n"
    "v_mfma_f32_16x16x32_bf16 v[172:175], v[84:87], v[124:127], v[172:175]\n"
    "ds_read_b64_tr_b16 v[110:111], v6 offset:21248\n"
    "v_cmp_gt_u32 vcc, 0xb0000008, v9\n"
    "s_nop 3\n"
    "s_waitcnt lgkmcnt(8)\n"
    "v_mfma_f32_16x16x32_bf16 v[176:179], v[88:91], v[120:123], v[176:179]\n"
    "s_cbranch_vccnz CS8_%=\n"
    "CB8_%=:\n"
    "ds_read_b64_tr_b16 v[64:65], v1 offset:16384\n"
    "v_mfma_f32_16x16x32_bf16 v[184:187], v[88:91], v[124:127], v[184:187]\n"
    "ds_read_b64_tr_b16 v[66:67], v1 offset:16640\n"
    "v_add_u32 v10, 114688, v8\n"
    "s_nop 3\n"
    "v_mfma_f32_16x16x32_bf16 v[180:183], v[92:95], v[120:123], v[180:183]\n"
    "ds_read_b64_tr_b16 v[68:69], v4 offset:16384\n"
    "ds_read_b128 v[224:227], v10 offset:0\n"
    "v_mfma_f32_16x16x32_bf16 v[188:191], v[92:95], v[124:127], v[188:191]\n"
    "ds_read_b64_tr_b16 v[70:71], v4 offset:16640\n"
    "s_nop 3\n"
    "s_waitcnt lgkmcnt(9)\n"
    "v_mfma_f32_16x16x32_bf16 v[192:195], v[96:99], v[120:123], v[192:195]\n"
    "ds_read_b64_tr_b16 v[72:73], v1 offset:16896\n"
    "ds_read_b128 v[228:231], v10 offset:2048\n"
    "v_mfma_f32_16x16x32_bf16 v[200:203], v[96:99], v[124:127], v[200:203]\n"
    "ds_read_b64_tr_b16 v[74:75], v1 offset:17152\n"
    "ds_read_b128 v[232:235], v10 offset:512\n"
    "s_nop 3\n"
    "v_mfma_f32_16x16x32_bf16 v[196:199], v[100:103], v[120:123], v[196:199]\n"
    "ds_read_b64_tr_b16 v[76:77], v4 offset:16896\n"
    "ds_read_b128 v[236:239], v10 offset:2560\n"
    "v_mfma_f32_16x16x32_bf16 v[204:207], v[100:103], v[124:127], v[204:207]\n"
    "s_waitcnt lgkmcnt(14)\n"
    "ds_read_b64_tr_b16 v[78:79], v4 offset:17152\n"
    "s_nop 3\n"
    "s_waitcnt lgkmcnt(12)\n"
    "v_mfma_f32_16x16x32_bf16 v[208:211], v[104:107], v[120:123], v[208:211]\n"
    "ds_read_b64_tr_b16 v[80:81], v1 offset:24576\n"
    "buffer_load_dword off, s[88:91], 0 offset:56 lds\n"
    "v_mfma_f32_16x16x32_bf16 v[216:219], v[104:107], v[124:127], v[216:219]\n"
    "ds_read_b64_tr_b16 v[82:83], v1 offset:24832\n"
    "s_nop 3\n"
    "v_mfma_f32_16x16x32_bf16 v[212:215], v[108:111], v[120:123], v[212:215]\n"
    "ds_read_b64_tr_b16 v[84:85], v4 offset:24576\n"
    "v_mfma_f32_16x16x32_bf16 v[220:223], v[108:111], v[124:127], v[220:223]\n"
    "s_waitcnt lgkmcnt(14)\n"
    "ds_read_b64_tr_b16 v[86:87], v4 offset:24832\n"
    "s_nop 3\n"
    "s_waitcnt lgkmcnt(11)\n"
    "v_mfma_f32_16x16x32_bf16 v[128:131], v[64:67], v[224:227], v[128:131]\n"
    "ds_read_b64_tr_b16 v[88:89], v1 offset:25088\n"
    "v_add_u32 v13, 0, v12\n"
    "s_waitcnt lgkmcnt(10)\n"
    "v_mfma_f32_16x16x32_bf16 v[136:139], v[64:67], v[228:231], v[136:139]\n"
    "ds_read_b64_tr_b16 v[90:91], v1 offset:25344\n"
    "v_xor_b32 v14, 32, v13\n"
    "s_nop 3\n"
    "v_mfma_f32_16x16x32_bf16 v[132:135], v[68:71], v[224:227], v[132:135]\n"
    "ds_read_b64_tr_b16 v[92:93], v4 offset:25088\n"
    "ds_read_b64 v[32:33], v13 offset:0\n"
    "v_mfma_f32_16x16x32_bf16 v[140:143], v[68:71], v[228:231], v[140:143]\n"
    "ds_read_b64_tr_b16 v[94:95], v4 offset:25344\n"
    "s_nop 3\n"
    "s_waitcnt lgkmcnt(9)\n"
    "v_mfma_f32_16x16x32_bf16 v[144:147], v[72:75], v[224:227], v[144:147]\n"
    "ds_read_b64_tr_b16 v[96:97], v2 offset:0\n"
    "ds_read_b64 v[34:35], v14 offset:0\n"
    "v_mfma_f32_16x16x32_bf16 v[152:155], v[72:75], v[228:231], v[152:155]\n"
    "ds_read_b64_tr_b16 v[98:99], v2 offset:256\n"
    "s_nop 3\n"
    "v_mfma_f32_16x16x32_bf16 v[148:151], v[76:79], v[224:227], v[148:151]\n"
    "ds_read_b64_tr_b16 v[100:101], v5 offset:0\n"
    "buffer_load_dword off, s[88:91], 0 offset:60 lds\n"
    "v_mfma_f32_16x16x32_bf16 v[156:159], v[76:79], v[228:231], v[156:159]\n"
    "ds_read_b64_tr_b16 v[102:103], v5 offset:256\n"
    "s_nop 3\n"
    "s_waitcnt lgkmcnt(10)\n"
    "v_mfma_f32_16x16x32_bf16 v[160:163], v[80:83], v[224:227], v[160:163]\n"
    "ds_read_b64_tr_b16 v[104:105], v2 offset:512\n"
    "ds_read_b64 v[36:37], v13 offset:2048\n"
    "v_mfma_f32_16x16x32_bf16 v[168:171], v[80:83], v[228:231], v[168:171]\n"
    "ds_read_b64_tr_b16 v[106:107], v2 offset:768\n"
    "ds_read_b64 v[38:39], v14 offset:2048\n"
    "s_nop 3\n"
    "v_mfma_f32_16x16x32_bf16 v[164:167], v[84:87], v[224:227], v[164:167]\n"
    "ds_read_b64_tr_b16 v[108:109], v5 offset:512\n"
    "s_waitcnt lgkmcnt(14)\n"
    "ds_read_b64 v[40:41], v13 offset:512\n"
    "v_mfma_f32_16x16x32_bf16 v[172:175], v[84:87], v[228:231], v[172:175]\n"
    "s_waitcnt lgkmcnt(14)\n"
    "ds_read_b64_tr_b16 v[110:111], v5 offset:768\n"
    "s_nop 3\n"
    "s_waitcnt lgkmcnt(12)\n"
    "v_mfma_f32_16x16x32_bf16 v[176:179], v[88:91], v[224:227], v[176:179]\n"
    "ds_read_b64_tr_b16 v[64:65], v1 offset:20480\n"
    "ds_read_b64 v[42:43], v14 offset:512\n"
    "v_mfma_f32_16x16x32_bf16 v[184:187], v[88:91], v[228:231], v[184:187]\n"
    "ds_read_b64_tr_b16 v[66:67], v1 offset:20736\n"
    "s_nop 3\n"
    "v_mfma_f32_16x16x32_bf16 v[180:183], v[92:95], v[224:227], v[180:183]\n"
    "s_waitcnt lgkmcnt(14)\n"
    "ds_read_b64_tr_b16 v[68:69], v4 offset:20480\n"
    "s_waitcnt lgkmcnt(14)\n"
    "ds_read_b64 v[44:45], v13 offset:2560\n"
    "v_mfma_f32_16x16x32_bf16 v[188:191], v[92:95], v[228:231], v[188:191]\n"
    "s_waitcnt lgkmcnt(14)\n"
    "ds_read_b64_tr_b16 v[70:71], v4 offset:20736\n"
    "s_nop 3\n"
    "s_waitcnt lgkmcnt(13)\n"
    "v_mfma_f32_16x16x32_bf16 v[192:195], v[96:99], v[224:227], v[192:195]\n"
    "ds_read_b64_tr_b16 v[72:73], v1 offset:20992\n"
    "ds_read_b64 v[46:47], v14 offset:2560\n"
    "v_mfma_f32_16x16x32_bf16 v[200:203], v[96:99], v[228:231], v[200:203]\n"
    "s_waitcnt lgkmcnt(14)\n"
    "ds_read_b64_tr_b16 v[74:75], v1 offset:21248\n"
    "s_nop 3\n"
    "v_mfma_f32_16x16x32_bf16 v[196:199], v[100:103], v[224:227], v[196:199]\n"
    "s_waitcnt lgkmcnt(14)\n"
    "ds_read_b64_tr_b16 v[76:77], v4 offset:20992\n"
    "v_mfma_f32_16x16x32_bf16 v[204:207], v[100:103], v[228:231], v[204:207]\n"
    "s_waitcnt lgkmcnt(14)\n"
    "ds_read_b64_tr_b16 v[78:79], v4 offset:21248\n"
    "s_nop 3\n"
    "s_waitcnt lgkmcnt(11)\n"
    "v_mfma_f32_16x16x32_bf16 v[208:211], v[104:107], v[224:227], v[208:211]\n"
    "ds_read_b64_tr_b16 v[80:81], v1 offset:28672\n"
    "v_mfma_f32_16x16x32_bf16 v[216:219], v[104:107], v[228:231], v[216:219]\n"
    "ds_read_b64_tr_b16 v[82:83], v1 offset:28928\n"
    "s_nop 3\n"
    "v_mfma_f32_16x16x32_bf16 v[212:215], v[108:111], v[224:227], v[212:215]\n"
    "ds_read_b64_tr_b16 v[84:85], v4 offset:28672\n"
    "ds_read_b32 v9, v7\n"
    "v_mfma_f32_16x16x32_bf16 v[220:223], v[108:111], v[228:231], v[220:223]\n"
    "s_waitcnt lgkmcnt(14)\n"
    "ds_read_b64_tr_b16 v[86:87], v4 offset:28928\n"
    "s_nop 3\n"
    "s_waitcnt lgkmcnt(10)\n"
    "v_mfma_f32_16x16x32_bf16 v[128:131], v[64:67], v[232:235], v[128:131]\n"
    "ds_read_b64_tr_b16 v[88:89], v1 offset:29184\n"
    "v_mfma_f32_16x16x32_bf16 v[136:139], v[64:67], v[236:239], v[136:139]\n"
    "ds_read_b64_tr_b16 v[90:91], v1 offset:29440\n"
    "s_nop 3\n"
    "v_mfma_f32_16x16x32_bf16 v[132:135], v[68:71], v[232:235], v[132:135]\n"
    "ds_read_b64_tr_b16 v[92:93], v4 offset:29184\n"
    "v_mfma_f32_16x16x32_bf16 v[140:143], v[68:71], v[236:239], v[140:143]\n"
    "ds_read_b64_tr_b16 v[94:95], v4 offset:29440\n"
    "s_nop 3\n"
    "s_waitcnt lgkmcnt(9)\n"
    "v_mfma_f32_16x16x32_bf16 v[144:147], v[72:75], v[232:235], v[144:147]\n"
    "ds_read_b64_tr_b16 v[96:97], v2 offset:4096\n"
    "v_mfma_f32_16x16x32_bf16 v[152:155], v[72:75], v[236:239], v[152:155]\n"
    "ds_read_b64_tr_b16 v[98:99], v2 offset:4352\n"
    "s_nop 3\n"
    "v_mfma_f32_16x16x32_bf16 v[148:151], v[76:79], v[232:235], v[148:151]\n"
    "ds_read_b64_tr_b16 v[100:101], v5 offset:4096\n"
    "v_mfma_f32_16x16x32_bf16 v[156:159], v[76:79], v[236:239], v[156:159]\n"
    "ds_read_b64_tr_b16 v[102:103], v5 offset:4352\n"
    "s_nop 3\n"
    "s_waitcnt lgkmcnt(8)\n"
    "v_mfma_f32_16x16x32_bf16 v[160:163], v[80:83], v[232:235], v[160:163]\n"
    "ds_read_b64_tr_b16 v[104:105], v2 offset:4608\n"
    "v_mfma_f32_16x16x32_bf16 v[168:171], v[80:83], v[236:239], v[168:171]\n"
    "ds_read_b64_tr_b16 v[106:107], v2 offset:4864\n"
    "s_nop 3\n"
    "v_mfma_f32_16x16x32_bf16 v[164:167], v[84:87], v[232:235], v[164:167]\n"
    "ds_read_b64_tr_b16 v[108:109], v5 offset:4608\n"
    "v_mfma_f32_16x16x32_bf16 v[172:175], v[84:87], v[236:239], v[172:175]\n"
    "ds_read_b64_tr_b16 v[110:111], v5 offset:4864\n"
    "v_cmp_gt_u32 vcc, 0xb0000009, v9\n"
    "s_nop 3\n"
    "s_waitcnt lgkmcnt(8)\n"
    "v_mfma_f32_16x16x32_bf16 v[176:179], v[88:91], v[232:235], v[176:179]\n"
    "s_cbranch_vccnz CS9_%=\n"
    "CB9_%=:\n"
    "ds_read_b64_tr_b16 v[64:65], v1 offset:49152\n"
    "v_mfma_f32_16x16x32_bf16 v[184:187], v[88:91], v[236:239], v[184:187]\n"
    "ds_read_b64_tr_b16 v[66:67], v1 offset:49408\n"
    "v_add_u32 v10, 32768, v8\n"
    "s_nop 3\n"
    "v_mfma_f32_16x16x32_bf16 v[180:183], v[92:95], v[232:235], v[180:183]\n"
    "ds_read_b64_tr_b16 v[68:69], v4 offset:49152\n"
    "ds_read_b128 v[112:115], v10 offset:0\n"
    "v_mfma_f32_16x16x32_bf16 v[188:191], v[92:95], v[236:239], v[188:191]\n"
    "ds_read_b64_tr_b16 v[70:71], v4 offset:49408\n"
    "s_nop 3\n"
    "s_waitcnt lgkmcnt(9)\n"
    "v_mfma_f32_16x16x32_bf16 v[192:195], v[96:99], v[232:235], v[192:195]\n"
    "ds_read_b64_tr_b16 v[72:73], v1 offset:49664\n"
    "ds_read_b128 v[116:119], v10 offset:2048\n"
    "v_mfma_f32_16x16x32_bf16 v[200:203], v[96:99], v[236:239], v[200:203]\n"
    "ds_read_b64_tr_b16 v[74:75], v1 offset:49920\n"
    "ds_read_b128 v[120:123], v10 offset:512\n"
    "s_nop 3\n"
    "v_mfma_f32_16x16x32_bf16 v[196:199], v[100:103], v[232:235], v[196:199]\n"
    "ds_read_b64_tr_b16 v[76:77], v4 offset:49664\n"
    "ds_read_b128 v[124:127], v10 offset:2560\n"
    "v_mfma_f32_16x16x32_bf16 v[204:207], v[100:103], v[236:239], v[204:207]\n"
    "s_waitcnt lgkmcnt(14)\n"
    "ds_read_b64_tr_b16 v[78:79], v4 offset:49920\n"
    "s_nop 3\n"
    "s_waitcnt lgkmcnt(12)\n"
    "v_mfma_f32_16x16x32_bf16 v[208:211], v[104:107], v[232:235], v[208:211]\n"
    "ds_read_b64_tr_b16 v[80:81], v1 offset:57344\n"
    "buffer_load_dword off, s[88:91], 0 offset:64 lds\n"
    "v_mfma_f32_16x16x32_bf16 v[216:219], v[104:107], v[236:239], v[216:219]\n"
    "ds_read_b64_tr_b16 v[82:83], v1 offset:57600\n"
    "s_nop 3\n"
    "v_mfma_f32_16x16x32_bf16 v[212:215], v[108:111], v[232:235], v[212:215]\n"
    "ds_read_b64_tr_b16 v[84:85], v4 offset:57344\n"
    "v_mfma_f32_16x16x32_bf16 v[220:223], v[108:111], v[236:239], v[220:223]\n"
    "s_waitcnt lgkmcnt(14)\n"
    "ds_read_b64_tr_b16 v[86:87], v4 offset:57600\n"
    "s_nop 3\n"
    "s_waitcnt lgkmcnt(11)\n"
    "v_mfma_f32_16x16x32_bf16 v[128:131], v[64:67], v[112:115], v[128:131]\n"
    "ds_read_b64_tr_b16 v[88:89], v1 offset:57856\n"
    "s_waitcnt lgkmcnt(10)\n"
    "v_mfma_f32_16x16x32_bf16 v[136:139], v[64:67], v[116:119], v[136:139]\n"
    "ds_read_b64_tr_b16 v[90:91], v1 offset:58112\n"
    "s_nop 3\n"
    "v_mfma_f32_16x16x32_bf16 v[132:135], v[68:71], v[112:115], v[132:135]\n"
    "ds_read_b64_tr_b16 v[92:93], v4 offset:57856\n"
    "v_mfma_f32_16x16x32_bf16 v[140:143], v[68:71], v[116:119], v[140:143]\n"
    "ds_read_b64_tr_b16 v[94:95], v4 offset:58112\n"
    "s_nop 3\n"
    "s_waitcnt lgkmcnt(8)\n"
    "v_mfma_f32_16x16x32_bf16 v[144:147], v[72:75], v[112:115], v[144:147]\n"
    "ds_read_b64_tr_b16 v[96:97], v2 offset:8192\n"
    "v_mfma_f32_16x16x32_bf16 v[152:155], v[72:75], v[116:119], v[152:155]\n"
    "ds_read_b64_tr_b16 v[98:99], v2 offset:8448\n"
    "s_nop 3\n"
    "v_mfma_f32_16x16x32_bf16 v[148:151], v[76:79], v[112:115], v[148:151]\n"
    "ds_read_b64_tr_b16 v[100:101], v5 offset:8192\n"
    "buffer_load_dword off, s[88:91], 0 offset:68 lds\n"
    "v_mfma_f32_16x16x32_bf16 v[156:159], v[76:79], v[116:119], v[156:159]\n"
    "ds_read_b64_tr_b16 v[102:103], v5 offset:8448\n"
    "s_nop 3\n"
    "s_waitcnt lgkmcnt(8)\n"
    "v_mfma_f32_16x16x32_bf16 v[160:163], v[80:83], v[112:115], v[160:163]\n"
    "ds_read_b64_tr_b16 v[104:105], v2 offset:8704\n"
    "v_mfma_f32_16x16x32_bf16 v[168:171], v[80:83], v[116:119], v[168:171]\n"
    "ds_read_b64_tr_b16 v[106:107], v2 offset:8960\n"
    "s_nop 3\n"
    "v_mfma_f32_16x16x32_bf16 v[164:167], v[84:87], v[112:115], v[164:167]\n"
    "ds_read_b64_tr_b16 v[108:109], v5 offset:8704\n"
    "v_mfma_f32_16x16x32_bf16 v[172:175], v[84:87], v[116:119], v[172:175]\n"
    "ds_read_b64_tr_b16 v[110:111], v5 offset:8960\n"
    "s_nop 3\n"
    "s_waitcnt lgkmcnt(8)\n"
    "v_mfma_f32_16x16x32_bf16 v[176:179], v[88:91], v[112:115], v[176:179]\n"
    "ds_read_b64_tr_b16 v[64:65], v1 offset:53248\n"
    "v_mfma_f32_16x16x32_bf16 v[184:187], v[88:91], v[116:119], v[184:187]\n"
    "ds_read_b64_tr_b16 v[66:67], v1 offset:53504\n"
    "s_nop 3\n"
    "v_mfma_f32_16x16x32_bf16 v[180:183], v[92:95], v[112:115], v[180:183]\n"
    "ds_read_b64_tr_b16 v[68:69], v4 offset:53248\n"
    "v_mfma_f32_16x16x32_bf16 v[188:191], v[92:95], v[116:119], v[188:191]\n"
    "ds_read_b64_tr_b16 v[70:71], v4 offset:53504\n"
    "s_nop 3\n"
    "s_waitcnt lgkmcnt(8)\n"
    "v_mfma_f32_16x16x32_bf16 v[192:195], v[96:99], v[112:115], v[192:195]\n"
    "ds_read_b64_tr_b16 v[72:73], v1 offset:53760\n"
    "v_mfma_f32_16x16x32_bf16 v[200:203], v[96:99], v[116:119], v[200:203]\n"
    "ds_read_b64_tr_b16 v[74:75], v1 offset:54016\n"
    "s_nop 3\n"
    "v_mfma_f32_16x16x32_bf16 v[196:199], v[100:103], v[112:115], v[196:199]\n"
    "ds_read_b64_tr_b16 v[76:77], v4 offset:53760\n"
    "v_mfma_f32_16x16x32_bf16 v[204:207], v[100:103], v[116:119], v[204:207]\n"
    "ds_read_b64_tr_b16 v[78:79], v4 offset:54016\n"
    "s_nop 3\n"
    "s_waitcnt lgkmcnt(8)\n"
    "v_mfma_f32_16x16x32_bf16 v[208:211], v[104:107], v[112:115], v[208:211]\n"
    "ds_read_b64_tr_b16 v[80:81], v1 offset:61440\n"
    "v_mfma_f32_16x16x32_bf16 v[216:219], v[104:107], v[116:119], v[216:219]\n"
    "ds_read_b64_tr_b16 v[82:83], v1 offset:61696\n"
    "s_nop 3\n"
    "v_mfma_f32_16x16x32_bf16 v[212:215], v[108:111], v[112:115], v[212:215]\n"
    "ds_read_b64_tr_b16 v[84:85], v4 offset:61440\n"
    "ds_read_b32 v9, v7\n"
    "v_mfma_f32_16x16x32_bf16 v[220:223], v[108:111], v[116:119], v[220:223]\n"
    "ds_read_b64_tr_b16 v[86:87], v4 offset:61696\n"
    "s_nop 3\n"
    "s_waitcnt lgkmcnt(9)\n"
    "v_mfma_f32_16x16x32_bf16 v[128:131], v[64:67], v[120:123], v[128:131]\n"
    "ds_read_b64_tr_b16 v[88:89], v1 offset:61952\n"
    "v_mfma_f32_16x16x32_bf16 v[136:139], v[64:67], v[124:127], v[136:139]\n"
    "ds_read_b64_tr_b16 v[90:91], v1 offset:62208\n"
    "s_nop 3\n"
    "v_mfma_f32_16x16x32_bf16 v[132:135], v[68:71], v[120:123], v[132:135]\n"
    "ds_read_b64_tr_b16 v[92:93], v4 offset:61952\n"
    "v_mfma_f32_16x16x32_bf16 v[140:143], v[68:71], v[124:127], v[140:143]\n"
    "ds_read_b64_tr_b16 v[94:95], v4 offset:62208\n"
    "s_nop 3\n"
    "s_waitcnt lgkmcnt(9)\n"
    "v_mfma_f32_16x16x32_bf16 v[144:147], v[72:75], v[120:123], v[144:147]\n"
    "ds_read_b64_tr_b16 v[96:97], v2 offset:12288\n"
    "v_mfma_f32_16x16x32_bf16 v[152:155], v[72:75], v[124:127], v[152:155]\n"
    "ds_read_b64_tr_b16 v[98:99], v2 offset:12544\n"
    "s_nop 3\n"
    "v_mfma_f32_16x16x32_bf16 v[148:151], v[76:79], v[120:123], v[148:151]\n"
    "ds_read_b64_tr_b16 v[100:101], v5 offset:12288\n"
    "v_mfma_f32_16x16x32_bf16 v[156:159], v[76:79], v[124:127], v[156:159]\n"
    "ds_read_b64_tr_b16 v[102:103], v5 offset:12544\n"
    "s_nop 3\n"
    "s_waitcnt lgkmcnt(8)\n"
    "v_mfma_f32_16x16x32_bf16 v[160:163], v[80:83], v[120:123], v[160:163]\n"
    "ds_read_b64_tr_b16 v[104:105], v2 offset:12800\n"
    "v_mfma_f32_16x16x32_bf16 v[168:171], v[80:83], v[124:127], v[168:171]\n"
    "ds_read_b64_tr_b16 v[106:107], v2 offset:13056\n"
    "s_nop 3\n"
    "v_mfma_f32_16x16x32_bf16 v[164:167], v[84:87], v[120:123], v[164:167]\n"
    "ds_read_b64_tr_b16 v[108:109], v5 offset:12800\n"
    "v_mfma_f32_16x16x32_bf16 v[172:175], v[84:87], v[124:127], v[172:175]\n"
    "ds_read_b64_tr_b16 v[110:111], v5 offset:13056\n"
    "v_cmp_gt_u32 vcc, 0xb000000a, v9\n"
    "s_nop 3\n"
    "s_waitcnt lgkmcnt(8)\n"
    "v_mfma_f32_16x16x32_bf16 v[176:179], v[88:91], v[120:123], v[176:179]\n"
    "s_cbranch_vccnz CS10_%=\n"
    "CB10_%=:\n"
    "ds_read_b64_tr_b16 v[64:65], v2 offset:32768\n"
    "v_mfma_f32_16x16x32_bf16 v[184:187], v[88:91], v[124:127], v[184:187]\n"
    "ds_read_b64_tr_b16 v[66:67], v2 offset:33024\n"
    "v_add_u32 v10, 81920, v8\n"
    "s_nop 3\n"
    "v_mfma_f32_16x16x32_bf16 v[180:183], v[92:95], v[120:123], v[180:183]\n"
    "ds_read_b64_tr_b16 v[68:69], v5 offset:32768\n"
    "ds_read_b128 v[224:227], v10 offset:0\n"
    "v_mfma_f32_16x16x32_bf16 v[188:191], v[92:95], v[124:127], v[188:191]\n"
    "ds_read_b64_tr_b16 v[70:71], v5 offset:33024\n"
    "s_nop 3\n"
    "s_waitcnt lgkmcnt(9)\n"
    "v_mfma_f32_16x16x32_bf16 v[192:195], v[96:99], v[120:123], v[192:195]\n"
    "ds_read_b64_tr_b16 v[72:73], v2 offset:33280\n"
    "ds_read_b128 v[228:231], v10 offset:2048\n"
    "v_mfma_f32_16x16x32_bf16 v[200:203], v[96:99], v[124:127], v[200:203]\n"
    "ds_read_b64_tr_b16 v[74:75], v2 offset:33536\n"
    "ds_read_b128 v[232:235], v10 offset:512\n"
    "s_nop 3\n"
    "v_mfma_f32_16x16x32_bf16 v[196:199], v[100:103], v[120:123], v[196:199]\n"
    "ds_read_b64_tr_b16 v[76:77], v5 offset:33280\n"
    "ds_read_b128 v[236:239], v10 offset:2560\n"
    "v_mfma_f32_16x16x32_bf16 v[204:207], v[100:103], v[124:127], v[204:207]\n"
    "s_waitcnt lgkmcnt(14)\n"
    "ds_read_b64_tr_b16 v[78:79], v5 offset:33536\n"
    "s_nop 3\n"
    "s_waitcnt lgkmcnt(12)\n"
    "v_mfma_f32_16x16x32_bf16 v[208:211], v[104:107], v[120:123], v[208:211]\n"
    "ds_read_b64_tr_b16 v[80:81], v2 offset:40960\n"
    "buffer_load_dword off, s[88:91], 0 offset:72 lds\n"
    "v_mfma_f32_16x16x32_bf16 v[216:219], v[104:107], v[124:127], v[216:219]\n"
    "ds_read_b64_tr_b16 v[82:83], v2 offset:41216\n"
    "s_nop 3\n"
    "v_mfma_f32_16x16x32_bf16 v[212:215], v[108:111], v[120:123], v[212:215]\n"
    "ds_read_b64_tr_b16 v[84:85], v5 offset:40960\n"
    "v_mfma_f32_16x16x32_bf16 v[220:223], v[108:111], v[124:127], v[220:223]\n"
    "s_waitcnt lgkmcnt(14)\n"
    "ds_read_b64_tr_b16 v[86:87], v5 offset:41216\n"
    "s_nop 3\n"
    "s_waitcnt lgkmcnt(11)\n"
    "v_mfma_f32_16x16x32_bf16 v[128:131], v[64:67], v[224:227], v[128:131]\n"
    "ds_read_b64_tr_b16 v[88:89], v2 offset:41472\n"
    "v_add_u32 v13, 131072, v12\n"
    "s_waitcnt lgkmcnt(10)\n"
    "v_mfma_f32_16x16x32_bf16 v[136:139], v[64:67], v[228:231], v[136:139]\n"
    "ds_read_b64_tr_b16 v[90:91], v2 offset:41728\n"
    "v_xor_b32 v14, 32, v13\n"
    "s_nop 3\n"
    "v_mfma_f32_16x16x32_bf16 v[132:135], v[68:71], v[224:227], v[132:135]\n"
    "ds_read_b64_tr_b16 v[92:93], v5 offset:41472\n"
    "ds_read_b64 v[48:49], v13 offset:0\n"
    "v_mfma_f32_16x16x32_bf16 v[140:143], v[68:71], v[228:231], v[140:143]\n"
    "ds_read_b64_tr_b16 v[94:95], v5 offset:41728\n"
    "s_nop 3\n"
    "s_waitcnt lgkmcnt(9)\n"
    "v_mfma_f32_16x16x32_bf16 v[144:147], v[72:75], v[224:227], v[144:147]\n"
    "ds_read_b64_tr_b16 v[96:97], v3 offset:16384\n"
    "ds_read_b64 v[50:51], v14 offset:0\n"
    "v_mfma_f32_16x16x32_bf16 v[152:155], v[72:75], v[228:231], v[152:155]\n"
    "ds_read_b64_tr_b16 v[98:99], v3 offset:16640\n"
    "ds_read_b64 v[52:53], v13 offset:2048\n"
    "s_nop 3\n"
    "v_mfma_f32_16x16x32_bf16 v[148:151], v[76:79], v[224:227], v[148:151]\n"
    "ds_read_b64_tr_b16 v[100:101], v6 offset:16384\n"
    "ds_read_b64 v[54:55], v14 offset:2048\n"
    "v_mfma_f32_16x16x32_bf16 v[156:159], v[76:79], v[228:231], v[156:159]\n"
    "s_waitcnt lgkmcnt(14)\n"
    "ds_read_b64_tr_b16 v[102:103], v6 offset:16640\n"
    "s_nop 3\n"
    "s_waitcnt lgkmcnt(12)\n"
    "v_mfma_f32_16x16x32_bf16 v[160:163], v[80:83], v[224:227], v[160:163]\n"
    "ds_read_b64_tr_b16 v[104:105], v3 offset:16896\n"
    "buffer_load_dword off, s[88:91], 0 offset:76 lds\n"
    "v_mfma_f32_16x16x32_bf16 v[168:171], v[80:83], v[228:231], v[168:171]\n"
    "ds_read_b64_tr_b16 v[106:107], v3 offset:17152\n"
    "ds_read_b64 v[56:57], v13 offset:512\n"
    "s_nop 3\n"
    "v_mfma_f32_16x16x32_bf16 v[164:167], v[84:87], v[224:227], v[164:167]\n"
    "s_waitcnt lgkmcnt(14)\n"
    "ds_read_b64_tr_b16 v[108:109], v6 offset:16896\n"
    "s_waitcnt lgkmcnt(14)\n"
    "ds_read_b64 v[58:59], v14 offset:512\n"
    "v_mfma_f32_16x16x32_bf16 v[172:175], v[84:87], v[228:231], v[172:175]\n"
    "s_waitcnt lgkmcnt(14)\n"
    "ds_read_b64_tr_b16 v[110:111], v6 offset:17152\n"
    "s_nop 3\n"
    "s_waitcnt lgkmcnt(13)\n"
    "v_mfma_f32_16x16x32_bf16 v[176:179], v[88:91], v[224:227], v[176:179]\n"
    "ds_read_b64_tr_b16 v[64:65], v2 offset:36864\n"
    "ds_read_b64 v[60:61], v13 offset:2560\n"
    "v_mfma_f32_16x16x32_bf16 v[184:187], v[88:91], v[228:231], v[184:187]\n"
    "s_waitcnt lgkmcnt(14)\n"
    "ds_read_b64_tr_b16 v[66:67], v2 offset:37120\n"
    "s_waitcnt lgkmcnt(14)\n"
    "ds_read_b64 v[62:63], v14 offset:2560\n"
    "s_nop 3\n"
    "v_mfma_f32_16x16x32_bf16 v[180:183], v[92:95], v[224:227], v[180:183]\n"
    "s_waitcnt lgkmcnt(14)\n"
    "ds_read_b64_tr_b16 v[68:69], v5 offset:36864\n"
    "v_mfma_f32_16x16x32_bf16 v[188:191], v[92:95], v[228:231], v[188:191]\n"
    "s_waitcnt lgkmcnt(14)\n"
    "ds_read_b64_tr_b16 v[70:71], v5 offset:37120\n"
    "s_nop 3\n"
    "s_waitcnt lgkmcnt(12)\n"
    "v_mfma_f32_16x16x32_bf16 v[192:195], v[96:99], v[224:227], v[192:195]\n"
    "ds_read_b64_tr_b16 v[72:73], v2 offset:37376\n"
    "v_mfma_f32_16x16x32_bf16 v[200:203], v[96:99], v[228:231], v[200:203]\n"
    "ds_read_b64_tr_b16 v[74:75], v2 offset:37632\n"
    "s_nop 3\n"
    "v_mfma_f32_16x16x32_bf16 v[196:199], v[100:103], v[224:227], v[196:199]\n"
    "ds_read_b64_tr_b16 v[76:77], v5 offset:37376\n"
    "v_mfma_f32_16x16x32_bf16 v[204:207], v[100:103], v[228:231], v[204:207]\n"
    "s_waitcnt lgkmcnt(14)\n"
    "ds_read_b64_tr_b16 v[78:79], v5 offset:37632\n"
    "s_nop 3\n"
    "s_waitcnt lgkmcnt(10)\n"
    "v_mfma_f32_16x16x32_bf16 v[208:211], v[104:107], v[224:227], v[208:211]\n"
    "ds_read_b64_tr_b16 v[80:81], v2 offset:45056\n"
    "v_mfma_f32_16x16x32_bf16 v[216:219], v[104:107], v[228:231], v[216:219]\n"
    "ds_read_b64_tr_b16 v[82:83], v2 offset:45312\n"
    "s_nop 3\n"
    "v_mfma_f32_16x16x32_bf16 v[212:215], v[108:111], v[224:227], v[212:215]\n"
    "ds_read_b64_tr_b16 v[84:85], v5 offset:45056\n"
    "v_mfma_f32_16x16x32_bf16 v[220:223], v[108:111], v[228:231], v[220:223]\n"
    "ds_read_b64_tr_b16 v[86:87], v5 offset:45312\n"
    "s_nop 3\n"
    "s_waitcnt lgkmcnt(8)\n"
    "v_mfma_f32_16x16x32_bf16 v[128:131], v[64:67], v[232:235], v[128:131]\n"
    "ds_read_b64_tr_b16 v[88:89], v2 offset:45568\n"
    "v_mfma_f32_16x16x32_bf16 v[136:139], v[64:67], v[236:239], v[136:139]\n"
    "ds_read_b64_tr_b16 v[90:91], v2 offset:45824\n"
    "s_nop 3\n"
    "v_mfma_f32_16x16x32_bf16 v[132:135], v[68:71], v[232:235], v[132:135]\n"
    "ds_read_b64_tr_b16 v[92:93], v5 offset:45568\n"
    "v_mfma_f32_16x16x32_bf16 v[140:143], v[68:71], v[236:239], v[140:143]\n"
    "ds_read_b64_tr_b16 v[94:95], v5 offset:45824\n"
    "s_nop 3\n"
    "s_waitcnt lgkmcnt(8)\n"
    "v_mfma_f32_16x16x32_bf16 v[144:147], v[72:75], v[232:235], v[144:147]\n"
    "ds_read_b64_tr_b16 v[96:97], v3 offset:20480\n"
    "v_mfma_f32_16x16x32_bf16 v[152:155], v[72:75], v[236:239], v[152:155]\n"
    "ds_read_b64_tr_b16 v[98:99], v3 offset:20736\n"
    "s_nop 3\n"
    "v_mfma_f32_16x16x32_bf16 v[148:151], v[76:79], v[232:235], v[148:151]\n"
    "ds_read_b64_tr_b16 v[100:101], v6 offset:20480\n"
    "v_mfma_f32_16x16x32_bf16 v[156:159], v[76:79], v[236:239], v[156:159]\n"
    "ds_read_b64_tr_b16 v[102:103], v6 offset:20736\n"
    "s_nop 3\n"
    "s_waitcnt lgkmcnt(8)\n"
    "v_mfma_f32_16x16x32_bf16 v[160:163], v[80:83], v[232:235], v[160:163]\n"
    "ds_read_b64_tr_b16 v[104:105], v3 offset:20992\n"
    "v_mfma_f32_16x16x32_bf16 v[168:171], v[80:83], v[236:239], v[168:171]\n"
    "ds_read_b64_tr_b16 v[106:107], v3 offset:21248\n"
    "s_nop 3\n"
    "v_mfma_f32_16x16x32_bf16 v[164:167], v[84:87], v[232:235], v[164:167]\n"
    "ds_read_b64_tr_b16 v[108:109], v6 offset:20992\n"
    "v_mfma_f32_16x16x32_bf16 v[172:175], v[84:87], v[236:239], v[172:175]\n"
    "ds_read_b64_tr_b16 v[110:111], v6 offset:21248\n"
    "s_nop 3\n"
    "s_waitcnt lgkmcnt(8)\n"
    "v_mfma_f32_16x16x32_bf16 v[176:179], v[88:91], v[232:235], v[176:179]\n"
    "v_mfma_f32_16x16x32_bf16 v[184:187], v[88:91], v[236:239], v[184:187]\n"
    "s_nop 3\n"
    "v_mfma_f32_16x16x32_bf16 v[180:183], v[92:95], v[232:235], v[180:183]\n"
    "v_mfma_f32_16x16x32_bf16 v[188:191], v[92:95], v[236:239], v[188:191]\n"
    "s_nop 3\n"
    "s_waitcnt lgkmcnt(4)\n"
    "v_mfma_f32_16x16x32_bf16 v[192:195], v[96:99], v[232:235], v[192:195]\n"
    "v_mfma_f32_16x16x32_bf16 v[200:203], v[96:99], v[236:239], v[200:203]\n"
    "s_nop 3\n"
    "v_mfma_f32_16x16x32_bf16 v[196:199], v[100:103], v[232:235], v[196:199]\n"
    "s_waitcnt lgkmcnt(0)\n"
    "buffer_load_dword off, s[88:91], 0 offset:80 lds\n"
    "v_mfma_f32_16x16x32_bf16 v[204:207], v[100:103], v[236:239], v[204:207]\n"
    "s_nop 3\n"
    "v_mfma_f32_16x16x32_bf16 v[208:211], v[104:107], v[232:235], v[208:211]\n"
    "v_mfma_f32_16x16x32_bf16 v[216:219], v[104:107], v[236:239], v[216:219]\n"
    "s_nop 3\n"
    "v_mfma_f32_16x16x32_bf16 v[212:215], v[108:111], v[232:235], v[212:215]\n"
    "v_mfma_f32_16x16x32_bf16 v[220:223], v[108:111], v[236:239], v[220:223]\n"
    "s_nop 3\n"
    "v_cvt_pk_bf16_f32 v128, v128, v129\n"
    "v_cvt_pk_bf16_f32 v129, v130, v131\n"
    "v_cvt_pk_bf16_f32 v130, v132, v133\n"
    "v_cvt_pk_bf16_f32 v131, v134, v135\n"
    "v_cvt_pk_bf16_f32 v136, v136, v137\n"
    "v_cvt_pk_bf16_f32 v137, v138, v139\n"
    "v_cvt_pk_bf16_f32 v138, v140, v141\n"
    "v_cvt_pk_bf16_f32 v139, v142, v143\n"
    "v_cvt_pk_bf16_f32 v144, v144, v145\n"
    "v_cvt_pk_bf16_f32 v145, v146, v147\n"
    "v_mfma_f32_16x16x32_bf16 v[240:243], v[16:19], v[128:131], 0\n"
    "v_cvt_pk_bf16_f32 v146, v148, v149\n"
    "v_cvt_pk_bf16_f32 v147, v150, v151\n"
    "v_mfma_f32_16x16x32_bf16 v[244:247], v[20:23], v[128:131], 0\n"
    "v_cvt_pk_bf16_f32 v152, v152, v153\n"
    "v_cvt_pk_bf16_f32 v153, v154, v155\n"
    "v_mfma_f32_16x16x32_bf16 v[248:251], v[16:19], v[136:139], 0\n"
    "v_cvt_pk_bf16_f32 v154, v156, v157\n"
    "v_cvt_pk_bf16_f32 v155, v158, v159\n"
    "v_mfma_f32_16x16x32_bf16 v[252:255], v[20:23], v[136:139], 0\n"
    "v_cvt_pk_bf16_f32 v160, v160, v161\n"
    "v_cvt_pk_bf16_f32 v161, v162, v163\n"
    "v_mfma_f32_16x16x32_bf16 v[240:243], v[24:27], v[144:147], v[240:243]\n"
    "v_cvt_pk_bf16_f32 v162, v164, v165\n"
    "v_cvt_pk_bf16_f32 v163, v166, v167\n"
    "v_mfma_f32_16x16x32_bf16 v[244:247], v[28:31], v[144:147], v[244:247]\n"
    "v_cvt_pk_bf16_f32 v168, v168, v169\n"
    "v_cvt_pk_bf16_f32 v169, v170, v171\n"
    "v_mfma_f32_16x16x32_bf16 v[248:251], v[24:27], v[152:155], v[248:251]\n"
    "v_cvt_pk_bf16_f32 v170, v172, v173\n"
    "v_cvt_pk_bf16_f32 v171, v174, v175\n"
    "v_mfma_f32_16x16x32_bf16 v[252:255], v[28:31], v[152:155], v[252:255]\n"
    "v_cvt_pk_bf16_f32 v176, v176, v177\n"
    "v_cvt_pk_bf16_f32 v177, v178, v179\n"
    "v_mfma_f32_16x16x32_bf16 v[240:243], v[32:35], v[160:163], v[240:243]\n"
    "v_cvt_pk_bf16_f32 v178, v180, v181\n"
    "v_cvt_pk_bf16_f32 v179, v182, v183\n"
    "v_mfma_f32_16x16x32_bf16 v[244:247], v[36:39], v[160:163], v[244:247]\n"
    "v_cvt_pk_bf16_f32 v184, v184, v185\n"
    "v_cvt_pk_bf16_f32 v185, v186, v187\n"
    "v_mfma_f32_16x16x32_bf16 v[248:251], v[32:35], v[168:171], v[248:251]\n"
    "v_cvt_pk_bf16_f32 v186, v188, v189\n"
    "v_cvt_pk_bf16_f32 v187, v190, v191\n"
    "v_mfma_f32_16x16x32_bf16 v[252:255], v[36:39], v[168:171], v[252:255]\n"
    "v_cvt_pk_bf16_f32 v192, v192, v193\n"
    "v_cvt_pk_bf16_f32 v193, v194, v195\n"
    "v_mfma_f32_16x16x32_bf16 v[240:243], v[40:43], v[176:179], v[240:243]\n"
    "v_cvt_pk_bf16_f32 v194, v196, v197\n"
    "v_cvt_pk_bf16_f32 v195, v198, v199\n"
    "v_mfma_f32_16x16x32_bf16 v[244:247], v[44:47], v[176:179], v[244:247]\n"
    "v_cvt_pk_bf16_f32 v200, v200, v201\n"
    "v_cvt_pk_bf16_f32 v201, v202, v203\n"
    "v_mfma_f32_16x16x32_bf16 v[248:251], v[40:43], v[184:187], v[248:251]\n"
    "v_cvt_pk_bf16_f32 v202, v204, v205\n"
    "v_cvt_pk_bf16_f32 v203, v206, v207\n"
    "v_mfma_f32_16x16x32_bf16 v[252:255], v[44:47], v[184:187], v[252:255]\n"
    "v_cvt_pk_bf16_f32 v208, v208, v209\n"
    "v_cvt_pk_bf16_f32 v209, v210, v211\n"
    "v_mfma_f32_16x16x32_bf16 v[240:243], v[48:51], v[192:195], v[240:243]\n"
    "v_cvt_pk_bf16_f32 v210, v212, v213\n"
    "v_cvt_pk_bf16_f32 v211, v214, v215\n"
    "v_mfma_f32_16x16x32_bf16 v[244:247], v[52:55], v[192:195], v[244:247]\n"
    "v_cvt_pk_bf16_f32 v216, v216, v217\n"
    "v_cvt_pk_bf16_f32 v217, v218, v219\n"
    "v_mfma_f32_16x16x32_bf16 v[248:251], v[48:51], v[200:203], v[248:251]\n"
    "v_cvt_pk_bf16_f32 v218, v220, v221\n"
    "v_cvt_pk_bf16_f32 v219, v222, v223\n"
    "v_mfma_f32_16x16x32_bf16 v[252:255], v[52:55], v[200:203], v[252:255]\n"
    "v_mfma_f32_16x16x32_bf16 v[240:243], v[56:59], v[208:211], v[240:243]\n"
    "v_mfma_f32_16x16x32_bf16 v[244:247], v[60:63], v[208:211], v[244:247]\n"
    "v_mfma_f32_16x16x32_bf16 v[248:251], v[56:59], v[216:219], v[248:251]\n"
    "v_mfma_f32_16x16x32_bf16 v[252:255], v[60:63], v[216:219], v[252:255]\n"
    "s_mov_b32 s72, 0x3dd53b95\n"
    "s_nop 3\n"
    "v_max3_f32 v224, v240, v241, v242\n"
    "v_max3_f32 v226, v243, v244, v245\n"
    "v_max3_f32 v225, v248, v249, v250\n"
    "v_max3_f32 v227, v251, v252, v253\n"
    "v_max3_f32 v224, v224, v246, v247\n"
    "v_max3_f32 v225, v225, v254, v255\n"
    "v_max_f32 v224, v224, v226\n"
    "v_max_f32 v225, v225, v227\n"
    "s_nop 1\n"
    "v_permlane32_swap_b32 v224, v225\n"
    "v_max_f32 v224, v224, v225\n"
    "v_mov_b32 v225, v224\n"
    "s_nop 1\n"
    "v_permlane16_swap_b32 v224, v225\n"
    "v_max_f32 v224, v224, v225\n"
    "v_mul_f32 v224, s72, v224\n"
    "v_mov_b32 v225, v224\n"
    "s_nop 1\n"
    "v_permlane32_swap_b32 v224, v225\n"
    "v_fma_f32 v240, v240, s72, -v224\n"
    "v_fma_f32 v241, v241, s72, -v224\n"
    "v_fma_f32 v242, v242, s72, -v224\n"
    "v_fma_f32 v243, v243, s72, -v224\n"
    "v_fma_f32 v244, v244, s72, -v224\n"
    "v_fma_f32 v245, v245, s72, -v224\n"
    "v_fma_f32 v246, v246, s72, -v224\n"
    "v_fma_f32 v247, v247, s72, -v224\n"
    "v_fma_f32 v248, v248, s72, -v225\n"
    "v_fma_f32 v249, v249, s72, -v225\n"
    "v_fma_f32 v250, v250, s72, -v225\n"
    "v_fma_f32 v251, v251, s72, -v225\n"
    "v_fma_f32 v252, v252, s72, -v225\n"
    "v_fma_f32 v253, v253, s72, -v225\n"
    "v_fma_f32 v254, v254, s72, -v225\n"
    "v_fma_f32 v255, v255, s72, -v225\n"
    "v_exp_f32 v240, v240\n"
    "v_exp_f32 v241, v241\n"
    "v_exp_f32 v242, v242\n"
    "v_exp_f32 v243, v243\n"
    "v_exp_f32 v244, v244\n"
    "v_exp_f32 v245, v245\n"
    "v_exp_f32 v246, v246\n"
    "v_exp_f32 v247, v247\n"
    "v_exp_f32 v248, v248\n"
    "v_exp_f32 v249, v249\n"
    "v_exp_f32 v250, v250\n"
    "v_exp_f32 v251, v251\n"
    "v_exp_f32 v252, v252\n"
    "v_exp_f32 v253, v253\n"
    "v_exp_f32 v254, v254\n"
    "v_exp_f32 v255, v255\n"
    "v_add_f32 v224, v240, v241\n"
    "v_add_f32 v226, v242, v243\n"
    "v_add_f32 v225, v248, v249\n"
    "v_add_f32 v227, v250, v251\n"
    "v_add_f32 v224, v224, v244\n"
    "v_add_f32 v226, v226, v245\n"
    "v_add_f32 v225, v225, v252\n"
    "v_add_f32 v227, v227, v253\n"
    "v_add_f32 v224, v224, v246\n"
    "v_add_f32 v226, v226, v247\n"
    "v_add_f32 v225, v225, v254\n"
    "v_add_f32 v227, v227, v255\n"
    "v_add_f32 v224, v224, v226\n"
    "v_add_f32 v225, v225, v227\n"
    "s_nop 1\n"
    "v_permlane32_swap_b32 v224, v225\n"
    "v_add_f32 v224, v224, v225\n"
    "v_mov_b32 v225, v224\n"
    "s_nop 1\n"
    "v_permlane16_swap_b32 v224, v225\n"
    "v_add_f32 v224, v224, v225\n"
    "v_rcp_f32 v224, v224\n"
    "s_nop 0\n"
    "v_mov_b32 v225, v224\n"
    "s_nop 1\n"
    "v_permlane32_swap_b32 v224, v225\n"
    "v_mul_f32 v240, v224, v240\n"
    "v_mul_f32 v241, v224, v241\n"
    "v_mul_f32 v242, v224, v242\n"
    "v_mul_f32 v243, v224, v243\n"
    "v_mul_f32 v244, v224, v244\n"
    "v_mul_f32 v245, v224, v245\n"
    "v_mul_f32 v246, v224, v246\n"
    "v_mul_f32 v247, v224, v247\n"
    "v_mul_f32 v248, v225, v248\n"
    "v_mul_f32 v249, v225, v249\n"
    "v_mul_f32 v250, v225, v250\n"
    "v_mul_f32 v251, v225, v251\n"
    "v_mul_f32 v252, v225, v252\n"
    "v_mul_f32 v253, v225, v253\n"
    "v_mul_f32 v254, v225, v254\n"
    "v_mul_f32 v255, v225, v255\n"
    "v_cvt_pk_bf16_f32 v240, v240, v241\n"
    "v_cvt_pk_bf16_f32 v241, v242, v243\n"
    "v_cvt_pk_bf16_f32 v242, v244, v245\n"
    "v_cvt_pk_bf16_f32 v243, v246, v247\n"
    "v_cvt_pk_bf16_f32 v244, v248, v249\n"
    "v_cvt_pk_bf16_f32 v245, v250, v251\n"
    "v_cvt_pk_bf16_f32 v246, v252, v253\n"
    "v_cvt_pk_bf16_f32 v247, v254, v255\n"
    "buffer_store_dwordx4 v[240:243], v11, s[48:51], s14 offen sc1\n"
    "buffer_store_dwordx4 v[244:247], v11, s[48:51], s14 offen offset:1024 sc1\n"
    "v_and_b32 v12, 15, v0\n"
    "v_lshlrev_b32 v12, 3, v12\n"
    "v_bfe_u32 v13, v0, 4, 1\n"
    "v_lshl_add_u32 v12, v13, 10, v12\n"
    "v_lshrrev_b32 v13, 5, v0\n"
    "v_lshl_add_u32 v12, v13, 14, v12\n"
    "ds_read_b32 v9, v7\n"
    "s_waitcnt lgkmcnt(0)\n"
    "v_cmp_gt_u32 vcc, 0xb000000b, v9\n"
    "s_cbranch_vccnz CS11_%=\n"
    "CB11_%=:\n"
    "v_add_u32 v10, 114688, v8\n"
    "ds_read_b128 v[64:67], v10 offset:0\n"
    "ds_read_b128 v[68:71], v10 offset:2048\n"
    "ds_read_b64_tr_b16 v[112:113], v2 offset:0\n"
    "ds_read_b64_tr_b16 v[114:115], v2 offset:256\n"
    "ds_read_b64_tr_b16 v[116:117], v5 offset:0\n"
    "ds_read_b64_tr_b16 v[118:119], v5 offset:256\n"
    "ds_read_b128 v[72:75], v10 offset:512\n"
    "ds_read_b128 v[76:79], v10 offset:2560\n"
    "ds_read_b64_tr_b16 v[120:121], v2 offset:4096\n"
    "ds_read_b64_tr_b16 v[122:123], v2 offset:4352\n"
    "ds_read_b64_tr_b16 v[124:125], v5 offset:4096\n"
    "ds_read_b64_tr_b16 v[126:127], v5 offset:4352\n"
    "v_cmp_gt_u32 vcc, 0xb000000c, v9\n"
    "s_cbranch_vccnz CS12_%=\n"
    "CB12_%=:\n"
    "v_add_u32 v10, 0, v8\n"
    "ds_read_b128 v[80:83], v10 offset:0\n"
    "ds_read_b128 v[84:87], v10 offset:2048\n"
    "ds_read_b64_tr_b16 v[208:209], v1 offset:16384\n"
    "s_waitcnt lgkmcnt(14)\n"
    "ds_read_b64_tr_b16 v[210:211], v1 offset:16640\n"
    "s_waitcnt lgkmcnt(14)\n"
    "ds_read_b64_tr_b16 v[212:213], v4 offset:16384\n"
    "s_waitcnt lgkmcnt(14)\n"
    "ds_read_b64_tr_b16 v[214:215], v4 offset:16640\n"
    "s_waitcnt lgkmcnt(14)\n"
    "v_mfma_f32_16x16x32_bf16 v[128:131], v[64:67], v[112:115], 0\n"
    "ds_read_b128 v[88:91], v10 offset:512\n"
    "s_waitcnt lgkmcnt(14)\n"
    "ds_read_b128 v[92:95], v10 offset:2560\n"
    "v_mfma_f32_16x16x32_bf16 v[132:135], v[68:71], v[112:115], 0\n"
    "s_waitcnt lgkmcnt(14)\n"
    "ds_read_b64_tr_b16 v[216:217], v1 offset:20480\n"
    "s_waitcnt lgkmcnt(14)\n"
    "ds_read_b64_tr_b16 v[218:219], v1 offset:20736\n"
    "v_mfma_f32_16x16x32_bf16 v[136:139], v[64:67], v[116:119], 0\n"
    "s_waitcnt lgkmcnt(14)\n"
    "ds_read_b64_tr_b16 v[220:221], v4 offset:20480\n"
    "v_mfma_f32_16x16x32_bf16 v[140:143], v[68:71], v[116:119], 0\n"
    "s_waitcnt lgkmcnt(14)\n"
    "ds_read_b64_tr_b16 v[222:223], v4 offset:20736\n"
    "s_waitcnt lgkmcnt(14)\n"
    "v_mfma_f32_16x16x32_bf16 v[128:131], v[72:75], v[120:123], v[128:131]\n"
    "v_cmp_gt_u32 vcc, 0xb000000d, v9\n"
    "s_cbranch_vccnz CS13_%=\n"
    "CB13_%=:\n"
    "v_add_u32 v10, 32768, v8\n"
    "ds_read_b128 v[96:99], v10 offset:0\n"
    "v_mfma_f32_16x16x32_bf16 v[132:135], v[76:79], v[120:123], v[132:135]\n"
    "s_waitcnt lgkmcnt(14)\n"
    "ds_read_b128 v[100:103], v10 offset:2048\n"
    "s_waitcnt lgkmcnt(14)\n"
    "ds_read_b64_tr_b16 v[112:113], v1 offset:24576\n"
    "s_waitcnt lgkmcnt(14)\n"
    "ds_read_b64_tr_b16 v[114:115], v1 offset:24832\n"
    "v_mfma_f32_16x16x32_bf16 v[136:139], v[72:75], v[124:127], v[136:139]\n"
    "s_waitcnt lgkmcnt(14)\n"
    "ds_read_b64_tr_b16 v[116:117], v4 offset:24576\n"
    "v_mfma_f32_16x16x32_bf16 v[140:143], v[76:79], v[124:127], v[140:143]\n"
    "s_waitcnt lgkmcnt(14)\n"
    "ds_read_b64_tr_b16 v[118:119], v4 offset:24832\n"
    "s_waitcnt lgkmcnt(14)\n"
    "v_mfma_f32_16x16x32_bf16 v[128:131], v[80:83], v[208:211], v[128:131]\n"
    "ds_read_b128 v[104:107], v10 offset:512\n"
    "s_waitcnt lgkmcnt(14)\n"
    "ds_read_b128 v[108:111], v10 offset:2560\n"
    "v_mfma_f32_16x16x32_bf16 v[132:135], v[84:87], v[208:211], v[132:135]\n"
    "s_waitcnt lgkmcnt(14)\n"
    "ds_read_b64_tr_b16 v[120:121], v1 offset:28672\n"
    "s_waitcnt lgkmcnt(14)\n"
    "ds_read_b64_tr_b16 v[122:123], v1 offset:28928\n"
    "v_mfma_f32_16x16x32_bf16 v[136:139], v[80:83], v[212:215], v[136:139]\n"
    "s_waitcnt lgkmcnt(14)\n"
    "ds_read_b64_tr_b16 v[124:125], v4 offset:28672\n"
    "v_mfma_f32_16x16x32_bf16 v[140:143], v[84:87], v[212:215], v[140:143]\n"
    "s_waitcnt lgkmcnt(14)\n"
    "ds_read_b64_tr_b16 v[126:127], v4 offset:28928\n"
    "s_waitcnt lgkmcnt(14)\n"
    "v_mfma_f32_16x16x32_bf16 v[128:131], v[88:91], v[216:219], v[128:131]\n"
    "ds_read_b64_tr_b16 v[208:209], v2 offset:512\n"
    "v_mfma_f32_16x16x32_bf16 v[132:135], v[92:95], v[216:219], v[132:135]\n"
    "s_waitcnt lgkmcnt(14)\n"
    "ds_read_b64_tr_b16 v[210:211], v2 offset:768\n"
    "s_waitcnt lgkmcnt(14)\n"
    "v_mfma_f32_16x16x32_bf16 v[136:139], v[88:91], v[220:223], v[136:139]\n"
    "ds_read_b64_tr_b16 v[212:213], v5 offset:512\n"
    "v_mfma_f32_16x16x32_bf16 v[140:143], v[92:95], v[220:223], v[140:143]\n"
    "s_waitcnt lgkmcnt(14)\n"
    "ds_read_b64_tr_b16 v[214:215], v5 offset:768\n"
    "s_waitcnt lgkmcnt(12)\n"
    "v_mfma_f32_16x16x32_bf16 v[128:131], v[96:99], v[112:115], v[128:131]\n"
    "ds_read_b64_tr_b16 v[216:217], v2 offset:4608\n"
    "v_mfma_f32_16x16x32_bf16 v[132:135], v[100:103], v[112:115], v[132:135]\n"
    "ds_read_b64_tr_b16 v[218:219], v2 offset:4864\n"
    "s_waitcnt lgkmcnt(12)\n"
    "v_mfma_f32_16x16x32_bf16 v[136:139], v[96:99], v[116:119], v[136:139]\n"
    "ds_read_b64_tr_b16 v[220:221], v5 offset:4608\n"
    "v_mfma_f32_16x16x32_bf16 v[140:143], v[100:103], v[116:119], v[140:143]\n"
    "ds_read_b64_tr_b16 v[222:223], v5 offset:4864\n"
    "s_waitcnt lgkmcnt(10)\n"
    "v_mfma_f32_16x16x32_bf16 v[128:131], v[104:107], v[120:123], v[128:131]\n"
    "ds_read_b64_tr_b16 v[112:113], v1 offset:16896\n"
    "v_lshlrev_b32 v13, 3, v0\n"
    "v_mfma_f32_16x16x32_bf16 v[132:135], v[108:111], v[120:123], v[132:135]\n"
    "ds_read_b64_tr_b16 v[114:115], v1 offset:17152\n"
    "s_waitcnt lgkmcnt(10)\n"
    "v_mfma_f32_16x16x32_bf16 v[136:139], v[104:107], v[124:127], v[136:139]\n"
    "ds_read_b64_tr_b16 v[116:117], v4 offset:16896\n"
    "v_mov_b32 v14, s6\n"
    "v_mfma_f32_16x16x32_bf16 v[140:143], v[108:111], v[124:127], v[140:143]\n"
    "ds_read_b64_tr_b16 v[118:119], v4 offset:17152\n"
    "s_waitcnt lgkmcnt(8)\n"
    "v_mfma_f32_16x16x32_bf16 v[144:147], v[64:67], v[208:211], 0\n"
    "ds_read_b64_tr_b16 v[120:121], v1 offset:20992\n"
    "ds_read_b64_tr_b16 v[122:123], v1 offset:21248\n"
    "v_mfma_f32_16x16x32_bf16 v[148:151], v[68:71], v[208:211], 0\n"
    "ds_read_b32 v9, v7\n"
    "v_mov_b32 v15, s7\n"
    "v_mfma_f32_16x16x32_bf16 v[152:155], v[64:67], v[212:215], 0\n"
    "ds_read_b64_tr_b16 v[124:125], v4 offset:20992\n"
    "s_waitcnt vmcnt(0)\n"
    "s_mov_b64 exec, 0xffff\n"
    "buffer_store_dwordx2 v[14:15], v13, s[48:51], s15 offen sc1\n"
    "s_mov_b64 exec, -1\n"
    "v_mfma_f32_16x16x32_bf16 v[156:159], v[68:71], v[212:215], 0\n"
    "ds_read_b64_tr_b16 v[126:127], v4 offset:21248\n"
    "s_waitcnt lgkmcnt(9)\n"
    "v_mfma_f32_16x16x32_bf16 v[144:147], v[72:75], v[216:219], v[144:147]\n"
    "ds_read_b64_tr_b16 v[208:209], v1 offset:25088\n"
    "v_mfma_f32_16x16x32_bf16 v[148:151], v[76:79], v[216:219], v[148:151]\n"
    "ds_read_b64_tr_b16 v[210:211], v1 offset:25344\n"
    "v_mfma_f32_16x16x32_bf16 v[152:155], v[72:75], v[220:223], v[152:155]\n"
    "ds_read_b64_tr_b16 v[212:213], v4 offset:25088\n"
    "v_mfma_f32_16x16x32_bf16 v[156:159], v[76:79], v[220:223], v[156:159]\n"
    "ds_read_b64_tr_b16 v[214:215], v4 offset:25344\n"
    "s_waitcnt lgkmcnt(9)\n"
    "v_mfma_f32_16x16x32_bf16 v[144:147], v[80:83], v[112:115], v[144:147]\n"
    "ds_read_b64_tr_b16 v[216:217], v1 offset:29184\n"
    "v_mfma_f32_16x16x32_bf16 v[148:151], v[84:87], v[112:115], v[148:151]\n"
    "ds_read_b64_tr_b16 v[218:219], v1 offset:29440\n"
    "buffer_load_dword off, s[88:91], 0 offset:88 lds\n"
    "v_mfma_f32_16x16x32_bf16 v[152:155], v[80:83], v[116:119], v[152:155]\n"
    "ds_read_b64_tr_b16 v[220:221], v4 offset:29184\n"
    "buffer_load_dword off, s[88:91], 0 offset:92 lds\n"
    "v_mfma_f32_16x16x32_bf16 v[156:159], v[84:87], v[116:119], v[156:159]\n"
    "ds_read_b64_tr_b16 v[222:223], v4 offset:29440\n"
    "s_waitcnt lgkmcnt(10)\n"
    "v_cmp_gt_u32 vcc, 0xb000000e, v9\n"
    "s_waitcnt lgkmcnt(8)\n"
    "v_mfma_f32_16x16x32_bf16 v[144:147], v[88:91], v[120:123], v[144:147]\n"
    "s_cbranch_vccnz CS14_%=\n"
    "CB14_%=:\n"
    "ds_read_b64_tr_b16 v[112:113], v2 offset:8192\n"
    "v_mfma_f32_16x16x32_bf16 v[148:151], v[92:95], v[120:123], v[148:151]\n"
    "ds_read_b64_tr_b16 v[114:115], v2 offset:8448\n"
    "v_cvt_pk_bf16_f32 v128, v128, v129\n"
    "v_mfma_f32_16x16x32_bf16 v[152:155], v[88:91], v[124:127], v[152:155]\n"
    "ds_read_b64_tr_b16 v[116:117], v5 offset:8192\n"
    "v_cvt_pk_bf16_f32 v129, v130, v131\n"
    "v_mfma_f32_16x16x32_bf16 v[156:159], v[92:95], v[124:127], v[156:159]\n"
    "ds_read_b64_tr_b16 v[118:119], v5 offset:8448\n"
    "s_waitcnt lgkmcnt(8)\n"
    "v_mfma_f32_16x16x32_bf16 v[144:147], v[96:99], v[208:211], v[144:147]\n"
    "ds_read_b64_tr_b16 v[120:121], v2 offset:12288\n"
    "v_mfma_f32_16x16x32_bf16 v[148:151], v[100:103], v[208:211], v[148:151]\n"
    "ds_read_b64_tr_b16 v[122:123], v2 offset:12544\n"
    "v_cvt_pk_bf16_f32 v130, v132, v133\n"
    "v_mfma_f32_16x16x32_bf16 v[152:155], v[96:99], v[212:215], v[152:155]\n"
    "ds_read_b64_tr_b16 v[124:125], v5 offset:12288\n"
    "v_cvt_pk_bf16_f32 v131, v134, v135\n"
    "v_mfma_f32_16x16x32_bf16 v[156:159], v[100:103], v[212:215], v[156:159]\n"
    "ds_read_b64_tr_b16 v[126:127], v5 offset:12544\n"
    "s_waitcnt lgkmcnt(8)\n"
    "v_mfma_f32_16x16x32_bf16 v[144:147], v[104:107], v[216:219], v[144:147]\n"
    "ds_read_b64_tr_b16 v[208:209], v1 offset:49152\n"
    "v_cvt_pk_bf16_f32 v132, v136, v137\n"
    "v_mfma_f32_16x16x32_bf16 v[148:151], v[108:111], v[216:219], v[148:151]\n"
    "ds_read_b64_tr_b16 v[210:211], v1 offset:49408\n"
    "v_cvt_pk_bf16_f32 v133, v138, v139\n"
    "v_mfma_f32_16x16x32_bf16 v[152:155], v[104:107], v[220:223], v[152:155]\n"
    "ds_read_b64_tr_b16 v[212:213], v4 offset:49152\n"
    "v_mfma_f32_16x16x32_bf16 v[156:159], v[108:111], v[220:223], v[156:159]\n"
    "ds_read_b64_tr_b16 v[214:215], v4 offset:49408\n"
    "v_cvt_pk_bf16_f32 v134, v140, v141\n"
    "v_cvt_pk_bf16_f32 v135, v142, v143\n"
    "s_waitcnt lgkmcnt(8)\n"
    "v_mfma_f32_16x16x32_bf16 v[160:163], v[64:67], v[112:115], 0\n"
    "ds_read_b64_tr_b16 v[216:217], v1 offset:53248\n"
    "buffer_load_dword off, s[88:91], 0 offset:104 lds\n"
    "v_mfma_f32_16x16x32_bf16 v[164:167], v[68:71], v[112:115], 0\n"
    "ds_read_b64_tr_b16 v[218:219], v1 offset:53504\n"
    "v_mfma_f32_16x16x32_bf16 v[168:171], v[64:67], v[116:119], 0\n"
    "ds_read_b64_tr_b16 v[220:221], v4 offset:53248\n"
    "ds_read_b32 v9, v7\n"
    "v_mfma_f32_16x16x32_bf16 v[172:175], v[68:71], v[116:119], 0\n"
    "ds_read_b64_tr_b16 v[222:223], v4 offset:53504\n"
    "s_waitcnt lgkmcnt(9)\n"
    "v_mfma_f32_16x16x32_bf16 v[160:163], v[72:75], v[120:123], v[160:163]\n"
    "ds_read_b64_tr_b16 v[112:113], v1 offset:57344\n"
    "v_cvt_pk_bf16_f32 v144, v144, v145\n"
    "v_mfma_f32_16x16x32_bf16 v[164:167], v[76:79], v[120:123], v[164:167]\n"
    "ds_read_b64_tr_b16 v[114:115], v1 offset:57600\n"
    "v_cvt_pk_bf16_f32 v145, v146, v147\n"
    "v_mfma_f32_16x16x32_bf16 v[168:171], v[72:75], v[124:127], v[168:171]\n"
    "ds_read_b64_tr_b16 v[116:117], v4 offset:57344\n"
    "v_cvt_pk_bf16_f32 v146, v148, v149\n"
    "v_mfma_f32_16x16x32_bf16 v[172:175], v[76:79], v[124:127], v[172:175]\n"
    "ds_read_b64_tr_b16 v[118:119], v4 offset:57600\n"
    "s_waitcnt lgkmcnt(9)\n"
    "v_mfma_f32_16x16x32_bf16 v[160:163], v[80:83], v[208:211], v[160:163]\n"
    "ds_read_b64_tr_b16 v[120:121], v1 offset:61440\n"
    "v_cvt_pk_bf16_f32 v147, v150, v151\n"
    "v_mfma_f32_16x16x32_bf16 v[164:167], v[84:87], v[208:211], v[164:167]\n"
    "ds_read_b64_tr_b16 v[122:123], v1 offset:61696\n"
    "v_mfma_f32_16x16x32_bf16 v[168:171], v[80:83], v[212:215], v[168:171]\n"
    "ds_read_b64_tr_b16 v[124:125], v4 offset:61440\n"
    "v_cvt_pk_bf16_f32 v148, v152, v153\n"
    "v_mfma_f32_16x16x32_bf16 v[172:175], v[84:87], v[212:215], v[172:175]\n"
    "ds_read_b64_tr_b16 v[126:127], v4 offset:61696\n"
    "s_waitcnt lgkmcnt(8)\n"
    "v_mfma_f32_16x16x32_bf16 v[160:163], v[88:91], v[216:219], v[160:163]\n"
    "ds_read_b64_tr_b16 v[208:209], v2 offset:8704\n"
    "v_cmp_gt_u32 vcc, 0xb000000f, v9\n"
    "v_mfma_f32_16x16x32_bf16 v[164:167], v[92:95], v[216:219], v[164:167]\n"
    "ds_read_b64_tr_b16 v[210:211], v2 offset:8960\n"
    "v_add_u32 v10, 81920, v8\n"
    "v_mfma_f32_16x16x32_bf16 v[168:171], v[88:91], v[220:223], v[168:171]\n"
    "ds_read_b64_tr_b16 v[212:213], v5 offset:8704\n"
    "s_cbranch_vccnz CS15_%=\n"
    "CB15_%=:\n"
    "v_mfma_f32_16x16x32_bf16 v[172:175], v[92:95], v[220:223], v[172:175]\n"
    "ds_read_b64_tr_b16 v[214:215], v5 offset:8960\n"
    "s_waitcnt lgkmcnt(8)\n"
    "v_mfma_f32_16x16x32_bf16 v[160:163], v[96:99], v[112:115], v[160:163]\n"
    "ds_read_b64_tr_b16 v[216:217], v2 offset:12800\n"
    "ds_read_b128 v[16:19], v10 offset:0\n"
    "v_mfma_f32_16x16x32_bf16 v[164:167], v[100:103], v[112:115], v[164:167]\n"
    "ds_read_b64_tr_b16 v[218:219], v2 offset:13056\n"
    "v_cvt_pk_bf16_f32 v149, v154, v155\n"
    "v_mfma_f32_16x16x32_bf16 v[168:171], v[96:99], v[116:119], v[168:171]\n"
    "ds_read_b64_tr_b16 v[220:221], v5 offset:12800\n"
    "ds_read_b128 v[20:23], v10 offset:2048\n"
    "v_mfma_f32_16x16x32_bf16 v[172:175], v[100:103], v[116:119], v[172:175]\n"
    "ds_read_b64_tr_b16 v[222:223], v5 offset:13056\n"
    "s_waitcnt lgkmcnt(10)\n"
    "v_mfma_f32_16x16x32_bf16 v[160:163], v[104:107], v[120:123], v[160:163]\n"
    "ds_read_b64_tr_b16 v[112:113], v1 offset:49664\n"
    "v_cvt_pk_bf16_f32 v150, v156, v157\n"
    "v_mfma_f32_16x16x32_bf16 v[164:167], v[108:111], v[120:123], v[164:167]\n"
    "ds_read_b64_tr_b16 v[114:115], v1 offset:49920\n"
    "ds_read_b128 v[24:27], v10 offset:512\n"
    "v_mfma_f32_16x16x32_bf16 v[168:171], v[104:107], v[124:127], v[168:171]\n"
    "ds_read_b64_tr_b16 v[116:117], v4 offset:49664\n"
    "v_cvt_pk_bf16_f32 v151, v158, v159\n"
    "v_mfma_f32_16x16x32_bf16 v[172:175], v[108:111], v[124:127], v[172:175]\n"
    "ds_read_b64_tr_b16 v[118:119], v4 offset:49920\n"
    "s_waitcnt lgkmcnt(11)\n"
    "v_mfma_f32_16x16x32_bf16 v[176:179], v[64:67], v[208:211], 0\n"
    "ds_read_b64_tr_b16 v[120:121], v1 offset:53760\n"
    "ds_read_b64_tr_b16 v[122:123], v1 offset:54016\n"
    "v_mfma_f32_16x16x32_bf16 v[180:183], v[68:71], v[208:211], 0\n"
    "ds_read_b32 v9, v7\n"
    "ds_read_b128 v[28:31], v10 offset:2560\n"
    "v_mfma_f32_16x16x32_bf16 v[184:187], v[64:67], v[212:215], 0\n"
    "s_waitcnt lgkmcnt(14)\n"
    "ds_read_b64_tr_b16 v[124:125], v4 offset:53760\n"
    "s_waitcnt lgkmcnt(1)\n"
    "buffer_load_dword off, s[88:91], 0 offset:108 lds\n"
    "v_mfma_f32_16x16x32_bf16 v[188:191], v[68:71], v[212:215], 0\n"
    "ds_read_b64_tr_b16 v[126:127], v4 offset:54016\n"
    "v_mfma_f32_16x16x32_bf16 v[176:179], v[72:75], v[216:219], v[176:179]\n"
    "ds_read_b64_tr_b16 v[208:209], v1 offset:57856\n"
    "v_cvt_pk_bf16_f32 v160, v160, v161\n"
    "v_mfma_f32_16x16x32_bf16 v[180:183], v[76:79], v[216:219], v[180:183]\n"
    "ds_read_b64_tr_b16 v[210:211], v1 offset:58112\n"
    "v_cvt_pk_bf16_f32 v161, v162, v163\n"
    "v_mfma_f32_16x16x32_bf16 v[184:187], v[72:75], v[220:223], v[184:187]\n"
    "ds_read_b64_tr_b16 v[212:213], v4 offset:57856\n"
    "buffer_load_dword off, s[88:91], 0 offset:112 lds\n"
    "v_mfma_f32_16x16x32_bf16 v[188:191], v[76:79], v[220:223], v[188:191]\n"
    "ds_read_b64_tr_b16 v[214:215], v4 offset:58112\n"
    "v_mfma_f32_16x16x32_bf16 v[176:179], v[80:83], v[112:115], v[176:179]\n"
    "ds_read_b64_tr_b16 v[216:217], v1 offset:61952\n"
    "v_mfma_f32_16x16x32_bf16 v[180:183], v[84:87], v[112:115], v[180:183]\n"
    "ds_read_b64_tr_b16 v[218:219], v1 offset:62208\n"
    "v_cvt_pk_bf16_f32 v162, v164, v165\n"
    "v_mfma_f32_16x16x32_bf16 v[184:187], v[80:83], v[116:119], v[184:187]\n"
    "ds_read_b64_tr_b16 v[220:221], v4 offset:61952\n"
    "v_cvt_pk_bf16_f32 v163, v166, v167\n"
    "v_mfma_f32_16x16x32_bf16 v[188:191], v[84:87], v[116:119], v[188:191]\n"
    "ds_read_b64_tr_b16 v[222:223], v4 offset:62208\n"
    "v_cmp_gt_u32 vcc, 0xb0000010, v9\n"
    "s_waitcnt lgkmcnt(8)\n"
    "v_mfma_f32_16x16x32_bf16 v[176:179], v[88:91], v[120:123], v[176:179]\n"
    "s_cbranch_vccnz CS16_%=\n"
    "CB16_%=:\n"
    "ds_read_b64_tr_b16 v[112:113], v3 offset:16384\n"
    "v_mfma_f32_16x16x32_bf16 v[180:183], v[92:95], v[120:123], v[180:183]\n"
    "ds_read_b64_tr_b16 v[114:115], v3 offset:16640\n"
    "v_cvt_pk_bf16_f32 v164, v168, v169\n"
    "v_mfma_f32_16x16x32_bf16 v[184:187], v[88:91], v[124:127], v[184:187]\n"
    "ds_read_b64_tr_b16 v[116:117], v6 offset:16384\n"
    "v_cvt_pk_bf16_f32 v165, v170, v171\n"
    "v_mfma_f32_16x16x32_bf16 v[188:191], v[92:95], v[124:127], v[188:191]\n"
    "ds_read_b64_tr_b16 v[118:119], v6 offset:16640\n"
    "s_waitcnt lgkmcnt(8)\n"
    "v_mfma_f32_16x16x32_bf16 v[176:179], v[96:99], v[208:211], v[176:179]\n"
    "ds_read_b64_tr_b16 v[120:121], v3 offset:20480\n"
    "v_mfma_f32_16x16x32_bf16 v[180:183], v[100:103], v[208:211], v[180:183]\n"
    "ds_read_b64_tr_b16 v[122:123], v3 offset:20736\n"
    "v_cvt_pk_bf16_f32 v166, v172, v173\n"
    "v_mfma_f32_16x16x32_bf16 v[184:187], v[96:99], v[212:215], v[184:187]\n"
    "ds_read_b64_tr_b16 v[124:125], v6 offset:20480\n"
    "v_cmp_gt_u32 vcc, 0xb0000011, v9\n"
    "v_mfma_f32_16x16x32_bf16 v[188:191], v[100:103], v[212:215], v[188:191]\n"
    "ds_read_b64_tr_b16 v[126:127], v6 offset:20736\n"
    "s_waitcnt lgkmcnt(8)\n"
    "v_mfma_f32_16x16x32_bf16 v[176:179], v[104:107], v[216:219], v[176:179]\n"
    "ds_read_b64_tr_b16 v[208:209], v2 offset:32768\n"
    "v_add_u32 v10, 131072, v8\n"
    "v_mfma_f32_16x16x32_bf16 v[180:183], v[108:111], v[216:219], v[180:183]\n"
    "ds_read_b64_tr_b16 v[210:211], v2 offset:33024\n"
    "s_cbranch_vccnz CS17_%=\n"
    "CB17_%=:\n"
    "v_mfma_f32_16x16x32_bf16 v[184:187], v[104:107], v[220:223], v[184:187]\n"
    "ds_read_b64_tr_b16 v[212:213], v5 offset:32768\n"
    "ds_read_b128 v[32:35], v10 offset:0\n"
    "v_mfma_f32_16x16x32_bf16 v[188:191], v[108:111], v[220:223], v[188:191]\n"
    "ds_read_b64_tr_b16 v[214:215], v5 offset:33024\n"
    "v_cvt_pk_bf16_f32 v167, v174, v175\n"
    "s_waitcnt lgkmcnt(9)\n"
    "v_mfma_f32_16x16x32_bf16 v[192:195], v[64:67], v[112:115], 0\n"
    "ds_read_b64_tr_b16 v[216:217], v2 offset:36864\n"
    "ds_read_b128 v[36:39], v10 offset:2048\n"
    "v_mfma_f32_16x16x32_bf16 v[196:199], v[68:71], v[112:115], 0\n"
    "ds_read_b64_tr_b16 v[218:219], v2 offset:37120\n"
    "ds_read_b128 v[40:43], v10 offset:512\n"
    "v_mfma_f32_16x16x32_bf16 v[200:203], v[64:67], v[116:119], 0\n"
    "ds_read_b64_tr_b16 v[220:221], v5 offset:36864\n"
    "buffer_load_dword off, s[88:91], 0 offset:120 lds\n"
    "v_mfma_f32_16x16x32_bf16 v[204:207], v[68:71], v[116:119], 0\n"
    "ds_read_b64_tr_b16 v[222:223], v5 offset:37120\n"
    "s_waitcnt lgkmcnt(11)\n"
    "v_mfma_f32_16x16x32_bf16 v[192:195], v[72:75], v[120:123], v[192:195]\n"
    "ds_read_b64_tr_b16 v[112:113], v2 offset:40960\n"
    "ds_read_b128 v[44:47], v10 offset:2560\n"
    "v_mfma_f32_16x16x32_bf16 v[196:199], v[76:79], v[120:123], v[196:199]\n"
    "ds_read_b64_tr_b16 v[114:115], v2 offset:41216\n"
    "v_cvt_pk_bf16_f32 v176, v176, v177\n"
    "v_mfma_f32_16x16x32_bf16 v[200:203], v[72:75], v[124:127], v[200:203]\n"
    "ds_read_b64_tr_b16 v[116:117], v5 offset:40960\n"
    "s_waitcnt lgkmcnt(2)\n"
    "buffer_load_dword off, s[88:91], 0 offset:124 lds\n"
    "v_mfma_f32_16x16x32_bf16 v[204:207], v[76:79], v[124:127], v[204:207]\n"
    "ds_read_b64_tr_b16 v[118:119], v5 offset:41216\n"
    "v_mfma_f32_16x16x32_bf16 v[192:195], v[80:83], v[208:211], v[192:195]\n"
    "ds_read_b64_tr_b16 v[120:121], v2 offset:45056\n"
    "v_cvt_pk_bf16_f32 v177, v178, v179\n"
    "v_mfma_f32_16x16x32_bf16 v[196:199], v[84:87], v[208:211], v[196:199]\n"
    "ds_read_b64_tr_b16 v[122:123], v2 offset:45312\n"
    "ds_read_b32 v9, v7\n"
    "v_mfma_f32_16x16x32_bf16 v[200:203], v[80:83], v[212:215], v[200:203]\n"
    "ds_read_b64_tr_b16 v[124:125], v5 offset:45056\n"
    "v_cvt_pk_bf16_f32 v178, v180, v181\n"
    "v_mfma_f32_16x16x32_bf16 v[204:207], v[84:87], v[212:215], v[204:207]\n"
    "ds_read_b64_tr_b16 v[126:127], v5 offset:45312\n"
    "v_mfma_f32_16x16x32_bf16 v[192:195], v[88:91], v[216:219], v[192:195]\n"
    "ds_read_b64_tr_b16 v[208:209], v3 offset:16896\n"
    "v_cvt_pk_bf16_f32 v179, v182, v183\n"
    "v_mfma_f32_16x16x32_bf16 v[196:199], v[92:95], v[216:219], v[196:199]\n"
    "ds_read_b64_tr_b16 v[210:211], v3 offset:17152\n"
    "v_cvt_pk_bf16_f32 v180, v184, v185\n"
    "v_mfma_f32_16x16x32_bf16 v[200:203], v[88:91], v[220:223], v[200:203]\n"
    "ds_read_b64_tr_b16 v[212:213], v6 offset:16896\n"
    "v_cvt_pk_bf16_f32 v181, v186, v187\n"
    "v_mfma_f32_16x16x32_bf16 v[204:207], v[92:95], v[220:223], v[204:207]\n"
    "ds_read_b64_tr_b16 v[214:215], v6 offset:17152\n"
    "s_waitcnt lgkmcnt(9)\n"
    "v_mfma_f32_16x16x32_bf16 v[192:195], v[96:99], v[112:115], v[192:195]\n"
    "ds_read_b64_tr_b16 v[216:217], v3 offset:20992\n"
    "v_cvt_pk_bf16_f32 v182, v188, v189\n"
    "v_mfma_f32_16x16x32_bf16 v[196:199], v[100:103], v[112:115], v[196:199]\n"
    "ds_read_b64_tr_b16 v[218:219], v3 offset:21248\n"
    "s_waitcnt lgkmcnt(8)\n"
    "v_cmp_gt_u32 vcc, 0xb0000012, v9\n"
    "v_mfma_f32_16x16x32_bf16 v[200:203], v[96:99], v[116:119], v[200:203]\n"
    "ds_read_b64_tr_b16 v[220:221], v6 offset:20992\n"
    "v_add_u32 v10, 114688, v8\n"
    "v_mfma_f32_16x16x32_bf16 v[204:207], v[100:103], v[116:119], v[204:207]\n"
    "ds_read_b64_tr_b16 v[222:223], v6 offset:21248\n"
    "s_waitcnt lgkmcnt(8)\n"
    "v_mfma_f32_16x16x32_bf16 v[192:195], v[104:107], v[120:123], v[192:195]\n"
    "ds_read_b64_tr_b16 v[112:113], v2 offset:33280\n"
    "s_cbranch_vccnz CS18_%=\n"
    "CB18_%=:\n"
    "v_mfma_f32_16x16x32_bf16 v[196:199], v[108:111], v[120:123], v[196:199]\n"
    "ds_read_b64_tr_b16 v[114:115], v2 offset:33536\n"
    "ds_read_b128 v[48:51], v10 offset:0\n"
    "v_mfma_f32_16x16x32_bf16 v[200:203], v[104:107], v[124:127], v[200:203]\n"
    "ds_read_b64_tr_b16 v[116:117], v5 offset:33280\n"
    "v_cvt_pk_bf16_f32 v183, v190, v191\n"
    "v_mfma_f32_16x16x32_bf16 v[204:207], v[108:111], v[124:127], v[204:207]\n"
    "ds_read_b64_tr_b16 v[118:119], v5 offset:33536\n"
    "s_waitcnt lgkmcnt(9)\n"
    "v_mfma_f32_16x16x32_bf16 v[240:243], v[64:67], v[208:211], 0\n"
    "ds_read_b64_tr_b16 v[120:121], v2 offset:37376\n"
    "ds_read_b64_tr_b16 v[122:123], v2 offset:37632\n"
    "v_mfma_f32_16x16x32_bf16 v[244:247], v[68:71], v[208:211], 0\n"
    "ds_read_b32 v9, v7\n"
    "ds_read_b128 v[52:55], v10 offset:2048\n"
    "v_mfma_f32_16x16x32_bf16 v[248:251], v[64:67], v[212:215], 0\n"
    "ds_read_b64_tr_b16 v[124:125], v5 offset:37376\n"
    "ds_read_b128 v[56:59], v10 offset:512\n"
    "v_mfma_f32_16x16x32_bf16 v[252:255], v[68:71], v[212:215], 0\n"
    "s_waitcnt lgkmcnt(14)\n"
    "ds_read_b64_tr_b16 v[126:127], v5 offset:37632\n"
    "s_waitcnt lgkmcnt(12)\n"
    "v_mfma_f32_16x16x32_bf16 v[240:243], v[72:75], v[216:219], v[240:243]\n"
    "ds_read_b64_tr_b16 v[208:209], v2 offset:41472\n"
    "ds_read_b128 v[60:63], v10 offset:2560\n"
    "v_mfma_f32_16x16x32_bf16 v[244:247], v[76:79], v[216:219], v[244:247]\n"
    "ds_read_b64_tr_b16 v[210:211], v2 offset:41728\n"
    "v_cvt_pk_bf16_f32 v192, v192, v193\n"
    "v_mfma_f32_16x16x32_bf16 v[248:251], v[72:75], v[220:223], v[248:251]\n"
    "s_waitcnt lgkmcnt(14)\n"
    "ds_read_b64_tr_b16 v[212:213], v5 offset:41472\n"
    "s_waitcnt lgkmcnt(2)\n"
    "buffer_load_dword off, s[88:91], 0 offset:128 lds\n"
    "v_mfma_f32_16x16x32_bf16 v[252:255], v[76:79], v[220:223], v[252:255]\n"
    "ds_read_b64_tr_b16 v[214:215], v5 offset:41728\n"
    "v_mfma_f32_16x16x32_bf16 v[240:243], v[80:83], v[112:115], v[240:243]\n"
    "ds_read_b64_tr_b16 v[216:217], v2 offset:45568\n"
    "v_cvt_pk_bf16_f32 v193, v194, v195\n"
    "v_mfma_f32_16x16x32_bf16 v[244:247], v[84:87], v[112:115], v[244:247]\n"
    "ds_read_b64_tr_b16 v[218:219], v2 offset:45824\n"
    "buffer_load_dword off, s[88:91], 0 offset:132 lds\n"
    "v_mfma_f32_16x16x32_bf16 v[248:251], v[80:83], v[116:119], v[248:251]\n"
    "ds_read_b64_tr_b16 v[220:221], v5 offset:45568\n"
    "v_cvt_pk_bf16_f32 v194, v196, v197\n"
    "v_mfma_f32_16x16x32_bf16 v[252:255], v[84:87], v[116:119], v[252:255]\n"
    "ds_read_b64_tr_b16 v[222:223], v5 offset:45824\n"
    "v_cmp_gt_u32 vcc, 0xb0000013, v9\n"
    "v_mfma_f32_16x16x32_bf16 v[240:243], v[88:91], v[120:123], v[240:243]\n"
    "s_cbranch_vccnz CS19_%=\n"
    "CB19_%=:\n"
    "ds_read_b64_tr_b16 v[112:113], v1 offset:0\n"
    "v_mfma_f32_16x16x32_bf16 v[244:247], v[92:95], v[120:123], v[244:247]\n"
    "ds_read_b64_tr_b16 v[114:115], v1 offset:256\n"
    "v_cvt_pk_bf16_f32 v195, v198, v199\n"
    "v_mfma_f32_16x16x32_bf16 v[248:251], v[88:91], v[124:127], v[248:251]\n"
    "ds_read_b64_tr_b16 v[116:117], v4 offset:0\n"
    "v_and_b32 v13, 3, v0\n"
    "v_mfma_f32_16x16x32_bf16 v[252:255], v[92:95], v[124:127], v[252:255]\n"
    "ds_read_b64_tr_b16 v[118:119], v4 offset:256\n"
    "s_waitcnt lgkmcnt(8)\n"
    "v_mfma_f32_16x16x32_bf16 v[240:243], v[96:99], v[208:211], v[240:243]\n"
    "ds_read_b64_tr_b16 v[120:121], v1 offset:4096\n"
    "v_lshlrev_b32 v13, 7, v13\n"
    "v_mfma_f32_16x16x32_bf16 v[244:247], v[100:103], v[208:211], v[244:247]\n"
    "ds_read_b64_tr_b16 v[122:123], v1 offset:4352\n"
    "buffer_load_dwordx2 v[14:15], v13, s[48:51], s16 offen sc1\n"
    "v_mfma_f32_16x16x32_bf16 v[248:251], v[96:99], v[212:215], v[248:251]\n"
    "ds_read_b64_tr_b16 v[124:125], v4 offset:4096\n"
    "v_cvt_pk_bf16_f32 v196, v200, v201\n"
    "v_mfma_f32_16x16x32_bf16 v[252:255], v[100:103], v[212:215], v[252:255]\n"
    "ds_read_b64_tr_b16 v[126:127], v4 offset:4352\n"
    "s_waitcnt lgkmcnt(8)\n"
    "v_mfma_f32_16x16x32_bf16 v[240:243], v[104:107], v[216:219], v[240:243]\n"
    "ds_read_b64_tr_b16 v[208:209], v1 offset:8192\n"
    "v_cvt_pk_bf16_f32 v197, v202, v203\n"
    "v_mfma_f32_16x16x32_bf16 v[244:247], v[108:111], v[216:219], v[244:247]\n"
    "ds_read_b64_tr_b16 v[210:211], v1 offset:8448\n"
    "v_mfma_f32_16x16x32_bf16 v[248:251], v[104:107], v[220:223], v[248:251]\n"
    "ds_read_b64_tr_b16 v[212:213], v4 offset:8192\n"
    "v_cvt_pk_bf16_f32 v198, v204, v205\n"
    "v_mfma_f32_16x16x32_bf16 v[252:255], v[108:111], v[220:223], v[252:255]\n"
    "ds_read_b64_tr_b16 v[214:215], v4 offset:8448\n"
    "v_cvt_pk_bf16_f32 v199, v206, v207\n"
    "s_waitcnt lgkmcnt(8)\n"
    "v_mfma_f32_16x16x32_bf16 v[224:227], v[16:19], v[112:115], 0\n"
    "ds_read_b64_tr_b16 v[216:217], v1 offset:12288\n"
    "buffer_load_dword off, s[88:91], 0 offset:140 lds\n"
    "v_mfma_f32_16x16x32_bf16 v[228:231], v[20:23], v[112:115], 0\n"
    "ds_read_b64_tr_b16 v[218:219], v1 offset:12544\n"
    "v_mfma_f32_16x16x32_bf16 v[232:235], v[16:19], v[116:119], 0\n"
    "ds_read_b64_tr_b16 v[220:221], v4 offset:12288\n"
    "v_cvt_pk_bf16_f32 v200, v240, v241\n"
    "v_mfma_f32_16x16x32_bf16 v[236:239], v[20:23], v[116:119], 0\n"
    "ds_read_b64_tr_b16 v[222:223], v4 offset:12544\n"
    "s_waitcnt lgkmcnt(8)\n"
    "v_mfma_f32_16x16x32_bf16 v[224:227], v[24:27], v[120:123], v[224:227]\n"
    "ds_read_b64_tr_b16 v[112:113], v1 offset:32768\n"
    "v_cvt_pk_bf16_f32 v201, v242, v243\n"
    "v_mfma_f32_16x16x32_bf16 v[228:231], v[28:31], v[120:123], v[228:231]\n"
    "ds_read_b64_tr_b16 v[114:115], v1 offset:33024\n"
    "v_mfma_f32_16x16x32_bf16 v[232:235], v[24:27], v[124:127], v[232:235]\n"
    "ds_read_b64_tr_b16 v[116:117], v4 offset:32768\n"
    "v_cvt_pk_bf16_f32 v202, v244, v245\n"
    "v_mfma_f32_16x16x32_bf16 v[236:239], v[28:31], v[124:127], v[236:239]\n"
    "ds_read_b64_tr_b16 v[118:119], v4 offset:33024\n"
    "s_waitcnt lgkmcnt(8)\n"
    "v_mfma_f32_16x16x32_bf16 v[224:227], v[32:35], v[208:211], v[224:227]\n"
    "ds_read_b64_tr_b16 v[120:121], v1 offset:36864\n"
    "v_cvt_pk_bf16_f32 v203, v246, v247\n"
    "v_mfma_f32_16x16x32_bf16 v[228:231], v[36:39], v[208:211], v[228:231]\n"
    "ds_read_b64_tr_b16 v[122:123], v1 offset:37120\n"
    "v_mfma_f32_16x16x32_bf16 v[232:235], v[32:35], v[212:215], v[232:235]\n"
    "ds_read_b64_tr_b16 v[124:125], v4 offset:36864\n"
    "v_cvt_pk_bf16_f32 v204, v248, v249\n"
    "v_mfma_f32_16x16x32_bf16 v[236:239], v[36:39], v[212:215], v[236:239]\n"
    "ds_read_b64_tr_b16 v[126:127], v4 offset:37120\n"
    "s_waitcnt lgkmcnt(8)\n"
    "v_mfma_f32_16x16x32_bf16 v[224:227], v[40:43], v[216:219], v[224:227]\n"
    "ds_read_b64_tr_b16 v[208:209], v1 offset:512\n"
    "v_cvt_pk_bf16_f32 v205, v250, v251\n"
    "v_mfma_f32_16x16x32_bf16 v[228:231], v[44:47], v[216:219], v[228:231]\n"
    "ds_read_b64_tr_b16 v[210:211], v1 offset:768\n"
    "v_mfma_f32_16x16x32_bf16 v[232:235], v[40:43], v[220:223], v[232:235]\n"
    "ds_read_b64_tr_b16 v[212:213], v4 offset:512\n"
    "v_cvt_pk_bf16_f32 v206, v252, v253\n"
    "v_mfma_f32_16x16x32_bf16 v[236:239], v[44:47], v[220:223], v[236:239]\n"
    "ds_read_b64_tr_b16 v[214:215], v4 offset:768\n"
    "s_waitcnt lgkmcnt(8)\n"
    "v_mfma_f32_16x16x32_bf16 v[224:227], v[48:51], v[112:115], v[224:227]\n"
    "ds_read_b64_tr_b16 v[216:217], v1 offset:4608\n"
    "v_cvt_pk_bf16_f32 v207, v254, v255\n"
    "v_mfma_f32_16x16x32_bf16 v[228:231], v[52:55], v[112:115], v[228:231]\n"
    "ds_read_b64_tr_b16 v[218:219], v1 offset:4864\n"
    "v_mfma_f32_16x16x32_bf16 v[232:235], v[48:51], v[116:119], v[232:235]\n"
    "ds_read_b64_tr_b16 v[220:221], v4 offset:4608\n"
    "v_mfma_f32_16x16x32_bf16 v[236:239], v[52:55], v[116:119], v[236:239]\n"
    "ds_read_b64_tr_b16 v[222:223], v4 offset:4864\n"
    "s_waitcnt lgkmcnt(8)\n"
    "v_mfma_f32_16x16x32_bf16 v[224:227], v[56:59], v[120:123], v[224:227]\n"
    "ds_read_b64_tr_b16 v[112:113], v1 offset:8704\n"
    "v_mfma_f32_16x16x32_bf16 v[228:231], v[60:63], v[120:123], v[228:231]\n"
    "ds_read_b64_tr_b16 v[114:115], v1 offset:8960\n"
    "v_mfma_f32_16x16x32_bf16 v[232:235], v[56:59], v[124:127], v[232:235]\n"
    "ds_read_b64_tr_b16 v[116:117], v4 offset:8704\n"
    "v_mfma_f32_16x16x32_bf16 v[236:239], v[60:63], v[124:127], v[236:239]\n"
    "ds_read_b64_tr_b16 v[118:119], v4 offset:8960\n"
    "s_waitcnt lgkmcnt(8)\n"
    "v_mfma_f32_16x16x32_bf16 v[240:243], v[16:19], v[208:211], 0\n"
    "ds_read_b64_tr_b16 v[120:121], v1 offset:12800\n"
    "ds_read_b64_tr_b16 v[122:123], v1 offset:13056\n"
    "v_mfma_f32_16x16x32_bf16 v[244:247], v[20:23], v[208:211], 0\n"
    "ds_read_b32 v9, v7\n"
    "v_mfma_f32_16x16x32_bf16 v[248:251], v[16:19], v[212:215], 0\n"
    "ds_read_b64_tr_b16 v[124:125], v4 offset:12800\n"
    "v_cvt_pk_bf16_f32 v136, v224, v225\n"
    "v_mfma_f32_16x16x32_bf16 v[252:255], v[20:23], v[212:215], 0\n"
    "ds_read_b64_tr_b16 v[126:127], v4 offset:13056\n"
    "s_waitcnt lgkmcnt(9)\n"
    "v_mfma_f32_16x16x32_bf16 v[240:243], v[24:27], v[216:219], v[240:243]\n"
    "ds_read_b64_tr_b16 v[208:209], v1 offset:33280\n"
    "v_cvt_pk_bf16_f32 v137, v226, v227\n"
    "v_mfma_f32_16x16x32_bf16 v[244:247], v[28:31], v[216:219], v[244:247]\n"
    "ds_read_b64_tr_b16 v[210:211], v1 offset:33536\n"
    "v_mfma_f32_16x16x32_bf16 v[248:251], v[24:27], v[220:223], v[248:251]\n"
    "ds_read_b64_tr_b16 v[212:213], v4 offset:33280\n"
    "v_mfma_f32_16x16x32_bf16 v[252:255], v[28:31], v[220:223], v[252:255]\n"
    "ds_read_b64_tr_b16 v[214:215], v4 offset:33536\n"
    "s_waitcnt lgkmcnt(9)\n"
    "v_mfma_f32_16x16x32_bf16 v[240:243], v[32:35], v[112:115], v[240:243]\n"
    "ds_read_b64_tr_b16 v[216:217], v1 offset:37376\n"
    "v_cvt_pk_bf16_f32 v138, v228, v229\n"
    "v_mfma_f32_16x16x32_bf16 v[244:247], v[36:39], v[112:115], v[244:247]\n"
    "ds_read_b64_tr_b16 v[218:219], v1 offset:37632\n"
    "v_cvt_pk_bf16_f32 v139, v230, v231\n"
    "v_mfma_f32_16x16x32_bf16 v[248:251], v[32:35], v[116:119], v[248:251]\n"
    "ds_read_b64_tr_b16 v[220:221], v4 offset:37376\n"
    "v_cvt_pk_bf16_f32 v140, v232, v233\n"
    "v_mfma_f32_16x16x32_bf16 v[252:255], v[36:39], v[116:119], v[252:255]\n"
    "ds_read_b64_tr_b16 v[222:223], v4 offset:37632\n"
    "s_waitcnt lgkmcnt(10)\n"
    "v_cmp_gt_u32 vcc, 0xb0000014, v9\n"
    "s_waitcnt lgkmcnt(8)\n"
    "v_mfma_f32_16x16x32_bf16 v[240:243], v[40:43], v[120:123], v[240:243]\n"
    "s_cbranch_vccnz CS20_%=\n"
    "CB20_%=:\n"
    "ds_read_b64_tr_b16 v[112:113], v1 offset:40960\n"
    "v_mfma_f32_16x16x32_bf16 v[244:247], v[44:47], v[120:123], v[244:247]\n"
    "ds_read_b64_tr_b16 v[114:115], v1 offset:41216\n"
    "v_cvt_pk_bf16_f32 v141, v234, v235\n"
    "v_mfma_f32_16x16x32_bf16 v[248:251], v[40:43], v[124:127], v[248:251]\n"
    "ds_read_b64_tr_b16 v[116:117], v4 offset:40960\n"
    "v_mfma_f32_16x16x32_bf16 v[252:255], v[44:47], v[124:127], v[252:255]\n"
    "ds_read_b64_tr_b16 v[118:119], v4 offset:41216\n"
    "s_waitcnt lgkmcnt(8)\n"
    "v_mfma_f32_16x16x32_bf16 v[240:243], v[48:51], v[208:211], v[240:243]\n"
    "ds_read_b64_tr_b16 v[120:121], v1 offset:45056\n"
    "ds_read_b32 v9, v7\n"
    "v_mfma_f32_16x16x32_bf16 v[244:247], v[52:55], v[208:211], v[244:247]\n"
    "ds_read_b64_tr_b16 v[122:123], v1 offset:45312\n"
    "v_cvt_pk_bf16_f32 v142, v236, v237\n"
    "v_mfma_f32_16x16x32_bf16 v[248:251], v[48:51], v[212:215], v[248:251]\n"
    "ds_read_b64_tr_b16 v[124:125], v4 offset:45056\n"
    "v_cvt_pk_bf16_f32 v143, v238, v239\n"
    "v_mfma_f32_16x16x32_bf16 v[252:255], v[52:55], v[212:215], v[252:255]\n"
    "ds_read_b64_tr_b16 v[126:127], v4 offset:45312\n"
    "s_waitcnt lgkmcnt(9)\n"
    "v_mfma_f32_16x16x32_bf16 v[240:243], v[56:59], v[216:219], v[240:243]\n"
    "ds_read_b64_tr_b16 v[208:209], v2 offset:0\n"
    "s_waitcnt vmcnt(1)\n"
    "v_cmp_ne_u64 vcc, s[6:7], v[14:15]\n"
    "s_cbranch_vccnz PS_%=\n"
    "PB_%=:\n"
    "v_mfma_f32_16x16x32_bf16 v[244:247], v[60:63], v[216:219], v[244:247]\n"
    "ds_read_b64_tr_b16 v[210:211], v2 offset:256\n"
    "v_mfma_f32_16x16x32_bf16 v[248:251], v[56:59], v[220:223], v[248:251]\n"
    "ds_read_b64_tr_b16 v[212:213], v5 offset:0\n"
    "buffer_load_dwordx4 v[80:83], v11, s[48:51], s17 offen offset:0 sc1\n"
    "v_mfma_f32_16x16x32_bf16 v[252:255], v[60:63], v[220:223], v[252:255]\n"
    "ds_read_b64_tr_b16 v[214:215], v5 offset:256\n"
    "s_waitcnt lgkmcnt(9)\n"
    "v_mfma_f32_16x16x32_bf16 v[224:227], v[16:19], v[112:115], 0\n"
    "ds_read_b64_tr_b16 v[216:217], v2 offset:4096\n"
    "buffer_load_dwordx4 v[84:87], v11, s[48:51], s17 offen offset:1024 sc1\n"
    "v_mfma_f32_16x16x32_bf16 v[228:231], v[20:23], v[112:115], 0\n"
    "ds_read_b64_tr_b16 v[218:219], v2 offset:4352\n"
    "buffer_load_dwordx4 v[88:91], v11, s[48:51], s18 offen offset:0 sc1\n"
    "v_mfma_f32_16x16x32_bf16 v[232:235], v[16:19], v[116:119], 0\n"
    "ds_read_b64_tr_b16 v[220:221], v5 offset:4096\n"
    "buffer_load_dwordx4 v[92:95], v11, s[48:51], s18 offen offset:1024 sc1\n"
    "v_mfma_f32_16x16x32_bf16 v[236:239], v[20:23], v[116:119], 0\n"
    "ds_read_b64_tr_b16 v[222:223], v5 offset:4352\n"
    "s_waitcnt lgkmcnt(8)\n"
    "v_mfma_f32_16x16x32_bf16 v[224:227], v[24:27], v[120:123], v[224:227]\n"
    "ds_read_b64_tr_b16 v[112:113], v1 offset:16384\n"
    "buffer_load_dword off, s[88:91], 0 offset:152 lds\n"
    "v_mfma_f32_16x16x32_bf16 v[228:231], v[28:31], v[120:123], v[228:231]\n"
    "ds_read_b64_tr_b16 v[114:115], v1 offset:16640\n"
    "v_cmp_gt_u32 vcc, 0xb0000015, v9\n"
    "v_mfma_f32_16x16x32_bf16 v[232:235], v[24:27], v[124:127], v[232:235]\n"
    "ds_read_b64_tr_b16 v[116:117], v4 offset:16384\n"
    "v_add_u32 v10, 81920, v8\n"
    "v_mfma_f32_16x16x32_bf16 v[236:239], v[28:31], v[124:127], v[236:239]\n"
    "ds_read_b64_tr_b16 v[118:119], v4 offset:16640\n"
    "s_waitcnt lgkmcnt(8)\n"
    "v_mfma_f32_16x16x32_bf16 v[224:227], v[32:35], v[208:211], v[224:227]\n"
    "ds_read_b64_tr_b16 v[120:121], v1 offset:20480\n"
    "s_cbranch_vccnz CS21_%=\n"
    "CB21_%=:\n"
    "v_mfma_f32_16x16x32_bf16 v[228:231], v[36:39], v[208:211], v[228:231]\n"
    "ds_read_b64_tr_b16 v[122:123], v1 offset:20736\n"
    "ds_read_b128 v[64:67], v10 offset:0\n"
    "v_mfma_f32_16x16x32_bf16 v[232:235], v[32:35], v[212:215], v[232:235]\n"
    "ds_read_b64_tr_b16 v[124:125], v4 offset:20480\n"
    "ds_read_b128 v[68:71], v10 offset:2048\n"
    "v_mfma_f32_16x16x32_bf16 v[236:239], v[36:39], v[212:215], v[236:239]\n"
    "ds_read_b64_tr_b16 v[126:127], v4 offset:20736\n"
    "s_waitcnt lgkmcnt(10)\n"
    "v_mfma_f32_16x16x32_bf16 v[224:227], v[40:43], v[216:219], v[224:227]\n"
    "ds_read_b64_tr_b16 v[208:209], v1 offset:41472\n"
    "v_cvt_pk_bf16_f32 v152, v240, v241\n"
    "v_mfma_f32_16x16x32_bf16 v[228:231], v[44:47], v[216:219], v[228:231]\n"
    "ds_read_b64_tr_b16 v[210:211], v1 offset:41728\n"
    "ds_read_b128 v[72:75], v10 offset:512\n"
    "v_mfma_f32_16x16x32_bf16 v[232:235], v[40:43], v[220:223], v[232:235]\n"
    "ds_read_b64_tr_b16 v[212:213], v4 offset:41472\n"
    "v_cvt_pk_bf16_f32 v153, v242, v243\n"
    "v_mfma_f32_16x16x32_bf16 v[236:239], v[44:47], v[220:223], v[236:239]\n"
    "ds_read_b64_tr_b16 v[214:215], v4 offset:41728\n"
    "s_waitcnt lgkmcnt(11)\n"
    "v_mfma_f32_16x16x32_bf16 v[224:227], v[48:51], v[112:115], v[224:227]\n"
    "ds_read_b64_tr_b16 v[216:217], v1 offset:45568\n"
    "ds_read_b128 v[76:79], v10 offset:2560\n"
    "v_mfma_f32_16x16x32_bf16 v[228:231], v[52:55], v[112:115], v[228:231]\n"
    "ds_read_b64_tr_b16 v[218:219], v1 offset:45824\n"
    "v_cvt_pk_bf16_f32 v154, v244, v245\n"
    "v_mfma_f32_16x16x32_bf16 v[232:235], v[48:51], v[116:119], v[232:235]\n"
    "ds_read_b64_tr_b16 v[220:221], v4 offset:45568\n"
    "v_cvt_pk_bf16_f32 v155, v246, v247\n"
    "v_mfma_f32_16x16x32_bf16 v[236:239], v[52:55], v[116:119], v[236:239]\n"
    "s_waitcnt lgkmcnt(14)\n"
    "ds_read_b64_tr_b16 v[222:223], v4 offset:45824\n"
    "s_waitcnt lgkmcnt(10)\n"
    "v_mfma_f32_16x16x32_bf16 v[224:227], v[56:59], v[120:123], v[224:227]\n"
    "ds_read_b64_tr_b16 v[112:113], v2 offset:512\n"
    "v_cvt_pk_bf16_f32 v156, v248, v249\n"
    "v_mfma_f32_16x16x32_bf16 v[228:231], v[60:63], v[120:123], v[228:231]\n"
    "ds_read_b64_tr_b16 v[114:115], v2 offset:768\n"
    "s_waitcnt lgkmcnt(5)\n"
    "buffer_load_dword off, s[88:91], 0 offset:156 lds\n"
    "v_mfma_f32_16x16x32_bf16 v[232:235], v[56:59], v[124:127], v[232:235]\n"
    "ds_read_b64_tr_b16 v[116:117], v5 offset:512\n"
    "v_cvt_pk_bf16_f32 v157, v250, v251\n"
    "v_mfma_f32_16x16x32_bf16 v[236:239], v[60:63], v[124:127], v[236:239]\n"
    "ds_read_b64_tr_b16 v[118:119], v5 offset:768\n"
    "v_cvt_pk_bf16_f32 v158, v252, v253\n"
    "v_cvt_pk_bf16_f32 v159, v254, v255\n"
    "v_mfma_f32_16x16x32_bf16 v[240:243], v[16:19], v[208:211], 0\n"
    "ds_read_b64_tr_b16 v[120:121], v2 offset:4608\n"
    "v_mfma_f32_16x16x32_bf16 v[244:247], v[20:23], v[208:211], 0\n"
    "ds_read_b64_tr_b16 v[122:123], v2 offset:4864\n"
    "v_mfma_f32_16x16x32_bf16 v[248:251], v[16:19], v[212:215], 0\n"
    "ds_read_b64_tr_b16 v[124:125], v5 offset:4608\n"
    "v_cvt_pk_bf16_f32 v168, v224, v225\n"
    "v_mfma_f32_16x16x32_bf16 v[252:255], v[20:23], v[212:215], 0\n"
    "ds_read_b64_tr_b16 v[126:127], v5 offset:4864\n"
    "s_waitcnt lgkmcnt(8)\n"
    "v_mfma_f32_16x16x32_bf16 v[240:243], v[24:27], v[216:219], v[240:243]\n"
    "ds_read_b64_tr_b16 v[208:209], v1 offset:16896\n"
    "v_cvt_pk_bf16_f32 v169, v226, v227\n"
    "v_mfma_f32_16x16x32_bf16 v[244:247], v[28:31], v[216:219], v[244:247]\n"
    "ds_read_b64_tr_b16 v[210:211], v1 offset:17152\n"
    "v_mfma_f32_16x16x32_bf16 v[248:251], v[24:27], v[220:223], v[248:251]\n"
    "ds_read_b64_tr_b16 v[212:213], v4 offset:16896\n"
    "buffer_load_dword off, s[88:91], 0 offset:160 lds\n"
    "v_mfma_f32_16x16x32_bf16 v[252:255], v[28:31], v[220:223], v[252:255]\n"
    "ds_read_b64_tr_b16 v[214:215], v4 offset:17152\n"
    "s_waitcnt lgkmcnt(8)\n"
    "v_mfma_f32_16x16x32_bf16 v[240:243], v[32:35], v[112:115], v[240:243]\n"
    "ds_read_b64_tr_b16 v[216:217], v1 offset:20992\n"
    "v_cvt_pk_bf16_f32 v170, v228, v229\n"
    "v_mfma_f32_16x16x32_bf16 v[244:247], v[36:39], v[112:115], v[244:247]\n"
    "ds_read_b64_tr_b16 v[218:219], v1 offset:21248\n"
    "v_cvt_pk_bf16_f32 v171, v230, v231\n"
    "v_mfma_f32_16x16x32_bf16 v[248:251], v[32:35], v[116:119], v[248:251]\n"
    "ds_read_b64_tr_b16 v[220:221], v4 offset:20992\n"
    "v_cvt_pk_bf16_f32 v172, v232, v233\n"
    "v_mfma_f32_16x16x32_bf16 v[252:255], v[36:39], v[116:119], v[252:255]\n"
    "ds_read_b64_tr_b16 v[222:223], v4 offset:21248\n"
    "s_waitcnt lgkmcnt(8)\n"
    "v_mfma_f32_16x16x32_bf16 v[240:243], v[40:43], v[120:123], v[240:243]\n"
    "v_cvt_pk_bf16_f32 v173, v234, v235\n"
    "v_mfma_f32_16x16x32_bf16 v[244:247], v[44:47], v[120:123], v[244:247]\n"
    "v_mfma_f32_16x16x32_bf16 v[248:251], v[40:43], v[124:127], v[248:251]\n"
    "buffer_load_dword off, s[88:91], 0 offset:164 lds\n"
    "v_cvt_pk_bf16_f32 v174, v236, v237\n"
    "v_mfma_f32_16x16x32_bf16 v[252:255], v[44:47], v[124:127], v[252:255]\n"
    "s_waitcnt lgkmcnt(4)\n"
    "v_mfma_f32_16x16x32_bf16 v[240:243], v[48:51], v[208:211], v[240:243]\n"
    "v_cvt_pk_bf16_f32 v175, v238, v239\n"
    "v_mfma_f32_16x16x32_bf16 v[244:247], v[52:55], v[208:211], v[244:247]\n"
    "v_mfma_f32_16x16x32_bf16 v[248:251], v[48:51], v[212:215], v[248:251]\n"
    "v_mfma_f32_16x16x32_bf16 v[252:255], v[52:55], v[212:215], v[252:255]\n"
    "s_waitcnt lgkmcnt(0)\n"
    "v_mfma_f32_16x16x32_bf16 v[240:243], v[56:59], v[216:219], v[240:243]\n"
    "buffer_load_dwordx4 v[96:99], v11, s[48:51], s19 offen offset:0 sc1\n"
    "buffer_load_dwordx4 v[100:103], v11, s[48:51], s19 offen offset:1024 sc1\n"
    "v_mfma_f32_16x16x32_bf16 v[244:247], v[60:63], v[216:219], v[244:247]\n"
    "v_mfma_f32_16x16x32_bf16 v[248:251], v[56:59], v[220:223], v[248:251]\n"
    "buffer_load_dword off, s[88:91], 0 offset:168 lds\n"
    "v_mfma_f32_16x16x32_bf16 v[252:255], v[60:63], v[220:223], v[252:255]\n"
    "CA_%=:\n"
    "s_waitcnt vmcnt(10)\n"
    "v_mfma_f32_16x16x32_bf16 v[224:227], v[192:195], v[80:83], 0\n"
    "ds_read_b32 v9, v7\n"
    "s_waitcnt vmcnt(9)\n"
    "v_mfma_f32_16x16x32_bf16 v[232:235], v[192:195], v[84:87], 0\n"
    "v_mfma_f32_16x16x32_bf16 v[228:231], v[196:199], v[80:83], 0\n"
    "v_mfma_f32_16x16x32_bf16 v[236:239], v[196:199], v[84:87], 0\n"
    "v_mfma_f32_16x16x32_bf16 v[208:211], v[200:203], v[80:83], 0\n"
    "v_cvt_pk_bf16_f32 v184, v240, v241\n"
    "s_waitcnt lgkmcnt(0)\n"
    "v_cmp_gt_u32 vcc, 0xb0000016, v9\n"
    "v_mfma_f32_16x16x32_bf16 v[216:219], v[200:203], v[84:87], 0\n"
    "v_add_u32 v10, 49152, v8\n"
    "s_cbranch_vccnz CS22_%=\n"
    "CB22_%=:\n"
    "v_mfma_f32_16x16x32_bf16 v[212:215], v[204:207], v[80:83], 0\n"
    "ds_read_b128 v[16:19], v10 offset:0\n"
    "v_cvt_pk_bf16_f32 v185, v242, v243\n"
    "v_mfma_f32_16x16x32_bf16 v[220:223], v[204:207], v[84:87], 0\n"
    "ds_read_b128 v[20:23], v10 offset:2048\n"
    "v_mfma_f32_16x16x32_bf16 v[192:195], v[128:131], v[80:83], 0\n"
    "v_cvt_pk_bf16_f32 v186, v244, v245\n"
    "v_mfma_f32_16x16x32_bf16 v[200:203], v[128:131], v[84:87], 0\n"
    "ds_read_b128 v[24:27], v10 offset:512\n"
    "v_mfma_f32_16x16x32_bf16 v[196:199], v[132:135], v[80:83], 0\n"
    "v_cvt_pk_bf16_f32 v187, v246, v247\n"
    "v_mfma_f32_16x16x32_bf16 v[204:207], v[132:135], v[84:87], 0\n"
    "ds_read_b128 v[28:31], v10 offset:2560\n"
    "s_waitcnt vmcnt(8)\n"
    "v_mfma_f32_16x16x32_bf16 v[192:195], v[136:139], v[88:91], v[192:195]\n"
    "v_cvt_pk_bf16_f32 v188, v248, v249\n"
    "s_waitcnt vmcnt(7)\n"
    "v_mfma_f32_16x16x32_bf16 v[200:203], v[136:139], v[92:95], v[200:203]\n"
    "v_mfma_f32_16x16x32_bf16 v[196:199], v[140:143], v[88:91], v[196:199]\n"
    "v_cvt_pk_bf16_f32 v189, v250, v251\n"
    "v_mfma_f32_16x16x32_bf16 v[204:207], v[140:143], v[92:95], v[204:207]\n"
    "v_mfma_f32_16x16x32_bf16 v[128:131], v[144:147], v[80:83], 0\n"
    "v_cvt_pk_bf16_f32 v190, v252, v253\n"
    "s_waitcnt lgkmcnt(0)\n"
    "buffer_load_dword off, s[88:91], 0 offset:172 lds\n"
    "v_mfma_f32_16x16x32_bf16 v[136:139], v[144:147], v[84:87], 0\n"
    "v_cmp_gt_u32 vcc, 0xb0000017, v9\n"
    "v_add_u32 v10, 131072, v8\n"
    "v_mfma_f32_16x16x32_bf16 v[132:135], v[148:151], v[80:83], 0\n"
    "s_cbranch_vccnz CS23_%=\n"
    "CB23_%=:\n"
    "ds_read_b128 v[32:35], v10 offset:0\n"
    "v_mfma_f32_16x16x32_bf16 v[140:143], v[148:151], v[84:87], 0\n"
    "v_cvt_pk_bf16_f32 v191, v254, v255\n"
    "v_mfma_f32_16x16x32_bf16 v[128:131], v[152:155], v[88:91], v[128:131]\n"
    "ds_read_b128 v[36:39], v10 offset:2048\n"
    "ds_read_b128 v[40:43], v10 offset:512\n"
    "v_mfma_f32_16x16x32_bf16 v[136:139], v[152:155], v[92:95], v[136:139]\n"
    "v_mfma_f32_16x16x32_bf16 v[132:135], v[156:159], v[88:91], v[132:135]\n"
    "ds_read_b128 v[44:47], v10 offset:2560\n"
    "v_mfma_f32_16x16x32_bf16 v[140:143], v[156:159], v[92:95], v[140:143]\n"
    "v_mfma_f32_16x16x32_bf16 v[144:147], v[160:163], v[80:83], 0\n"
    "v_mfma_f32_16x16x32_bf16 v[152:155], v[160:163], v[84:87], 0\n"
    "v_mfma_f32_16x16x32_bf16 v[148:151], v[164:167], v[80:83], 0\n"
    "ds_read_b32 v9, v7\n"
    "v_mfma_f32_16x16x32_bf16 v[156:159], v[164:167], v[84:87], 0\n"
    "v_mfma_f32_16x16x32_bf16 v[144:147], v[168:171], v[88:91], v[144:147]\n"
    "s_waitcnt lgkmcnt(1)\n"
    "buffer_load_dword off, s[88:91], 0 offset:176 lds\n"
    "v_mfma_f32_16x16x32_bf16 v[152:155], v[168:171], v[92:95], v[152:155]\n"
    "v_mfma_f32_16x16x32_bf16 v[148:151], v[172:175], v[88:91], v[148:151]\n"
    "v_mfma_f32_16x16x32_bf16 v[156:159], v[172:175], v[92:95], v[156:159]\n"
    "v_mfma_f32_16x16x32_bf16 v[160:163], v[176:179], v[80:83], 0\n"
    "s_waitcnt lgkmcnt(0)\n"
    "v_cmp_gt_u32 vcc, 0xb0000018, v9\n"
    "s_cbranch_vccnz CS24_%=\n"
    "CB24_%=:\n"
    "v_mfma_f32_16x16x32_bf16 v[168:171], v[176:179], v[84:87], 0\n"
    "ds_read_b64_tr_b16 v[112:113], v1 offset:24576\n"
    "ds_read_b64_tr_b16 v[114:115], v1 offset:24832\n"
    "v_mfma_f32_16x16x32_bf16 v[164:167], v[180:183], v[80:83], 0\n"
    "ds_read_b64_tr_b16 v[116:117], v4 offset:24576\n"
    "ds_read_b64_tr_b16 v[118:119], v4 offset:24832\n"
    "v_mfma_f32_16x16x32_bf16 v[172:175], v[180:183], v[84:87], 0\n"
    "v_mfma_f32_16x16x32_bf16 v[160:163], v[184:187], v[88:91], v[160:163]\n"
    "ds_read_b64_tr_b16 v[120:121], v1 offset:28672\n"
    "ds_read_b64_tr_b16 v[122:123], v1 offset:28928\n"
    "v_mfma_f32_16x16x32_bf16 v[168:171], v[184:187], v[92:95], v[168:171]\n"
    "v_mfma_f32_16x16x32_bf16 v[164:167], v[188:191], v[88:91], v[164:167]\n"
    "ds_read_b64_tr_b16 v[124:125], v4 offset:28672\n"
    "ds_read_b64_tr_b16 v[126:127], v4 offset:28928\n"
    "v_mfma_f32_16x16x32_bf16 v[172:175], v[188:191], v[92:95], v[172:175]\n"
    "s_waitcnt lgkmcnt(6)\n"
    "v_mfma_f32_16x16x32_bf16 v[240:243], v[64:67], v[112:115], 0\n"
    "ds_read_b64_tr_b16 v[80:81], v2 offset:8192\n"
    "ds_read_b64_tr_b16 v[82:83], v2 offset:8448\n"
    "v_mfma_f32_16x16x32_bf16 v[244:247], v[68:71], v[112:115], 0\n"
    "ds_read_b64_tr_b16 v[84:85], v5 offset:8192\n"
    "ds_read_b64_tr_b16 v[86:87], v5 offset:8448\n"
    "s_waitcnt lgkmcnt(8)\n"
    "v_mfma_f32_16x16x32_bf16 v[248:251], v[64:67], v[116:119], 0\n"
    "ds_read_b64_tr_b16 v[88:89], v2 offset:12288\n"
    "ds_read_b64_tr_b16 v[90:91], v2 offset:12544\n"
    "v_mfma_f32_16x16x32_bf16 v[252:255], v[68:71], v[116:119], 0\n"
    "ds_read_b64_tr_b16 v[92:93], v5 offset:12288\n"
    "ds_read_b64_tr_b16 v[94:95], v5 offset:12544\n"
    "s_waitcnt lgkmcnt(10)\n"
    "v_mfma_f32_16x16x32_bf16 v[240:243], v[72:75], v[120:123], v[240:243]\n"
    "ds_read_b64_tr_b16 v[112:113], v2 offset:49152\n"
    "v_mfma_f32_16x16x32_bf16 v[244:247], v[76:79], v[120:123], v[244:247]\n"
    "ds_read_b64_tr_b16 v[114:115], v2 offset:49408\n"
    "s_waitcnt lgkmcnt(10)\n"
    "v_mfma_f32_16x16x32_bf16 v[248:251], v[72:75], v[124:127], v[248:251]\n"
    "ds_read_b64_tr_b16 v[116:117], v5 offset:49152\n"
    "v_mfma_f32_16x16x32_bf16 v[252:255], v[76:79], v[124:127], v[252:255]\n"
    "ds_read_b64_tr_b16 v[118:119], v5 offset:49408\n"
    "s_waitcnt lgkmcnt(10)\n"
    "v_mfma_f32_16x16x32_bf16 v[240:243], v[16:19], v[80:83], v[240:243]\n"
    "ds_read_b64_tr_b16 v[120:121], v2 offset:53248\n"
    "v_mfma_f32_16x16x32_bf16 v[244:247], v[20:23], v[80:83], v[244:247]\n"
    "ds_read_b64_tr_b16 v[122:123], v2 offset:53504\n"
    "s_waitcnt lgkmcnt(10)\n"
    "v_mfma_f32_16x16x32_bf16 v[248:251], v[16:19], v[84:87], v[248:251]\n"
    "ds_read_b64_tr_b16 v[124:125], v5 offset:53248\n"
    "v_mfma_f32_16x16x32_bf16 v[252:255], v[20:23], v[84:87], v[252:255]\n"
    "ds_read_b64_tr_b16 v[126:127], v5 offset:53504\n"
    "s_waitcnt lgkmcnt(8)\n"
    "v_mfma_f32_16x16x32_bf16 v[240:243], v[24:27], v[88:91], v[240:243]\n"
    "ds_read_b64_tr_b16 v[80:81], v1 offset:25088\n"
    "v_mfma_f32_16x16x32_bf16 v[244:247], v[28:31], v[88:91], v[244:247]\n"
    "ds_read_b64_tr_b16 v[82:83], v1 offset:25344\n"
    "v_mfma_f32_16x16x32_bf16 v[248:251], v[24:27], v[92:95], v[248:251]\n"
    "ds_read_b64_tr_b16 v[84:85], v4 offset:25088\n"
    "v_mfma_f32_16x16x32_bf16 v[252:255], v[28:31], v[92:95], v[252:255]\n"
    "ds_read_b64_tr_b16 v[86:87], v4 offset:25344\n"
    "s_waitcnt lgkmcnt(8)\n"
    "v_mfma_f32_16x16x32_bf16 v[240:243], v[32:35], v[112:115], v[240:243]\n"
    "ds_read_b64_tr_b16 v[88:89], v1 offset:29184\n"
    "v_mfma_f32_16x16x32_bf16 v[244:247], v[36:39], v[112:115], v[244:247]\n"
    "ds_read_b64_tr_b16 v[90:91], v1 offset:29440\n"
    "v_mfma_f32_16x16x32_bf16 v[248:251], v[32:35], v[116:119], v[248:251]\n"
    "ds_read_b64_tr_b16 v[92:93], v4 offset:29184\n"
    "v_mfma_f32_16x16x32_bf16 v[252:255], v[36:39], v[116:119], v[252:255]\n"
    "ds_read_b64_tr_b16 v[94:95], v4 offset:29440\n"
    "s_waitcnt lgkmcnt(8)\n"
    "v_mfma_f32_16x16x32_bf16 v[240:243], v[40:43], v[120:123], v[240:243]\n"
    "ds_read_b64_tr_b16 v[112:113], v2 offset:8704\n"
    "v_mfma_f32_16x16x32_bf16 v[244:247], v[44:47], v[120:123], v[244:247]\n"
    "ds_read_b64_tr_b16 v[114:115], v2 offset:8960\n"
    "v_mfma_f32_16x16x32_bf16 v[248:251], v[40:43], v[124:127], v[248:251]\n"
    "ds_read_b64_tr_b16 v[116:117], v5 offset:8704\n"
    "v_mfma_f32_16x16x32_bf16 v[252:255], v[44:47], v[124:127], v[252:255]\n"
    "ds_read_b64_tr_b16 v[118:119], v5 offset:8960\n"
    "s_waitcnt lgkmcnt(8)\n"
    "v_mfma_f32_16x16x32_bf16 v[176:179], v[64:67], v[80:83], 0\n"
    "ds_read_b64_tr_b16 v[120:121], v2 offset:12800\n"
    "ds_read_b64_tr_b16 v[122:123], v2 offset:13056\n"
    "v_mfma_f32_16x16x32_bf16 v[180:183], v[68:71], v[80:83], 0\n"
    "ds_read_b32 v9, v7\n"
    "v_mfma_f32_16x16x32_bf16 v[184:187], v[64:67], v[84:87], 0\n"
    "ds_read_b64_tr_b16 v[124:125], v5 offset:12800\n"
    "v_cvt_pk_bf16_f32 v240, v240, v241\n"
    "v_mfma_f32_16x16x32_bf16 v[188:191], v[68:71], v[84:87], 0\n"
    "ds_read_b64_tr_b16 v[126:127], v5 offset:13056\n"
    "s_waitcnt lgkmcnt(9)\n"
    "v_mfma_f32_16x16x32_bf16 v[176:179], v[72:75], v[88:91], v[176:179]\n"
    "ds_read_b64_tr_b16 v[80:81], v2 offset:49664\n"
    "v_cvt_pk_bf16_f32 v241, v242, v243\n"
    "v_mfma_f32_16x16x32_bf16 v[180:183], v[76:79], v[88:91], v[180:183]\n"
    "ds_read_b64_tr_b16 v[82:83], v2 offset:49920\n"
    "v_mfma_f32_16x16x32_bf16 v[184:187], v[72:75], v[92:95], v[184:187]\n"
    "ds_read_b64_tr_b16 v[84:85], v5 offset:49664\n"
    "buffer_load_dword off, s[88:91], 0 offset:180 lds\n"
    "v_mfma_f32_16x16x32_bf16 v[188:191], v[76:79], v[92:95], v[188:191]\n"
    "ds_read_b64_tr_b16 v[86:87], v5 offset:49920\n"
    "s_waitcnt lgkmcnt(9)\n"
    "v_mfma_f32_16x16x32_bf16 v[176:179], v[16:19], v[112:115], v[176:179]\n"
    "ds_read_b64_tr_b16 v[88:89], v2 offset:53760\n"
    "v_mfma_f32_16x16x32_bf16 v[180:183], v[20:23], v[112:115], v[180:183]\n"
    "ds_read_b64_tr_b16 v[90:91], v2 offset:54016\n"
    "v_cvt_pk_bf16_f32 v242, v244, v245\n"
    "v_mfma_f32_16x16x32_bf16 v[184:187], v[16:19], v[116:119], v[184:187]\n"
    "ds_read_b64_tr_b16 v[92:93], v5 offset:53760\n"
    "v_cvt_pk_bf16_f32 v243, v246, v247\n"
    "v_mfma_f32_16x16x32_bf16 v[188:191], v[20:23], v[116:119], v[188:191]\n"
    "ds_read_b64_tr_b16 v[94:95], v5 offset:54016\n"
    "s_waitcnt lgkmcnt(10)\n"
    "v_cmp_gt_u32 vcc, 0xb0000019, v9\n"
    "s_waitcnt vmcnt(5)\n"
    "v_mfma_f32_16x16x32_bf16 v[224:227], v[240:243], v[96:99], v[224:227]\n"
    "v_cvt_pk_bf16_f32 v244, v248, v249\n"
    "v_cvt_pk_bf16_f32 v245, v250, v251\n"
    "s_waitcnt vmcnt(4)\n"
    "v_mfma_f32_16x16x32_bf16 v[232:235], v[240:243], v[100:103], v[232:235]\n"
    "v_cvt_pk_bf16_f32 v246, v252, v253\n"
    "v_cvt_pk_bf16_f32 v247, v254, v255\n"
    "s_waitcnt lgkmcnt(8)\n"
    "v_mfma_f32_16x16x32_bf16 v[176:179], v[24:27], v[120:123], v[176:179]\n"
    "s_cbranch_vccnz CS25_%=\n"
    "CB25_%=:\n"
    "ds_read_b64_tr_b16 v[112:113], v2 offset:57344\n"
    "v_mfma_f32_16x16x32_bf16 v[180:183], v[28:31], v[120:123], v[180:183]\n"
    "ds_read_b64_tr_b16 v[114:115], v2 offset:57600\n"
    "v_mfma_f32_16x16x32_bf16 v[184:187], v[24:27], v[124:127], v[184:187]\n"
    "ds_read_b64_tr_b16 v[116:117], v5 offset:57344\n"
    "v_mfma_f32_16x16x32_bf16 v[188:191], v[28:31], v[124:127], v[188:191]\n"
    "ds_read_b64_tr_b16 v[118:119], v5 offset:57600\n"
    "s_waitcnt lgkmcnt(8)\n"
    "v_mfma_f32_16x16x32_bf16 v[176:179], v[32:35], v[80:83], v[176:179]\n"
    "ds_read_b64_tr_b16 v[120:121], v2 offset:61440\n"
    "v_mfma_f32_16x16x32_bf16 v[228:231], v[244:247], v[96:99], v[228:231]\n"
    "buffer_load_dword off, s[88:91], 0 offset:184 lds\n"
    "v_mfma_f32_16x16x32_bf16 v[236:239], v[244:247], v[100:103], v[236:239]\n"
    "v_cmp_gt_u32 vcc, 0xb000001a, v9\n"
    "v_add_u32 v10, 0, v8\n"
    "v_mfma_f32_16x16x32_bf16 v[180:183], v[36:39], v[80:83], v[180:183]\n"
    "ds_read_b64_tr_b16 v[122:123], v2 offset:61696\n"
    "s_cbranch_vccnz CS26_%=\n"
    "CB26_%=:\n"
    "v_mfma_f32_16x16x32_bf16 v[184:187], v[32:35], v[84:87], v[184:187]\n"
    "ds_read_b64_tr_b16 v[124:125], v5 offset:61440\n"
    "ds_read_b128 v[48:51], v10 offset:0\n"
    "v_mfma_f32_16x16x32_bf16 v[188:191], v[36:39], v[84:87], v[188:191]\n"
    "ds_read_b64_tr_b16 v[126:127], v5 offset:61696\n"
    "s_waitcnt lgkmcnt(9)\n"
    "v_mfma_f32_16x16x32_bf16 v[176:179], v[40:43], v[88:91], v[176:179]\n"
    "ds_read_b64_tr_b16 v[80:81], v3 offset:16384\n"
    "buffer_load_dwordx4 v[104:107], v11, s[48:51], s20 offen offset:0 sc1\n"
    "v_mfma_f32_16x16x32_bf16 v[180:183], v[44:47], v[88:91], v[180:183]\n"
    "ds_read_b64_tr_b16 v[82:83], v3 offset:16640\n"
    "buffer_load_dwordx4 v[108:111], v11, s[48:51], s20 offen offset:1024 sc1\n"
    "v_mfma_f32_16x16x32_bf16 v[184:187], v[40:43], v[92:95], v[184:187]\n"
    "ds_read_b64_tr_b16 v[84:85], v6 offset:16384\n"
    "ds_read_b128 v[52:55], v10 offset:2048\n"
    "v_mfma_f32_16x16x32_bf16 v[188:191], v[44:47], v[92:95], v[188:191]\n"
    "ds_read_b64_tr_b16 v[86:87], v6 offset:16640\n"
    "s_waitcnt lgkmcnt(10)\n"
    "v_mfma_f32_16x16x32_bf16 v[240:243], v[64:67], v[112:115], 0\n"
    "ds_read_b64_tr_b16 v[88:89], v3 offset:20480\n"
    "v_cvt_pk_bf16_f32 v224, v224, v225\n"
    "v_mfma_f32_16x16x32_bf16 v[244:247], v[68:71], v[112:115], 0\n"
    "ds_read_b64_tr_b16 v[90:91], v3 offset:20736\n"
    "v_cvt_pk_bf16_f32 v225, v226, v227\n"
    "v_mfma_f32_16x16x32_bf16 v[248:251], v[64:67], v[116:119], 0\n"
    "ds_read_b64_tr_b16 v[92:93], v6 offset:20480\n"
    "ds_read_b128 v[56:59], v10 offset:512\n"
    "v_mfma_f32_16x16x32_bf16 v[252:255], v[68:71], v[116:119], 0\n"
    "ds_read_b64_tr_b16 v[94:95], v6 offset:20736\n"
    "s_waitcnt lgkmcnt(10)\n"
    "v_mfma_f32_16x16x32_bf16 v[240:243], v[72:75], v[120:123], v[240:243]\n"
    "ds_read_b64_tr_b16 v[112:113], v2 offset:32768\n"
    "v_cvt_pk_bf16_f32 v226, v228, v229\n"
    "v_mfma_f32_16x16x32_bf16 v[244:247], v[76:79], v[120:123], v[244:247]\n"
    "ds_read_b64_tr_b16 v[114:115], v2 offset:33024\n"
    "v_mfma_f32_16x16x32_bf16 v[248:251], v[72:75], v[124:127], v[248:251]\n"
    "ds_read_b64_tr_b16 v[116:117], v5 offset:32768\n"
    "ds_read_b128 v[60:63], v10 offset:2560\n"
    "v_mfma_f32_16x16x32_bf16 v[252:255], v[76:79], v[124:127], v[252:255]\n"
    "ds_read_b64_tr_b16 v[118:119], v5 offset:33024\n"
    "s_waitcnt lgkmcnt(10)\n"
    "v_mfma_f32_16x16x32_bf16 v[240:243], v[16:19], v[80:83], v[240:243]\n"
    "ds_read_b64_tr_b16 v[120:121], v2 offset:36864\n"
    "v_cvt_pk_bf16_f32 v227, v230, v231\n"
    "v_mfma_f32_16x16x32_bf16 v[244:247], v[20:23], v[80:83], v[244:247]\n"
    "ds_read_b64_tr_b16 v[122:123], v2 offset:37120\n"
    "v_cvt_pk_bf16_f32 v228, v232, v233\n"
    "v_mfma_f32_16x16x32_bf16 v[248:251], v[16:19], v[84:87], v[248:251]\n"
    "ds_read_b64_tr_b16 v[124:125], v5 offset:36864\n"
    "v_cvt_pk_bf16_f32 v229, v234, v235\n"
    "v_mfma_f32_16x16x32_bf16 v[252:255], v[20:23], v[84:87], v[252:255]\n"
    "ds_read_b64_tr_b16 v[126:127], v5 offset:37120\n"
    "v_cvt_pk_bf16_f32 v230, v236, v237\n"
    "v_cvt_pk_bf16_f32 v231, v238, v239\n"
    "v_cvt_pk_bf16_f32 v176, v176, v177\n"
    "s_add_u32 s72, s52, 0\n"
    "buffer_store_dwordx4 v[224:227], v11, s[48:51], s72 offen sc1\n"
    "s_add_u32 s72, s52, 16384\n"
    "buffer_store_dwordx4 v[228:231], v11, s[48:51], s72 offen sc1\n"
    "s_waitcnt lgkmcnt(9)\n"
    "v_mfma_f32_16x16x32_bf16 v[240:243], v[24:27], v[88:91], v[240:243]\n"
    "ds_read_b64_tr_b16 v[80:81], v2 offset:57856\n"
    "v_cvt_pk_bf16_f32 v177, v178, v179\n"
    "v_mfma_f32_16x16x32_bf16 v[244:247], v[28:31], v[88:91], v[244:247]\n"
    "ds_read_b64_tr_b16 v[82:83], v2 offset:58112\n"
    "v_cvt_pk_bf16_f32 v178, v180, v181\n"
    "v_mfma_f32_16x16x32_bf16 v[248:251], v[24:27], v[92:95], v[248:251]\n"
    "ds_read_b64_tr_b16 v[84:85], v5 offset:57856\n"
    "ds_read_b32 v9, v7\n"
    "v_mfma_f32_16x16x32_bf16 v[252:255], v[28:31], v[92:95], v[252:255]\n"
    "ds_read_b64_tr_b16 v[86:87], v5 offset:58112\n"
    "s_waitcnt lgkmcnt(9)\n"
    "v_mfma_f32_16x16x32_bf16 v[240:243], v[32:35], v[112:115], v[240:243]\n"
    "ds_read_b64_tr_b16 v[88:89], v2 offset:61952\n"
    "v_cvt_pk_bf16_f32 v179, v182, v183\n"
    "v_mfma_f32_16x16x32_bf16 v[244:247], v[36:39], v[112:115], v[244:247]\n"
    "ds_read_b64_tr_b16 v[90:91], v2 offset:62208\n"
    "v_mfma_f32_16x16x32_bf16 v[208:211], v[176:179], v[96:99], v[208:211]\n"
    "v_cvt_pk_bf16_f32 v180, v184, v185\n"
    "v_cvt_pk_bf16_f32 v181, v186, v187\n"
    "v_mfma_f32_16x16x32_bf16 v[216:219], v[176:179], v[100:103], v[216:219]\n"
    "v_cvt_pk_bf16_f32 v182, v188, v189\n"
    "v_cvt_pk_bf16_f32 v183, v190, v191\n"
    "v_mfma_f32_16x16x32_bf16 v[248:251], v[32:35], v[116:119], v[248:251]\n"
    "ds_read_b64_tr_b16 v[92:93], v5 offset:61952\n"
    "v_mfma_f32_16x16x32_bf16 v[212:215], v[180:183], v[96:99], v[212:215]\n"
    "v_mfma_f32_16x16x32_bf16 v[220:223], v[180:183], v[100:103], v[220:223]\n"
    "s_waitcnt lgkmcnt(4)\n"
    "v_cmp_gt_u32 vcc, 0xb000001b, v9\n"
    "v_add_u32 v10, 81920, v8\n"
    "v_mfma_f32_16x16x32_bf16 v[252:255], v[36:39], v[116:119], v[252:255]\n"
    "ds_read_b64_tr_b16 v[94:95], v5 offset:62208\n"
    "v_mfma_f32_16x16x32_bf16 v[240:243], v[40:43], v[120:123], v[240:243]\n"
    "ds_read_b64_tr_b16 v[112:113], v3 offset:16896\n"
    "s_cbranch_vccnz CS27_%=\n"
    "CB27_%=:\n"
    "v_mfma_f32_16x16x32_bf16 v[244:247], v[44:47], v[120:123], v[244:247]\n"
    "ds_read_b64_tr_b16 v[114:115], v3 offset:17152\n"
    "ds_read_b128 v[224:227], v10 offset:0\n"
    "v_mfma_f32_16x16x32_bf16 v[248:251], v[40:43], v[124:127], v[248:251]\n"
    "ds_read_b64_tr_b16 v[116:117], v6 offset:16896\n"
    "v_cvt_pk_bf16_f32 v208, v208, v209\n"
    "v_mfma_f32_16x16x32_bf16 v[252:255], v[44:47], v[124:127], v[252:255]\n"
    "ds_read_b64_tr_b16 v[118:119], v6 offset:17152\n"
    "s_waitcnt lgkmcnt(9)\n"
    "v_mfma_f32_16x16x32_bf16 v[176:179], v[64:67], v[80:83], 0\n"
    "ds_read_b64_tr_b16 v[120:121], v3 offset:20992\n"
    "ds_read_b64_tr_b16 v[122:123], v3 offset:21248\n"
    "v_mfma_f32_16x16x32_bf16 v[180:183], v[68:71], v[80:83], 0\n"
    "ds_read_b32 v9, v7\n"
    "v_cvt_pk_bf16_f32 v209, v210, v211\n"
    "v_mfma_f32_16x16x32_bf16 v[184:187], v[64:67], v[84:87], 0\n"
    "ds_read_b64_tr_b16 v[124:125], v6 offset:20992\n"
    "ds_read_b128 v[228:231], v10 offset:2048\n"
    "v_mfma_f32_16x16x32_bf16 v[188:191], v[68:71], v[84:87], 0\n"
    "ds_read_b64_tr_b16 v[126:127], v6 offset:21248\n"
    "s_waitcnt lgkmcnt(11)\n"
    "v_mfma_f32_16x16x32_bf16 v[176:179], v[72:75], v[88:91], v[176:179]\n"
    "ds_read_b64_tr_b16 v[80:81], v2 offset:33280\n"
    "v_cvt_pk_bf16_f32 v210, v212, v213\n"
    "v_mfma_f32_16x16x32_bf16 v[180:183], v[76:79], v[88:91], v[180:183]\n"
    "ds_read_b64_tr_b16 v[82:83], v2 offset:33536\n"
    "ds_read_b128 v[232:235], v10 offset:512\n"
    "v_mfma_f32_16x16x32_bf16 v[184:187], v[72:75], v[92:95], v[184:187]\n"
    "ds_read_b64_tr_b16 v[84:85], v5 offset:33280\n"
    "v_cvt_pk_bf16_f32 v211, v214, v215\n"
    "v_mfma_f32_16x16x32_bf16 v[188:191], v[76:79], v[92:95], v[188:191]\n"
    "s_waitcnt lgkmcnt(14)\n"
    "ds_read_b64_tr_b16 v[86:87], v5 offset:33536\n"
    "s_waitcnt lgkmcnt(11)\n"
    "v_mfma_f32_16x16x32_bf16 v[176:179], v[16:19], v[112:115], v[176:179]\n"
    "ds_read_b64_tr_b16 v[88:89], v2 offset:37376\n"
    "v_cvt_pk_bf16_f32 v212, v216, v217\n"
    "v_mfma_f32_16x16x32_bf16 v[180:183], v[20:23], v[112:115], v[180:183]\n"
    "ds_read_b64_tr_b16 v[90:91], v2 offset:37632\n"
    "ds_read_b128 v[236:239], v10 offset:2560\n"
    "v_mfma_f32_16x16x32_bf16 v[184:187], v[16:19], v[116:119], v[184:187]\n"
    "ds_read_b64_tr_b16 v[92:93], v5 offset:37376\n"
    "v_cvt_pk_bf16_f32 v213, v218, v219\n"
    "v_mfma_f32_16x16x32_bf16 v[188:191], v[20:23], v[116:119], v[188:191]\n"
    "s_waitcnt lgkmcnt(14)\n"
    "ds_read_b64_tr_b16 v[94:95], v5 offset:37632\n"
    "s_waitcnt lgkmcnt(13)\n"
    "v_cmp_gt_u32 vcc, 0xb000001c, v9\n"
    "v_cvt_pk_bf16_f32 v214, v220, v221\n"
    "v_cvt_pk_bf16_f32 v215, v222, v223\n"
    "v_cvt_pk_bf16_f32 v240, v240, v241\n"
    "s_add_u32 s72, s52, 131072\n"
    "buffer_store_dwordx4 v[208:211], v11, s[48:51], s72 offen sc1\n"
    "s_add_u32 s72, s52, 147456\n"
    "buffer_store_dwordx4 v[212:215], v11, s[48:51], s72 offen sc1\n"
    "s_waitcnt lgkmcnt(10)\n"
    "v_mfma_f32_16x16x32_bf16 v[176:179], v[24:27], v[120:123], v[176:179]\n"
    "s_cbranch_vccnz CS28_%=\n"
    "CB28_%=:\n"
    "ds_read_b64_tr_b16 v[112:113], v2 offset:40960\n"
    "v_mfma_f32_16x16x32_bf16 v[180:183], v[28:31], v[120:123], v[180:183]\n"
    "ds_read_b64_tr_b16 v[114:115], v2 offset:41216\n"
    "v_cvt_pk_bf16_f32 v241, v242, v243\n"
    "v_mfma_f32_16x16x32_bf16 v[184:187], v[24:27], v[124:127], v[184:187]\n"
    "ds_read_b64_tr_b16 v[116:117], v5 offset:40960\n"
    "v_mfma_f32_16x16x32_bf16 v[188:191], v[28:31], v[124:127], v[188:191]\n"
    "ds_read_b64_tr_b16 v[118:119], v5 offset:41216\n"
    "s_waitcnt lgkmcnt(9)\n"
    "v_mfma_f32_16x16x32_bf16 v[176:179], v[32:35], v[80:83], v[176:179]\n"
    "ds_read_b64_tr_b16 v[120:121], v2 offset:45056\n"
    "v_mfma_f32_16x16x32_bf16 v[180:183], v[36:39], v[80:83], v[180:183]\n"
    "ds_read_b64_tr_b16 v[122:123], v2 offset:45312\n"
    "ds_read_b32 v9, v7\n"
    "v_mfma_f32_16x16x32_bf16 v[184:187], v[32:35], v[84:87], v[184:187]\n"
    "ds_read_b64_tr_b16 v[124:125], v5 offset:45056\n"
    "v_cvt_pk_bf16_f32 v242, v244, v245\n"
    "v_mfma_f32_16x16x32_bf16 v[188:191], v[36:39], v[84:87], v[188:191]\n"
    "ds_read_b64_tr_b16 v[126:127], v5 offset:45312\n"
    "s_waitcnt lgkmcnt(9)\n"
    "v_mfma_f32_16x16x32_bf16 v[176:179], v[40:43], v[88:91], v[176:179]\n"
    "ds_read_b64_tr_b16 v[80:81], v1 offset:32768\n"
    "v_cvt_pk_bf16_f32 v243, v246, v247\n"
    "v_mfma_f32_16x16x32_bf16 v[180:183], v[44:47], v[88:91], v[180:183]\n"
    "ds_read_b64_tr_b16 v[82:83], v1 offset:33024\n"
    "v_mfma_f32_16x16x32_bf16 v[192:195], v[240:243], v[96:99], v[192:195]\n"
    "v_cvt_pk_bf16_f32 v244, v248, v249\n"
    "v_cvt_pk_bf16_f32 v245, v250, v251\n"
    "v_mfma_f32_16x16x32_bf16 v[200:203], v[240:243], v[100:103], v[200:203]\n"
    "v_cvt_pk_bf16_f32 v246, v252, v253\n"
    "v_cvt_pk_bf16_f32 v247, v254, v255\n"
    "v_mfma_f32_16x16x32_bf16 v[184:187], v[40:43], v[92:95], v[184:187]\n"
    "ds_read_b64_tr_b16 v[84:85], v4 offset:32768\n"
    "v_mfma_f32_16x16x32_bf16 v[196:199], v[244:247], v[96:99], v[196:199]\n"
    "v_mfma_f32_16x16x32_bf16 v[204:207], v[244:247], v[100:103], v[204:207]\n"
    "s_waitcnt lgkmcnt(5)\n"
    "v_cmp_gt_u32 vcc, 0xb000001d, v9\n"
    "v_add_u32 v10, 49152, v8\n"
    "v_mfma_f32_16x16x32_bf16 v[188:191], v[44:47], v[92:95], v[188:191]\n"
    "ds_read_b64_tr_b16 v[86:87], v4 offset:33024\n"
    "v_mfma_f32_16x16x32_bf16 v[240:243], v[48:51], v[112:115], 0\n"
    "ds_read_b64_tr_b16 v[88:89], v1 offset:36864\n"
    "s_cbranch_vccnz CS29_%=\n"
    "CB29_%=:\n"
    "v_mfma_f32_16x16x32_bf16 v[244:247], v[52:55], v[112:115], 0\n"
    "ds_read_b64_tr_b16 v[90:91], v1 offset:37120\n"
    "ds_read_b128 v[208:211], v10 offset:0\n"
    "v_mfma_f32_16x16x32_bf16 v[248:251], v[48:51], v[116:119], 0\n"
    "ds_read_b64_tr_b16 v[92:93], v4 offset:36864\n"
    "ds_read_b128 v[212:215], v10 offset:2048\n"
    "v_mfma_f32_16x16x32_bf16 v[252:255], v[52:55], v[116:119], 0\n"
    "ds_read_b64_tr_b16 v[94:95], v4 offset:37120\n"
    "s_waitcnt lgkmcnt(10)\n"
    "v_mfma_f32_16x16x32_bf16 v[240:243], v[56:59], v[120:123], v[240:243]\n"
    "ds_read_b64_tr_b16 v[112:113], v1 offset:40960\n"
    "ds_read_b128 v[216:219], v10 offset:512\n"
    "v_mfma_f32_16x16x32_bf16 v[244:247], v[60:63], v[120:123], v[244:247]\n"
    "ds_read_b64_tr_b16 v[114:115], v1 offset:41216\n"
    "v_mfma_f32_16x16x32_bf16 v[248:251], v[56:59], v[124:127], v[248:251]\n"
    "ds_read_b64_tr_b16 v[116:117], v4 offset:40960\n"
    "ds_read_b128 v[220:223], v10 offset:2560\n"
    "v_mfma_f32_16x16x32_bf16 v[252:255], v[60:63], v[124:127], v[252:255]\n"
    "s_waitcnt lgkmcnt(14)\n"
    "ds_read_b64_tr_b16 v[118:119], v4 offset:41216\n"
    "s_waitcnt lgkmcnt(12)\n"
    "v_mfma_f32_16x16x32_bf16 v[240:243], v[224:227], v[80:83], v[240:243]\n"
    "ds_read_b64_tr_b16 v[120:121], v1 offset:45056\n"
    "v_cvt_pk_bf16_f32 v176, v176, v177\n"
    "v_mfma_f32_16x16x32_bf16 v[244:247], v[228:231], v[80:83], v[244:247]\n"
    "ds_read_b64_tr_b16 v[122:123], v1 offset:45312\n"
    "v_mfma_f32_16x16x32_bf16 v[248:251], v[224:227], v[84:87], v[248:251]\n"
    "ds_read_b64_tr_b16 v[124:125], v4 offset:45056\n"
    "v_cvt_pk_bf16_f32 v177, v178, v179\n"
    "v_mfma_f32_16x16x32_bf16 v[252:255], v[228:231], v[84:87], v[252:255]\n"
    "s_waitcnt lgkmcnt(14)\n"
    "ds_read_b64_tr_b16 v[126:127], v4 offset:45312\n"
    "s_waitcnt lgkmcnt(10)\n"
    "v_mfma_f32_16x16x32_bf16 v[240:243], v[232:235], v[88:91], v[240:243]\n"
    "ds_read_b64_tr_b16 v[80:81], v2 offset:41472\n"
    "v_cvt_pk_bf16_f32 v178, v180, v181\n"
    "v_mfma_f32_16x16x32_bf16 v[244:247], v[236:239], v[88:91], v[244:247]\n"
    "ds_read_b64_tr_b16 v[82:83], v2 offset:41728\n"
    "v_cvt_pk_bf16_f32 v179, v182, v183\n"
    "v_mfma_f32_16x16x32_bf16 v[248:251], v[232:235], v[92:95], v[248:251]\n"
    "ds_read_b64_tr_b16 v[84:85], v5 offset:41472\n"
    "v_mfma_f32_16x16x32_bf16 v[128:131], v[176:179], v[96:99], v[128:131]\n"
    "v_cvt_pk_bf16_f32 v180, v184, v185\n"
    "v_cvt_pk_bf16_f32 v181, v186, v187\n"
    "v_mfma_f32_16x16x32_bf16 v[136:139], v[176:179], v[100:103], v[136:139]\n"
    "v_cvt_pk_bf16_f32 v182, v188, v189\n"
    "v_cvt_pk_bf16_f32 v183, v190, v191\n"
    "v_mfma_f32_16x16x32_bf16 v[252:255], v[236:239], v[92:95], v[252:255]\n"
    "ds_read_b64_tr_b16 v[86:87], v5 offset:41728\n"
    "s_waitcnt lgkmcnt(8)\n"
    "v_mfma_f32_16x16x32_bf16 v[240:243], v[208:211], v[112:115], v[240:243]\n"
    "ds_read_b64_tr_b16 v[88:89], v2 offset:45568\n"
    "v_mfma_f32_16x16x32_bf16 v[244:247], v[212:215], v[112:115], v[244:247]\n"
    "ds_read_b64_tr_b16 v[90:91], v2 offset:45824\n"
    "v_mfma_f32_16x16x32_bf16 v[248:251], v[208:211], v[116:119], v[248:251]\n"
    "ds_read_b64_tr_b16 v[92:93], v5 offset:45568\n"
    "v_mfma_f32_16x16x32_bf16 v[252:255], v[212:215], v[116:119], v[252:255]\n"
    "ds_read_b64_tr_b16 v[94:95], v5 offset:45824\n"
    "s_waitcnt lgkmcnt(8)\n"
    "v_mfma_f32_16x16x32_bf16 v[240:243], v[216:219], v[120:123], v[240:243]\n"
    "ds_read_b64_tr_b16 v[112:113], v1 offset:33280\n"
    "v_mfma_f32_16x16x32_bf16 v[132:135], v[180:183], v[96:99], v[132:135]\n"
    "v_mfma_f32_16x16x32_bf16 v[140:143], v[180:183], v[100:103], v[140:143]\n"
    "v_mfma_f32_16x16x32_bf16 v[244:247], v[220:223], v[120:123], v[244:247]\n"
    "ds_read_b64_tr_b16 v[114:115], v1 offset:33536\n"
    "v_mfma_f32_16x16x32_bf16 v[248:251], v[216:219], v[124:127], v[248:251]\n"
    "ds_read_b64_tr_b16 v[116:117], v4 offset:33280\n"
    "v_mfma_f32_16x16x32_bf16 v[252:255], v[220:223], v[124:127], v[252:255]\n"
    "ds_read_b64_tr_b16 v[118:119], v4 offset:33536\n"
    "s_waitcnt lgkmcnt(8)\n"
    "v_mfma_f32_16x16x32_bf16 v[176:179], v[48:51], v[80:83], 0\n"
    "ds_read_b64_tr_b16 v[120:121], v1 offset:37376\n"
    "ds_read_b64_tr_b16 v[122:123], v1 offset:37632\n"
    "v_mfma_f32_16x16x32_bf16 v[180:183], v[52:55], v[80:83], 0\n"
    "ds_read_b32 v9, v7\n"
    "v_mfma_f32_16x16x32_bf16 v[184:187], v[48:51], v[84:87], 0\n"
    "ds_read_b64_tr_b16 v[124:125], v4 offset:37376\n"
    "v_cvt_pk_bf16_f32 v240, v240, v241\n"
    "v_mfma_f32_16x16x32_bf16 v[188:191], v[52:55], v[84:87], 0\n"
    "ds_read_b64_tr_b16 v[126:127], v4 offset:37632\n"
    "s_waitcnt lgkmcnt(9)\n"
    "v_mfma_f32_16x16x32_bf16 v[176:179], v[56:59], v[88:91], v[176:179]\n"
    "ds_read_b64_tr_b16 v[80:81], v1 offset:41472\n"
    "v_cvt_pk_bf16_f32 v241, v242, v243\n"
    "v_mfma_f32_16x16x32_bf16 v[180:183], v[60:63], v[88:91], v[180:183]\n"
    "ds_read_b64_tr_b16 v[82:83], v1 offset:41728\n"
    "v_mov_b32 v14, s6\n"
    "v_mfma_f32_16x16x32_bf16 v[184:187], v[56:59], v[92:95], v[184:187]\n"
    "ds_read_b64_tr_b16 v[84:85], v4 offset:41472\n"
    "v_mov_b32 v15, s7\n"
    "v_mfma_f32_16x16x32_bf16 v[188:191], v[60:63], v[92:95], v[188:191]\n"
    "ds_read_b64_tr_b16 v[86:87], v4 offset:41728\n"
    "s_waitcnt lgkmcnt(9)\n"
    "v_mfma_f32_16x16x32_bf16 v[176:179], v[224:227], v[112:115], v[176:179]\n"
    "ds_read_b64_tr_b16 v[88:89], v1 offset:45568\n"
    "v_and_b32 v13, 0x400, v12\n"
    "v_mfma_f32_16x16x32_bf16 v[180:183], v[228:231], v[112:115], v[180:183]\n"
    "ds_read_b64_tr_b16 v[90:91], v1 offset:45824\n"
    "v_add_u32 v13, v13, v12\n"
    "v_mfma_f32_16x16x32_bf16 v[184:187], v[224:227], v[116:119], v[184:187]\n"
    "ds_read_b64_tr_b16 v[92:93], v4 offset:45568\n"
    "s_waitcnt vmcnt(0)\n"
    "buffer_store_dwordx2 v[14:15], v13, s[48:51], s53 offen sc1\n"
    "v_mfma_f32_16x16x32_bf16 v[188:191], v[228:231], v[116:119], v[188:191]\n"
    "ds_read_b64_tr_b16 v[94:95], v4 offset:45824\n"
    "s_waitcnt lgkmcnt(10)\n"
    "v_cmp_gt_u32 vcc, 0xb000001e, v9\n"
    "s_waitcnt lgkmcnt(8)\n"
    "v_mfma_f32_16x16x32_bf16 v[176:179], v[232:235], v[120:123], v[176:179]\n"
    "s_cbranch_vccnz CS30_%=\n"
    "CB30_%=:\n"
    "ds_read_b64_tr_b16 v[112:113], v2 offset:0\n"
    "v_mfma_f32_16x16x32_bf16 v[180:183], v[236:239], v[120:123], v[180:183]\n"
    "ds_read_b64_tr_b16 v[114:115], v2 offset:256\n"
    "v_mfma_f32_16x16x32_bf16 v[184:187], v[232:235], v[124:127], v[184:187]\n"
    "ds_read_b64_tr_b16 v[116:117], v5 offset:0\n"
    "buffer_load_dword off, s[88:91], 0 offset:216 lds\n"
    "v_mfma_f32_16x16x32_bf16 v[188:191], v[236:239], v[124:127], v[188:191]\n"
    "ds_read_b64_tr_b16 v[118:119], v5 offset:256\n"
    "s_waitcnt lgkmcnt(8)\n"
    "v_mfma_f32_16x16x32_bf16 v[176:179], v[208:211], v[80:83], v[176:179]\n"
    "ds_read_b64_tr_b16 v[120:121], v2 offset:4096\n"
    "v_cvt_pk_bf16_f32 v242, v244, v245\n"
    "v_mfma_f32_16x16x32_bf16 v[180:183], v[212:215], v[80:83], v[180:183]\n"
    "ds_read_b64_tr_b16 v[122:123], v2 offset:4352\n"
    "v_cvt_pk_bf16_f32 v243, v246, v247\n"
    "v_mfma_f32_16x16x32_bf16 v[184:187], v[208:211], v[84:87], v[184:187]\n"
    "ds_read_b64_tr_b16 v[124:125], v5 offset:4096\n"
    "v_mfma_f32_16x16x32_bf16 v[192:195], v[240:243], v[104:107], v[192:195]\n"
    "v_cvt_pk_bf16_f32 v244, v248, v249\n"
    "v_cvt_pk_bf16_f32 v245, v250, v251\n"
    "v_mfma_f32_16x16x32_bf16 v[200:203], v[240:243], v[108:111], v[200:203]\n"
    "v_cvt_pk_bf16_f32 v246, v252, v253\n"
    "v_cvt_pk_bf16_f32 v247, v254, v255\n"
    "v_mfma_f32_16x16x32_bf16 v[188:191], v[212:215], v[84:87], v[188:191]\n"
    "ds_read_b64_tr_b16 v[126:127], v5 offset:4352\n"
    "v_mfma_f32_16x16x32_bf16 v[196:199], v[244:247], v[104:107], v[196:199]\n"
    "v_cvt_pk_bf16_f32 v192, v192, v193\n"
    "v_cvt_pk_bf16_f32 v193, v194, v195\n"
    "s_waitcnt lgkmcnt(8)\n"
    "v_mfma_f32_16x16x32_bf16 v[176:179], v[216:219], v[88:91], v[176:179]\n"
    "ds_read_b64_tr_b16 v[80:81], v1 offset:16384\n"
    "v_mfma_f32_16x16x32_bf16 v[204:207], v[244:247], v[108:111], v[204:207]\n"
    "v_mfma_f32_16x16x32_bf16 v[180:183], v[220:223], v[88:91], v[180:183]\n"
    "ds_read_b64_tr_b16 v[82:83], v1 offset:16640\n"
    "v_mfma_f32_16x16x32_bf16 v[184:187], v[216:219], v[92:95], v[184:187]\n"
    "ds_read_b64_tr_b16 v[84:85], v4 offset:16384\n"
    "v_cvt_pk_bf16_f32 v194, v196, v197\n"
    "v_mfma_f32_16x16x32_bf16 v[188:191], v[220:223], v[92:95], v[188:191]\n"
    "ds_read_b64_tr_b16 v[86:87], v4 offset:16640\n"
    "s_waitcnt lgkmcnt(8)\n"
    "v_mfma_f32_16x16x32_bf16 v[240:243], v[64:67], v[112:115], 0\n"
    "ds_read_b64_tr_b16 v[88:89], v1 offset:20480\n"
    "ds_read_b64_tr_b16 v[90:91], v1 offset:20736\n"
    "v_mfma_f32_16x16x32_bf16 v[244:247], v[68:71], v[112:115], 0\n"
    "ds_read_b32 v9, v7\n"
    "v_cvt_pk_bf16_f32 v195, v198, v199\n"
    "v_mfma_f32_16x16x32_bf16 v[248:251], v[64:67], v[116:119], 0\n"
    "ds_read_b64_tr_b16 v[92:93], v4 offset:20480\n"
    "v_cvt_pk_bf16_f32 v196, v200, v201\n"
    "v_mfma_f32_16x16x32_bf16 v[252:255], v[68:71], v[116:119], 0\n"
    "ds_read_b64_tr_b16 v[94:95], v4 offset:20736\n"
    "s_waitcnt lgkmcnt(9)\n"
    "v_mfma_f32_16x16x32_bf16 v[240:243], v[72:75], v[120:123], v[240:243]\n"
    "ds_read_b64_tr_b16 v[112:113], v3 offset:0\n"
    "v_cvt_pk_bf16_f32 v197, v202, v203\n"
    "v_mfma_f32_16x16x32_bf16 v[244:247], v[76:79], v[120:123], v[244:247]\n"
    "ds_read_b64_tr_b16 v[114:115], v3 offset:256\n"
    "v_cvt_pk_bf16_f32 v198, v204, v205\n"
    "v_mfma_f32_16x16x32_bf16 v[248:251], v[72:75], v[124:127], v[248:251]\n"
    "ds_read_b64_tr_b16 v[116:117], v6 offset:0\n"
    "v_cvt_pk_bf16_f32 v199, v206, v207\n"
    "v_mfma_f32_16x16x32_bf16 v[252:255], v[76:79], v[124:127], v[252:255]\n"
    "ds_read_b64_tr_b16 v[118:119], v6 offset:256\n"
    "s_waitcnt lgkmcnt(9)\n"
    "v_mfma_f32_16x16x32_bf16 v[240:243], v[16:19], v[80:83], v[240:243]\n"
    "ds_read_b64_tr_b16 v[120:121], v3 offset:4096\n"
    "v_cvt_pk_bf16_f32 v176, v176, v177\n"
    "v_mfma_f32_16x16x32_bf16 v[244:247], v[20:23], v[80:83], v[244:247]\n"
    "ds_read_b64_tr_b16 v[122:123], v3 offset:4352\n"
    "s_add_u32 s72, s22, 0\n"
    "v_mfma_f32_16x16x32_bf16 v[248:251], v[16:19], v[84:87], v[248:251]\n"
    "ds_read_b64_tr_b16 v[124:125], v6 offset:4096\n"
    "buffer_store_dwordx4 v[192:195], v11, s[48:51], s72 offen sc1\n"
    "v_mfma_f32_16x16x32_bf16 v[252:255], v[20:23], v[84:87], v[252:255]\n"
    "ds_read_b64_tr_b16 v[126:127], v6 offset:4352\n"
    "s_waitcnt lgkmcnt(10)\n"
    "v_cmp_gt_u32 vcc, 0xb000001f, v9\n"
    "s_add_u32 s72, s22, 8192\n"
    "buffer_store_dwordx4 v[196:199], v11, s[48:51], s72 offen sc1\n"
    "s_waitcnt lgkmcnt(8)\n"
    "v_mfma_f32_16x16x32_bf16 v[240:243], v[24:27], v[88:91], v[240:243]\n"
    "s_cbranch_vccnz CS31_%=\n"
    "CB31_%=:\n"
    "ds_read_b64_tr_b16 v[80:81], v3 offset:8192\n"
    "v_mfma_f32_16x16x32_bf16 v[244:247], v[28:31], v[88:91], v[244:247]\n"
    "ds_read_b64_tr_b16 v[82:83], v3 offset:8448\n"
    "v_cvt_pk_bf16_f32 v177, v178, v179\n"
    "v_mfma_f32_16x16x32_bf16 v[248:251], v[24:27], v[92:95], v[248:251]\n"
    "ds_read_b64_tr_b16 v[84:85], v6 offset:8192\n"
    "v_cvt_pk_bf16_f32 v178, v180, v181\n"
    "v_mfma_f32_16x16x32_bf16 v[252:255], v[28:31], v[92:95], v[252:255]\n"
    "ds_read_b64_tr_b16 v[86:87], v6 offset:8448\n"
    "s_waitcnt lgkmcnt(8)\n"
    "v_mfma_f32_16x16x32_bf16 v[240:243], v[32:35], v[112:115], v[240:243]\n"
    "ds_read_b64_tr_b16 v[88:89], v3 offset:12288\n"
    "v_cvt_pk_bf16_f32 v179, v182, v183\n"
    "v_mfma_f32_16x16x32_bf16 v[244:247], v[36:39], v[112:115], v[244:247]\n"
    "ds_read_b64_tr_b16 v[90:91], v3 offset:12544\n"
    "v_mfma_f32_16x16x32_bf16 v[128:131], v[176:179], v[104:107], v[128:131]\n"
    "v_cvt_pk_bf16_f32 v180, v184, v185\n"
    "v_cvt_pk_bf16_f32 v181, v186, v187\n"
    "v_mfma_f32_16x16x32_bf16 v[136:139], v[176:179], v[108:111], v[136:139]\n"
    "v_cvt_pk_bf16_f32 v182, v188, v189\n"
    "v_cvt_pk_bf16_f32 v183, v190, v191\n"
    "v_mfma_f32_16x16x32_bf16 v[248:251], v[32:35], v[116:119], v[248:251]\n"
    "ds_read_b64_tr_b16 v[92:93], v6 offset:12288\n"
    "v_mfma_f32_16x16x32_bf16 v[132:135], v[180:183], v[104:107], v[132:135]\n"
    "v_cvt_pk_bf16_f32 v128, v128, v129\n"
    "v_cvt_pk_bf16_f32 v129, v130, v131\n"
    "v_mfma_f32_16x16x32_bf16 v[252:255], v[36:39], v[116:119], v[252:255]\n"
    "ds_read_b64_tr_b16 v[94:95], v6 offset:12544\n"
    "s_waitcnt lgkmcnt(8)\n"
    "v_mfma_f32_16x16x32_bf16 v[240:243], v[40:43], v[120:123], v[240:243]\n"
    "ds_read_b64_tr_b16 v[112:113], v1 offset:24576\n"
    "v_mfma_f32_16x16x32_bf16 v[140:143], v[180:183], v[108:111], v[140:143]\n"
    "v_mfma_f32_16x16x32_bf16 v[244:247], v[44:47], v[120:123], v[244:247]\n"
    "ds_read_b64_tr_b16 v[114:115], v1 offset:24832\n"
    "v_cvt_pk_bf16_f32 v130, v132, v133\n"
    "v_mfma_f32_16x16x32_bf16 v[248:251], v[40:43], v[124:127], v[248:251]\n"
    "ds_read_b64_tr_b16 v[116:117], v4 offset:24576\n"
    "v_cvt_pk_bf16_f32 v131, v134, v135\n"
    "v_mfma_f32_16x16x32_bf16 v[252:255], v[44:47], v[124:127], v[252:255]\n"
    "ds_read_b64_tr_b16 v[118:119], v4 offset:24832\n"
    "s_waitcnt lgkmcnt(8)\n"
    "v_mfma_f32_16x16x32_bf16 v[176:179], v[48:51], v[80:83], 0\n"
    "ds_read_b64_tr_b16 v[120:121], v1 offset:28672\n"
    "v_cvt_pk_bf16_f32 v132, v136, v137\n"
    "v_mfma_f32_16x16x32_bf16 v[180:183], v[52:55], v[80:83], 0\n"
    "ds_read_b64_tr_b16 v[122:123], v1 offset:28928\n"
    "v_cvt_pk_bf16_f32 v133, v138, v139\n"
    "v_mfma_f32_16x16x32_bf16 v[184:187], v[48:51], v[84:87], 0\n"
    "ds_read_b64_tr_b16 v[124:125], v4 offset:28672\n"
    "v_cvt_pk_bf16_f32 v134, v140, v141\n"
    "v_mfma_f32_16x16x32_bf16 v[188:191], v[52:55], v[84:87], 0\n"
    "ds_read_b64_tr_b16 v[126:127], v4 offset:28928\n"
    "s_waitcnt lgkmcnt(8)\n"
    "v_mfma_f32_16x16x32_bf16 v[176:179], v[56:59], v[88:91], v[176:179]\n"
    "ds_read_b64_tr_b16 v[80:81], v2 offset:8192\n"
    "v_cvt_pk_bf16_f32 v135, v142, v143\n"
    "v_mfma_f32_16x16x32_bf16 v[180:183], v[60:63], v[88:91], v[180:183]\n"
    "ds_read_b64_tr_b16 v[82:83], v2 offset:8448\n"
    "v_cvt_pk_bf16_f32 v240, v240, v241\n"
    "v_mfma_f32_16x16x32_bf16 v[184:187], v[56:59], v[92:95], v[184:187]\n"
    "ds_read_b64_tr_b16 v[84:85], v5 offset:8192\n"
    "s_add_u32 s72, s22, 262144\n"
    "v_mfma_f32_16x16x32_bf16 v[188:191], v[60:63], v[92:95], v[188:191]\n"
    "ds_read_b64_tr_b16 v[86:87], v5 offset:8448\n"
    "s_waitcnt lgkmcnt(8)\n"
    "v_mfma_f32_16x16x32_bf16 v[176:179], v[224:227], v[112:115], v[176:179]\n"
    "ds_read_b64_tr_b16 v[88:89], v2 offset:12288\n"
    "buffer_store_dwordx4 v[128:131], v11, s[48:51], s72 offen sc1\n"
    "v_mfma_f32_16x16x32_bf16 v[180:183], v[228:231], v[112:115], v[180:183]\n"
    "ds_read_b64_tr_b16 v[90:91], v2 offset:12544\n"
    "s_add_u32 s72, s22, 270336\n"
    "v_mfma_f32_16x16x32_bf16 v[184:187], v[224:227], v[116:119], v[184:187]\n"
    "ds_read_b64_tr_b16 v[92:93], v5 offset:12288\n"
    "buffer_store_dwordx4 v[132:135], v11, s[48:51], s72 offen sc1\n"
    "v_mfma_f32_16x16x32_bf16 v[188:191], v[228:231], v[116:119], v[188:191]\n"
    "ds_read_b64_tr_b16 v[94:95], v5 offset:12544\n"
    "s_waitcnt lgkmcnt(8)\n"
    "v_mfma_f32_16x16x32_bf16 v[176:179], v[232:235], v[120:123], v[176:179]\n"
    "ds_read_b64_tr_b16 v[112:113], v2 offset:512\n"
    "v_cvt_pk_bf16_f32 v241, v242, v243\n"
    "v_mfma_f32_16x16x32_bf16 v[180:183], v[236:239], v[120:123], v[180:183]\n"
    "ds_read_b64_tr_b16 v[114:115], v2 offset:768\n"
    "v_cvt_pk_bf16_f32 v242, v244, v245\n"
    "v_mfma_f32_16x16x32_bf16 v[184:187], v[232:235], v[124:127], v[184:187]\n"
    "ds_read_b64_tr_b16 v[116:117], v5 offset:512\n"
    "v_cvt_pk_bf16_f32 v243, v246, v247\n"
    "v_mfma_f32_16x16x32_bf16 v[188:191], v[236:239], v[124:127], v[188:191]\n"
    "ds_read_b64_tr_b16 v[118:119], v5 offset:768\n"
    "v_mfma_f32_16x16x32_bf16 v[144:147], v[240:243], v[96:99], v[144:147]\n"
    "v_cvt_pk_bf16_f32 v244, v248, v249\n"
    "v_cvt_pk_bf16_f32 v245, v250, v251\n"
    "v_mfma_f32_16x16x32_bf16 v[152:155], v[240:243], v[100:103], v[152:155]\n"
    "v_cvt_pk_bf16_f32 v246, v252, v253\n"
    "v_cvt_pk_bf16_f32 v247, v254, v255\n"
    "s_waitcnt lgkmcnt(8)\n"
    "v_mfma_f32_16x16x32_bf16 v[176:179], v[208:211], v[80:83], v[176:179]\n"
    "ds_read_b64_tr_b16 v[120:121], v2 offset:4608\n"
    "v_mfma_f32_16x16x32_bf16 v[180:183], v[212:215], v[80:83], v[180:183]\n"
    "ds_read_b64_tr_b16 v[122:123], v2 offset:4864\n"
    "v_mfma_f32_16x16x32_bf16 v[184:187], v[208:211], v[84:87], v[184:187]\n"
    "ds_read_b64_tr_b16 v[124:125], v5 offset:4608\n"
    "v_mfma_f32_16x16x32_bf16 v[188:191], v[212:215], v[84:87], v[188:191]\n"
    "ds_read_b64_tr_b16 v[126:127], v5 offset:4864\n"
    "s_waitcnt lgkmcnt(8)\n"
    "v_mfma_f32_16x16x32_bf16 v[176:179], v[216:219], v[88:91], v[176:179]\n"
    "ds_read_b64_tr_b16 v[80:81], v1 offset:16896\n"
    "v_mfma_f32_16x16x32_bf16 v[148:151], v[244:247], v[96:99], v[148:151]\n"
    "v_mfma_f32_16x16x32_bf16 v[156:159], v[244:247], v[100:103], v[156:159]\n"
    "v_mfma_f32_16x16x32_bf16 v[180:183], v[220:223], v[88:91], v[180:183]\n"
    "ds_read_b64_tr_b16 v[82:83], v1 offset:17152\n"
    "v_mfma_f32_16x16x32_bf16 v[184:187], v[216:219], v[92:95], v[184:187]\n"
    "ds_read_b64_tr_b16 v[84:85], v4 offset:16896\n"
    "v_mfma_f32_16x16x32_bf16 v[188:191], v[220:223], v[92:95], v[188:191]\n"
    "ds_read_b64_tr_b16 v[86:87], v4 offset:17152\n"
    "s_waitcnt lgkmcnt(8)\n"
    "v_mfma_f32_16x16x32_bf16 v[240:243], v[64:67], v[112:115], 0\n"
    "ds_read_b64_tr_b16 v[88:89], v1 offset:20992\n"
    "v_mfma_f32_16x16x32_bf16 v[244:247], v[68:71], v[112:115], 0\n"
    "ds_read_b64_tr_b16 v[90:91], v1 offset:21248\n"
    "v_mfma_f32_16x16x32_bf16 v[248:251], v[64:67], v[116:119], 0\n"
    "ds_read_b64_tr_b16 v[92:93], v4 offset:20992\n"
    "v_cvt_pk_bf16_f32 v176, v176, v177\n"
    "v_mfma_f32_16x16x32_bf16 v[252:255], v[68:71], v[116:119], 0\n"
    "ds_read_b64_tr_b16 v[94:95], v4 offset:21248\n"
    "s_waitcnt lgkmcnt(8)\n"
    "v_mfma_f32_16x16x32_bf16 v[240:243], v[72:75], v[120:123], v[240:243]\n"
    "ds_read_b64_tr_b16 v[112:113], v3 offset:512\n"
    "v_cvt_pk_bf16_f32 v177, v178, v179\n"
    "v_mfma_f32_16x16x32_bf16 v[244:247], v[76:79], v[120:123], v[244:247]\n"
    "ds_read_b64_tr_b16 v[114:115], v3 offset:768\n"
    "v_mfma_f32_16x16x32_bf16 v[248:251], v[72:75], v[124:127], v[248:251]\n"
    "ds_read_b64_tr_b16 v[116:117], v6 offset:512\n"
    "v_cvt_pk_bf16_f32 v178, v180, v181\n"
    "v_mfma_f32_16x16x32_bf16 v[252:255], v[76:79], v[124:127], v[252:255]\n"
    "ds_read_b64_tr_b16 v[118:119], v6 offset:768\n"
    "s_waitcnt lgkmcnt(8)\n"
    "v_mfma_f32_16x16x32_bf16 v[240:243], v[16:19], v[80:83], v[240:243]\n"
    "ds_read_b64_tr_b16 v[120:121], v3 offset:4608\n"
    "v_cvt_pk_bf16_f32 v179, v182, v183\n"
    "v_mfma_f32_16x16x32_bf16 v[244:247], v[20:23], v[80:83], v[244:247]\n"
    "ds_read_b64_tr_b16 v[122:123], v3 offset:4864\n"
    "v_mfma_f32_16x16x32_bf16 v[248:251], v[16:19], v[84:87], v[248:251]\n"
    "ds_read_b64_tr_b16 v[124:125], v6 offset:4608\n"
    "v_mfma_f32_16x16x32_bf16 v[144:147], v[176:179], v[104:107], v[144:147]\n"
    "v_cvt_pk_bf16_f32 v180, v184, v185\n"
    "v_cvt_pk_bf16_f32 v181, v186, v187\n"
    "v_mfma_f32_16x16x32_bf16 v[152:155], v[176:179], v[108:111], v[152:155]\n"
    "v_cvt_pk_bf16_f32 v182, v188, v189\n"
    "v_cvt_pk_bf16_f32 v183, v190, v191\n"
    "v_mfma_f32_16x16x32_bf16 v[252:255], v[20:23], v[84:87], v[252:255]\n"
    "ds_read_b64_tr_b16 v[126:127], v6 offset:4864\n"
    "s_waitcnt lgkmcnt(8)\n"
    "v_mfma_f32_16x16x32_bf16 v[240:243], v[24:27], v[88:91], v[240:243]\n"
    "ds_read_b64_tr_b16 v[80:81], v3 offset:8704\n"
    "v_mfma_f32_16x16x32_bf16 v[244:247], v[28:31], v[88:91], v[244:247]\n"
    "ds_read_b64_tr_b16 v[82:83], v3 offset:8960\n"
    "v_mfma_f32_16x16x32_bf16 v[248:251], v[24:27], v[92:95], v[248:251]\n"
    "ds_read_b64_tr_b16 v[84:85], v6 offset:8704\n"
    "v_mfma_f32_16x16x32_bf16 v[252:255], v[28:31], v[92:95], v[252:255]\n"
    "ds_read_b64_tr_b16 v[86:87], v6 offset:8960\n"
    "s_waitcnt lgkmcnt(8)\n"
    "v_mfma_f32_16x16x32_bf16 v[240:243], v[32:35], v[112:115], v[240:243]\n"
    "ds_read_b64_tr_b16 v[88:89], v3 offset:12800\n"
    "v_mfma_f32_16x16x32_bf16 v[244:247], v[36:39], v[112:115], v[244:247]\n"
    "ds_read_b64_tr_b16 v[90:91], v3 offset:13056\n"
    "v_mfma_f32_16x16x32_bf16 v[248:251], v[32:35], v[116:119], v[248:251]\n"
    "ds_read_b64_tr_b16 v[92:93], v6 offset:12800\n"
    "v_mfma_f32_16x16x32_bf16 v[148:151], v[180:183], v[104:107], v[148:151]\n"
    "v_cvt_pk_bf16_f32 v144, v144, v145\n"
    "v_cvt_pk_bf16_f32 v145, v146, v147\n"
    "v_mfma_f32_16x16x32_bf16 v[252:255], v[36:39], v[116:119], v[252:255]\n"
    "ds_read_b64_tr_b16 v[94:95], v6 offset:13056\n"
    "s_waitcnt lgkmcnt(8)\n"
    "v_mfma_f32_16x16x32_bf16 v[240:243], v[40:43], v[120:123], v[240:243]\n"
    "ds_read_b64_tr_b16 v[112:113], v1 offset:25088\n"
    "v_mfma_f32_16x16x32_bf16 v[156:159], v[180:183], v[108:111], v[156:159]\n"
    "v_mfma_f32_16x16x32_bf16 v[244:247], v[44:47], v[120:123], v[244:247]\n"
    "ds_read_b64_tr_b16 v[114:115], v1 offset:25344\n"
    "v_cvt_pk_bf16_f32 v146, v148, v149\n"
    "v_mfma_f32_16x16x32_bf16 v[248:251], v[40:43], v[124:127], v[248:251]\n"
    "ds_read_b64_tr_b16 v[116:117], v4 offset:25088\n"
    "v_cvt_pk_bf16_f32 v147, v150, v151\n"
    "v_mfma_f32_16x16x32_bf16 v[252:255], v[44:47], v[124:127], v[252:255]\n"
    "ds_read_b64_tr_b16 v[118:119], v4 offset:25344\n"
    "s_waitcnt lgkmcnt(8)\n"
    "v_mfma_f32_16x16x32_bf16 v[176:179], v[48:51], v[80:83], 0\n"
    "ds_read_b64_tr_b16 v[120:121], v1 offset:29184\n"
    "v_cvt_pk_bf16_f32 v148, v152, v153\n"
    "v_mfma_f32_16x16x32_bf16 v[180:183], v[52:55], v[80:83], 0\n"
    "ds_read_b64_tr_b16 v[122:123], v1 offset:29440\n"
    "v_cvt_pk_bf16_f32 v149, v154, v155\n"
    "v_mfma_f32_16x16x32_bf16 v[184:187], v[48:51], v[84:87], 0\n"
    "ds_read_b64_tr_b16 v[124:125], v4 offset:29184\n"
    "v_cvt_pk_bf16_f32 v150, v156, v157\n"
    "v_mfma_f32_16x16x32_bf16 v[188:191], v[52:55], v[84:87], 0\n"
    "ds_read_b64_tr_b16 v[126:127], v4 offset:29440\n"
    "s_waitcnt lgkmcnt(8)\n"
    "v_mfma_f32_16x16x32_bf16 v[176:179], v[56:59], v[88:91], v[176:179]\n"
    "ds_read_b64_tr_b16 v[80:81], v2 offset:8704\n"
    "v_cvt_pk_bf16_f32 v151, v158, v159\n"
    "v_mfma_f32_16x16x32_bf16 v[180:183], v[60:63], v[88:91], v[180:183]\n"
    "ds_read_b64_tr_b16 v[82:83], v2 offset:8960\n"
    "v_cvt_pk_bf16_f32 v240, v240, v241\n"
    "v_mfma_f32_16x16x32_bf16 v[184:187], v[56:59], v[92:95], v[184:187]\n"
    "ds_read_b64_tr_b16 v[84:85], v5 offset:8704\n"
    "s_add_u32 s73, s23, 0\n"
    "v_mfma_f32_16x16x32_bf16 v[188:191], v[60:63], v[92:95], v[188:191]\n"
    "ds_read_b64_tr_b16 v[86:87], v5 offset:8960\n"
    "s_waitcnt lgkmcnt(8)\n"
    "v_mfma_f32_16x16x32_bf16 v[176:179], v[224:227], v[112:115], v[176:179]\n"
    "ds_read_b64_tr_b16 v[88:89], v2 offset:12800\n"
    "s_waitcnt vmcnt(0)\n"
    "s_mov_b32 exec_hi, 0\n"
    "buffer_store_dwordx2 v[14:15], v12, s[48:51], s73 offen sc1\n"
    "s_mov_b32 exec_hi, -1\n"
    "v_mfma_f32_16x16x32_bf16 v[180:183], v[228:231], v[112:115], v[180:183]\n"
    "ds_read_b64_tr_b16 v[90:91], v2 offset:13056\n"
    "v_mfma_f32_16x16x32_bf16 v[184:187], v[224:227], v[116:119], v[184:187]\n"
    "ds_read_b64_tr_b16 v[92:93], v5 offset:12800\n"
    "v_mfma_f32_16x16x32_bf16 v[188:191], v[228:231], v[116:119], v[188:191]\n"
    "ds_read_b64_tr_b16 v[94:95], v5 offset:13056\n"
    "buffer_load_dword off, s[88:91], 0 offset:220 lds\n"
    "s_add_u32 s72, s22, 524288\n"
    "buffer_store_dwordx4 v[144:147], v11, s[48:51], s72 offen sc1\n"
    "s_add_u32 s72, s22, 532480\n"
    "buffer_store_dwordx4 v[148:151], v11, s[48:51], s72 offen sc1\n"
    "s_waitcnt lgkmcnt(8)\n"
    "v_mfma_f32_16x16x32_bf16 v[176:179], v[232:235], v[120:123], v[176:179]\n"
    "v_cvt_pk_bf16_f32 v241, v242, v243\n"
    "v_mfma_f32_16x16x32_bf16 v[180:183], v[236:239], v[120:123], v[180:183]\n"
    "v_cvt_pk_bf16_f32 v242, v244, v245\n"
    "v_cvt_pk_bf16_f32 v243, v246, v247\n"
    "v_mfma_f32_16x16x32_bf16 v[184:187], v[232:235], v[124:127], v[184:187]\n"
    "v_mfma_f32_16x16x32_bf16 v[188:191], v[236:239], v[124:127], v[188:191]\n"
    "v_mfma_f32_16x16x32_bf16 v[160:163], v[240:243], v[96:99], v[160:163]\n"
    "v_cvt_pk_bf16_f32 v244, v248, v249\n"
    "v_cvt_pk_bf16_f32 v245, v250, v251\n"
    "v_mfma_f32_16x16x32_bf16 v[168:171], v[240:243], v[100:103], v[168:171]\n"
    "v_cvt_pk_bf16_f32 v246, v252, v253\n"
    "v_cvt_pk_bf16_f32 v247, v254, v255\n"
    "s_waitcnt lgkmcnt(4)\n"
    "v_mfma_f32_16x16x32_bf16 v[176:179], v[208:211], v[80:83], v[176:179]\n"
    "v_mfma_f32_16x16x32_bf16 v[180:183], v[212:215], v[80:83], v[180:183]\n"
    "v_mfma_f32_16x16x32_bf16 v[184:187], v[208:211], v[84:87], v[184:187]\n"
    "v_mfma_f32_16x16x32_bf16 v[188:191], v[212:215], v[84:87], v[188:191]\n"
    "s_waitcnt lgkmcnt(0)\n"
    "v_mfma_f32_16x16x32_bf16 v[176:179], v[216:219], v[88:91], v[176:179]\n"
    "v_mfma_f32_16x16x32_bf16 v[164:167], v[244:247], v[96:99], v[164:167]\n"
    "v_mfma_f32_16x16x32_bf16 v[172:175], v[244:247], v[100:103], v[172:175]\n"
    "v_mfma_f32_16x16x32_bf16 v[180:183], v[220:223], v[88:91], v[180:183]\n"
    "v_mfma_f32_16x16x32_bf16 v[184:187], v[216:219], v[92:95], v[184:187]\n"
    "v_mfma_f32_16x16x32_bf16 v[188:191], v[220:223], v[92:95], v[188:191]\n"
    "s_nop 2\n"
    "v_cvt_pk_bf16_f32 v176, v176, v177\n"
    "v_cvt_pk_bf16_f32 v177, v178, v179\n"
    "s_nop 0\n"
    "v_cvt_pk_bf16_f32 v178, v180, v181\n"
    "v_cvt_pk_bf16_f32 v179, v182, v183\n"
    "s_nop 1\n"
    "v_mfma_f32_16x16x32_bf16 v[160:163], v[176:179], v[104:107], v[160:163]\n"
    "v_cvt_pk_bf16_f32 v180, v184, v185\n"
    "v_cvt_pk_bf16_f32 v181, v186, v187\n"
    "v_mfma_f32_16x16x32_bf16 v[168:171], v[176:179], v[108:111], v[168:171]\n"
    "v_cvt_pk_bf16_f32 v182, v188, v189\n"
    "v_cvt_pk_bf16_f32 v183, v190, v191\n"
    "s_nop 1\n"
    "v_mfma_f32_16x16x32_bf16 v[164:167], v[180:183], v[104:107], v[164:167]\n"
    "v_mfma_f32_16x16x32_bf16 v[172:175], v[180:183], v[108:111], v[172:175]\n"
    "v_cvt_pk_bf16_f32 v160, v160, v161\n"
    "v_cvt_pk_bf16_f32 v161, v162, v163\n"
    "s_nop 4\n"
    "v_cvt_pk_bf16_f32 v162, v164, v165\n"
    "v_cvt_pk_bf16_f32 v163, v166, v167\n"
    "v_cvt_pk_bf16_f32 v164, v168, v169\n"
    "v_cvt_pk_bf16_f32 v165, v170, v171\n"
    "v_cvt_pk_bf16_f32 v166, v172, v173\n"
    "v_cvt_pk_bf16_f32 v167, v174, v175\n"
    "s_add_u32 s72, s22, 786432\n"
    "buffer_store_dwordx4 v[160:163], v11, s[48:51], s72 offen sc1\n"
    "s_add_u32 s72, s22, 794624\n"
    "buffer_store_dwordx4 v[164:167], v11, s[48:51], s72 offen sc1\n"
    "buffer_load_dword off, s[88:91], 0 offset:224 lds\n"
    "s_add_u32 s73, s23, 32768\n"
    "s_waitcnt vmcnt(0)\n"
    "s_mov_b32 exec_hi, 0\n"
    "buffer_store_dwordx2 v[14:15], v12, s[48:51], s73 offen sc1\n"
    "s_mov_b32 exec_hi, -1\n"
    "s_branch LR2%=\n"
    "CS1_%=:\n"
    "CT1_%=:\n"
    "ds_read_b32 v9, v7\n"
    "s_waitcnt lgkmcnt(0)\n"
    "s_sub_u32 s95, s95, 1\n"
    "s_cbranch_scc1 LE%=\n"
    "v_cmp_gt_u32 vcc, 0xb0000001, v9\n"
    "s_cbranch_vccnz CT1_%=\n"
    "s_branch CB1_%=\n"
    "CS2_%=:\n"
    "s_waitcnt lgkmcnt(0)\n"
    "buffer_load_dword off, s[88:91], 0 offset:8 lds\n"
    "CT2_%=:\n"
    "ds_read_b32 v9, v7\n"
    "s_waitcnt lgkmcnt(0)\n"
    "s_sub_u32 s95, s95, 1\n"
    "s_cbranch_scc1 LE%=\n"
    "v_cmp_gt_u32 vcc, 0xb0000002, v9\n"
    "s_cbranch_vccnz CT2_%=\n"
    "s_branch CB2_%=\n"
    "CS3_%=:\n"
    "s_waitcnt lgkmcnt(0)\n"
    "buffer_load_dword off, s[88:91], 0 offset:16 lds\n"
    "CT3_%=:\n"
    "ds_read_b32 v9, v7\n"
    "s_waitcnt lgkmcnt(0)\n"
    "s_sub_u32 s95, s95, 1\n"
    "s_cbranch_scc1 LE%=\n"
    "v_cmp_gt_u32 vcc, 0xb0000003, v9\n"
    "s_cbranch_vccnz CT3_%=\n"
    "s_branch CB3_%=\n"
    "CS4_%=:\n"
    "s_waitcnt lgkmcnt(0)\n"
    "buffer_load_dword off, s[88:91], 0 offset:24 lds\n"
    "CT4_%=:\n"
    "ds_read_b32 v9, v7\n"
    "s_waitcnt lgkmcnt(0)\n"
    "s_sub_u32 s95, s95, 1\n"
    "s_cbranch_scc1 LE%=\n"
    "v_cmp_gt_u32 vcc, 0xb0000004, v9\n"
    "s_cbranch_vccnz CT4_%=\n"
    "s_branch CB4_%=\n"
    "CS5_%=:\n"
    "s_waitcnt lgkmcnt(0)\n"
    "buffer_load_dword off, s[88:91], 0 offset:32 lds\n"
    "CT5_%=:\n"
    "ds_read_b32 v9, v7\n"
    "s_waitcnt lgkmcnt(0)\n"
    "s_sub_u32 s95, s95, 1\n"
    "s_cbranch_scc1 LE%=\n"
    "v_cmp_gt_u32 vcc, 0xb0000005, v9\n"
    "s_cbranch_vccnz CT5_%=\n"
    "s_branch CB5_%=\n"
    "CS6_%=:\n"
    "s_waitcnt lgkmcnt(0)\n"
    "buffer_load_dword off, s[88:91], 0 offset:40 lds\n"
    "CT6_%=:\n"
    "ds_read_b32 v9, v7\n"
    "s_waitcnt lgkmcnt(0)\n"
    "s_sub_u32 s95, s95, 1\n"
    "s_cbranch_scc1 LE%=\n"
    "v_cmp_gt_u32 vcc, 0xb0000006, v9\n"
    "s_cbranch_vccnz CT6_%=\n"
    "s_branch CB6_%=\n"
    "CS7_%=:\n"
    "s_waitcnt lgkmcnt(0)\n"
    "buffer_load_dword off, s[88:91], 0 offset:48 lds\n"
    "CT7_%=:\n"
    "ds_read_b32 v9, v7\n"
    "s_waitcnt lgkmcnt(0)\n"
    "s_sub_u32 s95, s95, 1\n"
    "s_cbranch_scc1 LE%=\n"
    "v_cmp_gt_u32 vcc, 0xb0000007, v9\n"
    "s_cbranch_vccnz CT7_%=\n"
    "s_branch CB7_%=\n"
    "CS8_%=:\n"
    "s_waitcnt lgkmcnt(0)\n"
    "buffer_load_dword off, s[88:91], 0 offset:56 lds\n"
    "CT8_%=:\n"
    "ds_read_b32 v9, v7\n"
    "s_waitcnt lgkmcnt(0)\n"
    "s_sub_u32 s95, s95, 1\n"
    "s_cbranch_scc1 LE%=\n"
    "v_cmp_gt_u32 vcc, 0xb0000008, v9\n"
    "s_cbranch_vccnz CT8_%=\n"
    "s_branch CB8_%=\n"
    "CS9_%=:\n"
    "s_waitcnt lgkmcnt(0)\n"
    "buffer_load_dword off, s[88:91], 0 offset:64 lds\n"
    "CT9_%=:\n"
    "ds_read_b32 v9, v7\n"
    "s_waitcnt lgkmcnt(0)\n"
    "s_sub_u32 s95, s95, 1\n"
    "s_cbranch_scc1 LE%=\n"
    "v_cmp_gt_u32 vcc, 0xb0000009, v9\n"
    "s_cbranch_vccnz CT9_%=\n"
    "s_branch CB9_%=\n"
    "CS10_%=:\n"
    "s_waitcnt lgkmcnt(0)\n"
    "buffer_load_dword off, s[88:91], 0 offset:72 lds\n"
    "CT10_%=:\n"
    "ds_read_b32 v9, v7\n"
    "s_waitcnt lgkmcnt(0)\n"
    "s_sub_u32 s95, s95, 1\n"
    "s_cbranch_scc1 LE%=\n"
    "v_cmp_gt_u32 vcc, 0xb000000a, v9\n"
    "s_cbranch_vccnz CT10_%=\n"
    "s_branch CB10_%=\n"
    "CS11_%=:\n"
    "s_waitcnt vmcnt(0)\n"
    "v_lshlrev_b32 v13, 3, v0\n"
    "v_mov_b32 v14, s6\n"
    "v_mov_b32 v15, s7\n"
    "s_mov_b64 exec, 0xffff\n"
    "buffer_store_dwordx2 v[14:15], v13, s[48:51], s15 offen sc1\n"
    "s_mov_b64 exec, -1\n"
    "CT11_%=:\n"
    "ds_read_b32 v9, v7\n"
    "s_waitcnt lgkmcnt(0)\n"
    "s_sub_u32 s95, s95, 1\n"
    "s_cbranch_scc1 LE%=\n"
    "v_cmp_gt_u32 vcc, 0xb000000b, v9\n"
    "s_cbranch_vccnz CT11_%=\n"
    "s_branch CB11_%=\n"
    "CS12_%=:\n"
    "s_waitcnt lgkmcnt(0)\n"
    "buffer_load_dword off, s[88:91], 0 offset:84 lds\n"
    "CT12_%=:\n"
    "ds_read_b32 v9, v7\n"
    "s_waitcnt lgkmcnt(0)\n"
    "s_sub_u32 s95, s95, 1\n"
    "s_cbranch_scc1 LE%=\n"
    "v_cmp_gt_u32 vcc, 0xb000000c, v9\n"
    "s_cbranch_vccnz CT12_%=\n"
    "s_branch CB12_%=\n"
    "CS13_%=:\n"
    "s_waitcnt lgkmcnt(0)\n"
    "buffer_load_dword off, s[88:91], 0 offset:88 lds\n"
    "CT13_%=:\n"
    "ds_read_b32 v9, v7\n"
    "s_waitcnt lgkmcnt(0)\n"
    "s_sub_u32 s95, s95, 1\n"
    "s_cbranch_scc1 LE%=\n"
    "v_cmp_gt_u32 vcc, 0xb000000d, v9\n"
    "s_cbranch_vccnz CT13_%=\n"
    "s_branch CB13_%=\n"
    "CS14_%=:\n"
    "s_waitcnt lgkmcnt(0)\n"
    "buffer_load_dword off, s[88:91], 0 offset:104 lds\n"
    "CT14_%=:\n"
    "ds_read_b32 v9, v7\n"
    "s_waitcnt lgkmcnt(0)\n"
    "s_sub_u32 s95, s95, 1\n"
    "s_cbranch_scc1 LE%=\n"
    "v_cmp_gt_u32 vcc, 0xb000000e, v9\n"
    "s_cbranch_vccnz CT14_%=\n"
    "s_branch CB14_%=\n"
    "CS15_%=:\n"
    "CT15_%=:\n"
    "ds_read_b32 v9, v7\n"
    "s_waitcnt lgkmcnt(0)\n"
    "s_sub_u32 s95, s95, 1\n"
    "s_cbranch_scc1 LE%=\n"
    "v_cmp_gt_u32 vcc, 0xb000000f, v9\n"
    "s_cbranch_vccnz CT15_%=\n"
    "s_branch CB15_%=\n"
    "CS16_%=:\n"
    "s_waitcnt lgkmcnt(0)\n"
    "buffer_load_dword off, s[88:91], 0 offset:120 lds\n"
    "CT16_%=:\n"
    "ds_read_b32 v9, v7\n"
    "s_waitcnt lgkmcnt(0)\n"
    "s_sub_u32 s95, s95, 1\n"
    "s_cbranch_scc1 LE%=\n"
    "v_cmp_gt_u32 vcc, 0xb0000010, v9\n"
    "s_cbranch_vccnz CT16_%=\n"
    "s_branch CB16_%=\n"
    "CS17_%=:\n"
    "s_waitcnt lgkmcnt(0)\n"
    "buffer_load_dword off, s[88:91], 0 offset:120 lds\n"
    "CT17_%=:\n"
    "ds_read_b32 v9, v7\n"
    "s_waitcnt lgkmcnt(0)\n"
    "s_sub_u32 s95, s95, 1\n"
    "s_cbranch_scc1 LE%=\n"
    "v_cmp_gt_u32 vcc, 0xb0000011, v9\n"
    "s_cbranch_vccnz CT17_%=\n"
    "s_branch CB17_%=\n"
    "CS18_%=:\n"
    "CT18_%=:\n"
    "ds_read_b32 v9, v7\n"
    "s_waitcnt lgkmcnt(0)\n"
    "s_sub_u32 s95, s95, 1\n"
    "s_cbranch_scc1 LE%=\n"
    "v_cmp_gt_u32 vcc, 0xb0000012, v9\n"
    "s_cbranch_vccnz CT18_%=\n"
    "s_branch CB18_%=\n"
    "CS19_%=:\n"
    "s_waitcnt lgkmcnt(0)\n"
    "buffer_load_dword off, s[88:91], 0 offset:140 lds\n"
    "CT19_%=:\n"
    "ds_read_b32 v9, v7\n"
    "s_waitcnt lgkmcnt(0)\n"
    "s_sub_u32 s95, s95, 1\n"
    "s_cbranch_scc1 LE%=\n"
    "v_cmp_gt_u32 vcc, 0xb0000013, v9\n"
    "s_cbranch_vccnz CT19_%=\n"
    "s_branch CB19_%=\n"
    "CS20_%=:\n"
    "s_waitcnt lgkmcnt(0)\n"
    "buffer_load_dword off, s[88:91], 0 offset:152 lds\n"
    "CT20_%=:\n"
    "ds_read_b32 v9, v7\n"
    "s_waitcnt lgkmcnt(0)\n"
    "s_sub_u32 s95, s95, 1\n"
    "s_cbranch_scc1 LE%=\n"
    "v_cmp_gt_u32 vcc, 0xb0000014, v9\n"
    "s_cbranch_vccnz CT20_%=\n"
    "s_branch CB20_%=\n"
    "PS_%=:\n"
    "s_sleep 2\n"
    "buffer_load_dwordx2 v[14:15], v13, s[48:51], s16 offen sc1\n"
    "s_waitcnt vmcnt(0)\n"
    "s_sub_u32 s95, s95, 1\n"
    "s_cbranch_scc1 LE%=\n"
    "v_cmp_ne_u64 vcc, s[6:7], v[14:15]\n"
    "s_cbranch_vccnz PS_%=\n"
    "s_branch PB_%=\n"
    "CS21_%=:\n"
    "CT21_%=:\n"
    "ds_read_b32 v9, v7\n"
    "s_waitcnt lgkmcnt(0)\n"
    "s_sub_u32 s95, s95, 1\n"
    "s_cbranch_scc1 LE%=\n"
    "v_cmp_gt_u32 vcc, 0xb0000015, v9\n"
    "s_cbranch_vccnz CT21_%=\n"
    "s_branch CB21_%=\n"
    "CS22_%=:\n"
    "CT22_%=:\n"
    "ds_read_b32 v9, v7\n"
    "s_waitcnt lgkmcnt(0)\n"
    "s_sub_u32 s95, s95, 1\n"
    "s_cbranch_scc1 LE%=\n"
    "v_cmp_gt_u32 vcc, 0xb0000016, v9\n"
    "s_cbranch_vccnz CT22_%=\n"
    "s_branch CB22_%=\n"
    "CS23_%=:\n"
    "CT23_%=:\n"
    "ds_read_b32 v9, v7\n"
    "s_waitcnt lgkmcnt(0)\n"
    "s_sub_u32 s95, s95, 1\n"
    "s_cbranch_scc1 LE%=\n"
    "v_cmp_gt_u32 vcc, 0xb0000017, v9\n"
    "s_cbranch_vccnz CT23_%=\n"
    "s_branch CB23_%=\n"
    "CS24_%=:\n"
    "CT24_%=:\n"
    "ds_read_b32 v9, v7\n"
    "s_waitcnt lgkmcnt(0)\n"
    "s_sub_u32 s95, s95, 1\n"
    "s_cbranch_scc1 LE%=\n"
    "v_cmp_gt_u32 vcc, 0xb0000018, v9\n"
    "s_cbranch_vccnz CT24_%=\n"
    "s_branch CB24_%=\n"
    "CS25_%=:\n"
    "s_waitcnt lgkmcnt(0)\n"
    "buffer_load_dword off, s[88:91], 0 offset:184 lds\n"
    "CT25_%=:\n"
    "ds_read_b32 v9, v7\n"
    "s_waitcnt lgkmcnt(0)\n"
    "s_sub_u32 s95, s95, 1\n"
    "s_cbranch_scc1 LE%=\n"
    "v_cmp_gt_u32 vcc, 0xb0000019, v9\n"
    "s_cbranch_vccnz CT25_%=\n"
    "s_branch CB25_%=\n"
    "CS26_%=:\n"
    "CT26_%=:\n"
    "ds_read_b32 v9, v7\n"
    "s_waitcnt lgkmcnt(0)\n"
    "s_sub_u32 s95, s95, 1\n"
    "s_cbranch_scc1 LE%=\n"
    "v_cmp_gt_u32 vcc, 0xb000001a, v9\n"
    "s_cbranch_vccnz CT26_%=\n"
    "s_branch CB26_%=\n"
    "CS27_%=:\n"
    "CT27_%=:\n"
    "ds_read_b32 v9, v7\n"
    "s_waitcnt lgkmcnt(0)\n"
    "s_sub_u32 s95, s95, 1\n"
    "s_cbranch_scc1 LE%=\n"
    "v_cmp_gt_u32 vcc, 0xb000001b, v9\n"
    "s_cbranch_vccnz CT27_%=\n"
    "s_branch CB27_%=\n"
    "CS28_%=:\n"
    "CT28_%=:\n"
    "ds_read_b32 v9, v7\n"
    "s_waitcnt lgkmcnt(0)\n"
    "s_sub_u32 s95, s95, 1\n"
    "s_cbranch_scc1 LE%=\n"
    "v_cmp_gt_u32 vcc, 0xb000001c, v9\n"
    "s_cbranch_vccnz CT28_%=\n"
    "s_branch CB28_%=\n"
    "CS29_%=:\n"
    "CT29_%=:\n"
    "ds_read_b32 v9, v7\n"
    "s_waitcnt lgkmcnt(0)\n"
    "s_sub_u32 s95, s95, 1\n"
    "s_cbranch_scc1 LE%=\n"
    "v_cmp_gt_u32 vcc, 0xb000001d, v9\n"
    "s_cbranch_vccnz CT29_%=\n"
    "s_branch CB29_%=\n"
    "CS30_%=:\n"
    "CT30_%=:\n"
    "ds_read_b32 v9, v7\n"
    "s_waitcnt lgkmcnt(0)\n"
    "s_sub_u32 s95, s95, 1\n"
    "s_cbranch_scc1 LE%=\n"
    "v_cmp_gt_u32 vcc, 0xb000001e, v9\n"
    "s_cbranch_vccnz CT30_%=\n"
    "s_branch CB30_%=\n"
    "CS31_%=:\n"
    "CT31_%=:\n"
    "ds_read_b32 v9, v7\n"
    "s_waitcnt lgkmcnt(0)\n"
    "s_sub_u32 s95, s95, 1\n"
    "s_cbranch_scc1 LE%=\n"
    "v_cmp_gt_u32 vcc, 0xb000001f, v9\n"
    "s_cbranch_vccnz CT31_%=\n"
    "s_branch CB31_%=\n"
    ".p2align 7\n"
    "TAB_%=:\n"
    "s_movk_i32 s0, 0\n"
    "s_movk_i32 s0, 1\n"
    "s_movk_i32 s0, 2\n"
    "s_movk_i32 s0, 3\n"
    "s_movk_i32 s0, 4\n"
    "s_movk_i32 s0, 5\n"
    "s_movk_i32 s0, 6\n"
    "s_movk_i32 s0, 7\n"
    "s_movk_i32 s0, 8\n"
    "s_movk_i32 s0, 9\n"
    "s_movk_i32 s0, 10\n"
    "s_movk_i32 s0, 11\n"
    "s_movk_i32 s0, 12\n"
    "s_movk_i32 s0, 13\n"
    "s_movk_i32 s0, 14\n"
    "s_movk_i32 s0, 15\n"
    "s_movk_i32 s0, 16\n"
    "s_movk_i32 s0, 17\n"
    "s_movk_i32 s0, 18\n"
    "s_movk_i32 s0, 19\n"
    "s_movk_i32 s0, 20\n"
    "s_movk_i32 s0, 21\n"
    "s_movk_i32 s0, 22\n"
    "s_movk_i32 s0, 23\n"
    "s_movk_i32 s0, 24\n"
    "s_movk_i32 s0, 25\n"
    "s_movk_i32 s0, 26\n"
    "s_movk_i32 s0, 27\n"
    "s_movk_i32 s0, 28\n"
    "s_movk_i32 s0, 29\n"
    "s_movk_i32 s0, 30\n"
    "s_movk_i32 s0, 31\n"
    "s_movk_i32 s0, 32\n"
    "s_movk_i32 s0, 33\n"
    "s_movk_i32 s0, 34\n"
    "s_movk_i32 s0, 35\n"
    "s_movk_i32 s0, 36\n"
    "s_movk_i32 s0, 37\n"
    "s_movk_i32 s0, 38\n"
    "s_movk_i32 s0, 39\n"
    "s_movk_i32 s0, 40\n"
    "s_movk_i32 s0, 41\n"
    "s_movk_i32 s0, 42\n"
    "s_movk_i32 s0, 43\n"
    "s_movk_i32 s0, 44\n"
    "s_movk_i32 s0, 45\n"
    "s_movk_i32 s0, 46\n"
    "s_movk_i32 s0, 47\n"
    "s_movk_i32 s0, 48\n"
    "s_movk_i32 s0, 49\n"
    "s_movk_i32 s0, 50\n"
    "s_movk_i32 s0, 51\n"
    "s_movk_i32 s0, 52\n"
    "s_movk_i32 s0, 53\n"
    "s_movk_i32 s0, 54\n"
    "s_movk_i32 s0, 55\n"
    "s_movk_i32 s0, 56\n"
    "s_movk_i32 s0, 57\n"
    "s_movk_i32 s0, 58\n"
    "s_movk_i32 s0, 59\n"
    "s_movk_i32 s0, 60\n"
    "s_movk_i32 s0, 61\n"
    "s_movk_i32 s0, 62\n"
    "s_movk_i32 s0, 63\n"
    "LE%=:\n"

    :: "s"(__builtin_amdgcn_kernarg_segment_ptr()), "s"(blockIdx.x), "s"(wave), "s"((unsigned)(size_t)(__attribute__((address_space(3))) char*)smem)
    : "v0","v1","v2","v3","v4","v5","v6","v7","v8","v9","v10","v11","v12","v13","v14","v15","v16","v17","v18","v19","v20","v21","v22","v23","v24","v25","v26","v27","v28","v29","v30","v31","v32","v33","v34","v35","v36","v37","v38","v39","v40","v41","v42","v43","v44","v45","v46","v47","v48","v49","v50","v51","v52","v53","v54","v55","v56","v57","v58","v59","v60","v61","v62","v63","v64","v65","v66","v67","v68","v69","v70","v71","v72","v73","v74","v75","v76","v77","v78","v79","v80","v81","v82","v83","v84","v85","v86","v87","v88","v89","v90","v91","v92","v93","v94","v95","v96","v97","v98","v99","v100","v101","v102","v103","v104","v105","v106","v107","v108","v109","v110","v111","v112","v113","v114","v115","v116","v117","v118","v119","v120","v121","v122","v123","v124","v125","v126","v127","v128","v129","v130","v131","v132","v133","v134","v135","v136","v137","v138","v139","v140","v141","v142","v143","v144","v145","v146","v147","v148","v149","v150","v151","v152","v153","v154","v155","v156","v157","v158","v159","v160","v161","v162","v163","v164","v165","v166","v167","v168","v169","v170","v171","v172","v173","v174","v175","v176","v177","v178","v179","v180","v181","v182","v183","v184","v185","v186","v187","v188","v189","v190","v191","v192","v193","v194","v195","v196","v197","v198","v199","v200","v201","v202","v203","v204","v205","v206","v207","v208","v209","v210","v211","v212","v213","v214","v215","v216","v217","v218","v219","v220","v221","v222","v223","v224","v225","v226","v227","v228","v229","v230","v231","v232","v233","v234","v235","v236","v237","v238","v239","v240","v241","v242","v243","v244","v245","v246","v247","v248","v249","v250","v251","v252","v253","v254","v255","s5","s6","s7","s8","s9","s10","s11","s12","s13","s14","s15","s16","s17","s18","s19","s20","s21","s22","s23","s24","s25","s26","s27","s28","s29","s30","s31","s34","s35","s36","s37","s38","s39","s40","s41","s42","s43","s44","s45","s46","s47","s48","s49","s50","s51","s52","s53","s54","s55","s56","s57","s58","s59","s60","s61","s62","s63","s64","s65","s66","s67","s68","s69","s70","s71","s72","s73","s74","s75","s76","s77","s78","s79","s80","s81","s82","s83","s84","s85","s86","s87","s88","s89","s90","s91","s92","s93","s94","s95","s96","s97","s98","s99","vcc","scc","memory");
}

extern "C" void kernel_launch(void* const* d_in, const int* in_sizes, int n_in,
                              void* d_out, int out_size, void* d_ws, size_t ws_size,
                              hipStream_t stream) {
  fused_attn_kernel<<<256, 512, 0, stream>>>(d_in[0], d_in[1], d_in[2], d_in[3], d_in[4], d_in[5], d_in[6], d_in[7], d_in[8], d_in[9], d_out, d_ws);
}
```
